```python
import torch

def _softmax(x, axis=-1):
    return torch.softmax(x, dim=axis)
def _gelu(x):
    return torch.nn.functional.gelu(x, approximate="tanh")
def _silu(x):
    return torch.nn.functional.silu(x)
def _sigmoid(x):
    return torch.sigmoid(x)
def _layernorm(x, axis=-1, eps=1e-5):
    m = x.mean(dim=axis, keepdim=True)
    v = x.var(dim=axis, keepdim=True, unbiased=False)
    return (x - m) / torch.sqrt(v + eps)
def _scatter_add(tgt, idx, src, axis=0):
    return tgt.clone().index_add_(axis, idx, src)
def _segment_sum(src, seg, n):
    out = torch.zeros((n,) + src.shape[1:], dtype=src.dtype)
    return out.index_add_(0, seg, src)
def _pad(x, pw):
    flat = tuple(v for pair in reversed(pw) for v in pair)
    return torch.nn.functional.pad(x, flat)

kernel_name = 'concat_heads__gqa_block_cached__gqa_block_cached__near_ridge'

def setup_inputs():

    _w = (lambda a: (lambda t: t.to(torch.bfloat16) if t.is_floating_point() else t)(torch.as_tensor(a)))
    def _A_setup():
        B, Sq, D = 16, 112, 576
        Hq, Hkv, Sk, Dh = 32, 16, 512, 128
        assert Hq % Hkv == 0, (Hq, Hkv)
        return {
            "x": _w((lambda *s: torch.randn(*s, dtype=torch.bfloat16) * 0.8)(B, Sq, D)),
            "k_cache": _w((lambda *s: torch.randn(*s, dtype=torch.bfloat16) * 0.8)(B, Hkv, Sk, Dh)),
            "v_cache": _w((lambda *s: torch.randn(*s, dtype=torch.bfloat16) * 0.05)(B, Hkv, Sk, Dh)),
            "wq": _w((lambda *s: torch.randn(*s, dtype=torch.bfloat16) * 0.8)(D, Hq * Dh) * (D ** -0.5)),
            "wo": _w((lambda *s: torch.randn(*s, dtype=torch.bfloat16) * 0.05)(Hq * Dh, D) * ((Hq * Dh) ** -0.5)),
        }
    _A_in = _A_setup()

    def _B_setup():
        B, Sq, D = 16, 112, 576
        Hq, Hkv, Sk, Dh = 32, 16, 512, 128
        assert Hq % Hkv == 0, (Hq, Hkv)
        return {
            "x": _w((lambda *s: torch.randn(*s, dtype=torch.bfloat16) * 0.8)(B, Sq, D)),
            "k_cache": _w((lambda *s: torch.randn(*s, dtype=torch.bfloat16) * 0.8)(B, Hkv, Sk, Dh)),
            "v_cache": _w((lambda *s: torch.randn(*s, dtype=torch.bfloat16) * 0.05)(B, Hkv, Sk, Dh)),
            "wq": _w((lambda *s: torch.randn(*s, dtype=torch.bfloat16) * 0.8)(D, Hq * Dh) * (D ** -0.5)),
            "wo": _w((lambda *s: torch.randn(*s, dtype=torch.bfloat16) * 0.05)(Hq * Dh, D) * ((Hq * Dh) ** -0.5)),
        }
    _B_in = _B_setup()

    return {
        "A_x": _A_in["x"],
        "A_k_cache": _A_in["k_cache"],
        "A_v_cache": _A_in["v_cache"],
        "A_wq": _A_in["wq"],
        "A_wo": _A_in["wo"],
        "B_x": _B_in["x"],
        "B_k_cache": _B_in["k_cache"],
        "B_v_cache": _B_in["v_cache"],
        "B_wq": _B_in["wq"],
        "B_wo": _B_in["wo"],
    }

def reference(A_x, A_k_cache, A_v_cache, A_wq, A_wo, B_x, B_k_cache, B_v_cache, B_wq, B_wo):
    def _A_ref(x, k_cache, v_cache, wq, wo):
        B, Sq, D = x.shape
        Hq, Dh = 32, 128
        Hkv = k_cache.shape[1]
        rep = Hq // Hkv
        q = (lambda x, *d: x.permute(*d))((x @ wq).reshape(B, Sq, Hq, Dh), 0, 2, 1, 3)
        k = (lambda x, n, axis: torch.repeat_interleave(x, n, dim=axis))(k_cache, rep, axis=1)
        v = (lambda x, n, axis: torch.repeat_interleave(x, n, dim=axis))(v_cache, rep, axis=1)
        scores = torch.einsum("bhqd,bhkd->bhqk", q, k) * (Dh ** -0.5)
        p = _softmax(scores, axis=-1)
        o = torch.einsum("bhqk,bhkd->bhqd", p, v)
        return (lambda x, *d: x.permute(*d))(o, 0, 2, 1, 3).reshape(B, Sq, Hq * Dh) @ wo

    def _B_ref(x, k_cache, v_cache, wq, wo):
        B, Sq, D = x.shape
        Hq, Dh = 32, 128
        Hkv = k_cache.shape[1]
        rep = Hq // Hkv
        q = (lambda x, *d: x.permute(*d))((x @ wq).reshape(B, Sq, Hq, Dh), 0, 2, 1, 3)
        k = (lambda x, n, axis: torch.repeat_interleave(x, n, dim=axis))(k_cache, rep, axis=1)
        v = (lambda x, n, axis: torch.repeat_interleave(x, n, dim=axis))(v_cache, rep, axis=1)
        scores = torch.einsum("bhqd,bhkd->bhqk", q, k) * (Dh ** -0.5)
        p = _softmax(scores, axis=-1)
        o = torch.einsum("bhqk,bhkd->bhqd", p, v)
        return (lambda x, *d: x.permute(*d))(o, 0, 2, 1, 3).reshape(B, Sq, Hq * Dh) @ wo

    a = _A_ref(A_x, A_k_cache, A_v_cache, A_wq, A_wo)
    b = _B_ref(B_x, B_k_cache, B_v_cache, B_wq, B_wo)
    return torch.concatenate([a, b], axis=-1)
```

```cpp
#include <hip/hip_runtime.h>
#include <hip/hip_bf16.h>
typedef unsigned short u16;
__device__ __forceinline__ float b2f(u16 v){ return __uint_as_float(((unsigned)v)<<16); }
__device__ __forceinline__ u16 f2b(float f){ __hip_bfloat16 h = __float2bfloat16(f); return *reinterpret_cast<u16*>(&h); }
constexpr int B=16,SQ=112,D=576,HQ=32,HKV=16,SK=512,DH=128;
__global__ void gemm_naive(const u16* A,const u16* W,u16* C,int M,int N,int K,int ldc){
  int n = blockIdx.x*256+threadIdx.x; int m0=blockIdx.y*8;
  if(n>=N) return;
  float acc[8]={0,0,0,0,0,0,0,0};
  for(int k=0;k<K;k++){ float w=b2f(W[(size_t)k*N+n]);
    #pragma unroll
    for(int i=0;i<8;i++) acc[i]+=b2f(A[(size_t)(m0+i)*K+k])*w; }
  #pragma unroll
  for(int i=0;i<8;i++) C[(size_t)(m0+i)*ldc+n]=f2b(acc[i]);
}
__global__ void attn_naive(const u16* Q,const u16* Kc,const u16* Vc,u16* O){
  int q=blockIdx.x,h=blockIdx.y,b=blockIdx.z,t=threadIdx.x;
  __shared__ float qs[DH]; __shared__ float p[SK]; __shared__ float red[16];
  if(t<DH) qs[t]=b2f(Q[((size_t)(b*SQ+q))*4096+h*DH+t]);
  __syncthreads();
  const u16* kr=Kc+(((size_t)b*HKV+h/2)*SK+t)*DH;
  float s=0; for(int d=0;d<DH;d++) s+=qs[d]*b2f(kr[d]);
  s*=0.08838834764831845f;
  float m=s; for(int o=32;o;o>>=1) m=fmaxf(m,__shfl_xor(m,o));
  if((t&63)==0) red[t>>6]=m; __syncthreads();
  m=red[0]; for(int i=1;i<8;i++) m=fmaxf(m,red[i]);
  float e=__expf(s-m); p[t]=e; float l=e; for(int o=32;o;o>>=1) l+=__shfl_xor(l,o);
  if((t&63)==0) red[8+(t>>6)]=l; __syncthreads();
  l=0; for(int i=0;i<8;i++) l+=red[8+i];
  if(t<DH){ const u16* vr=Vc+(((size_t)b*HKV+h/2)*SK)*DH+t; float a=0; for(int k=0;k<SK;k++) a+=p[k]*b2f(vr[(size_t)k*DH]);
    O[((size_t)(b*SQ+q))*4096+h*DH+t]=f2b(a/l); }
}
extern "C" void kernel_launch(void* const* d_in,const int* in_sizes,int n_in,void* d_out,int out_size,void* d_ws,size_t ws_size,hipStream_t stream){
  u16* q=(u16*)d_ws; u16* o=q+(size_t)B*SQ*4096;
  for(int br=0;br<2;br++){
    const u16* x=(const u16*)d_in[br*5+0]; const u16* k=(const u16*)d_in[br*5+1]; const u16* v=(const u16*)d_in[br*5+2];
    const u16* wq=(const u16*)d_in[br*5+3]; const u16* wo=(const u16*)d_in[br*5+4];
    gemm_naive<<<dim3(16,B*SQ/8),256,0,stream>>>(x,wq,q,B*SQ,4096,D,4096);
    attn_naive<<<dim3(SQ,HQ,B),512,0,stream>>>(q,k,v,o);
    gemm_naive<<<dim3(3,B*SQ/8),256,0,stream>>>(o,wo,(u16*)d_out+br*D,B*SQ,D,4096,2*D);
  }
}
```

```cpp
#include <hip/hip_runtime.h>
#include <hip/hip_bf16.h>
typedef unsigned short u16;
__device__ __forceinline__ float b2f(u16 v){ return __uint_as_float(((unsigned)v)<<16); }
__device__ __forceinline__ u16 f2b(float f){ __hip_bfloat16 h = __float2bfloat16(f); return *reinterpret_cast<u16*>(&h); }
constexpr int B=16,SQ=112,D=576,HQ=32,HKV=16,SK=512,DH=128;
__global__ void gemm_naive(const u16* A,const u16* W,u16* C,int M,int N,int K,int ldc){
  int n = blockIdx.x*256+threadIdx.x; int m0=blockIdx.y*8;
  if(n>=N) return;
  float acc[8]={0,0,0,0,0,0,0,0};
  for(int k=0;k<K;k++){ float w=b2f(W[(size_t)k*N+n]);
    #pragma unroll
    for(int i=0;i<8;i++) acc[i]+=b2f(A[(size_t)(m0+i)*K+k])*w; }
  #pragma unroll
  for(int i=0;i<8;i++) C[(size_t)(m0+i)*ldc+n]=f2b(acc[i]);
}
__global__ void attn_naive(const u16* Q,const u16* Kc,const u16* Vc,u16* O){
  int q=blockIdx.x,h=blockIdx.y,b=blockIdx.z,t=threadIdx.x;
  __shared__ float qs[DH]; __shared__ float p[SK]; __shared__ float red[16];
  if(t<DH) qs[t]=b2f(Q[((size_t)(b*SQ+q))*4096+h*DH+t]);
  __syncthreads();
  const u16* kr=Kc+(((size_t)b*HKV+h/2)*SK+t)*DH;
  float s=0; for(int d=0;d<DH;d++) s+=qs[d]*b2f(kr[d]);
  s*=0.08838834764831845f;
  float m=s; for(int o=32;o;o>>=1) m=fmaxf(m,__shfl_xor(m,o));
  if((t&63)==0) red[t>>6]=m; __syncthreads();
  m=red[0]; for(int i=1;i<8;i++) m=fmaxf(m,red[i]);
  float e=__expf(s-m); p[t]=e; float l=e; for(int o=32;o;o>>=1) l+=__shfl_xor(l,o);
  if((t&63)==0) red[8+(t>>6)]=l; __syncthreads();
  l=0; for(int i=0;i<8;i++) l+=red[8+i];
  if(t<DH){ const u16* vr=Vc+(((size_t)b*HKV+h/2)*SK)*DH+t; float a=0; for(int k=0;k<SK;k++) a+=p[k]*b2f(vr[(size_t)k*DH]);
    O[((size_t)(b*SQ+q))*4096+h*DH+t]=f2b(a/l); }
}

__global__ __launch_bounds__(256,1) void stream(const char* k0, const char* v0, const char* k1, const char* v1){
  extern __shared__ char lds[];
  unsigned lane=threadIdx.x&63; unsigned w=__builtin_amdgcn_readfirstlane(threadIdx.x>>6);
  unsigned bid=blockIdx.x; bid=(bid&7)*32+(bid>>3);
  const char* k=(bid>=128)?k1:k0; const char* v=(bid>=128)?v1:v0; bid&=127;
  const char* base=((w&1)?v:k)+(size_t)(2*bid+(w>>1))*131072;
  __amdgpu_buffer_rsrc_t r=__builtin_amdgcn_make_buffer_rsrc((void*)base,0,131072,0x20000);
  unsigned vo=lane*16;
  for(int rep=0;rep<1;rep++)
  for(int it=0;it<16;it++){
    #pragma unroll
    for(int j=0;j<8;j++){
      unsigned m0v=w*32768+((it&3)*8+j)*1024; unsigned so=(it*8+j)*1024;
      asm volatile("s_mov_b32 m0, %0\n\ts_nop 0\n\tbuffer_load_dwordx4 %1, %2, %3 offen  lds" :: "s"(m0v),"v"(vo),"s"(r),"s"(so):"memory");
    }
    asm volatile("s_waitcnt vmcnt(16)":::"memory");
  }
  asm volatile("s_waitcnt vmcnt(0)":::"memory");
}
extern "C" void kernel_launch(void* const* d_in,const int* in_sizes,int n_in,void* d_out,int out_size,void* d_ws,size_t ws_size,hipStream_t stream_){
  u16* q=(u16*)d_ws; u16* o=q+(size_t)B*SQ*4096;
  hipFuncSetAttribute((const void*)stream, hipFuncAttributeMaxDynamicSharedMemorySize, 140000);
  stream<<<256,256,140000,stream_>>>((const char*)d_in[1],(const char*)d_in[2],(const char*)d_in[6],(const char*)d_in[7]);
  for(int br=0;br<2;br++){
    const u16* x=(const u16*)d_in[br*5+0]; const u16* k=(const u16*)d_in[br*5+1]; const u16* v=(const u16*)d_in[br*5+2];
    const u16* wq=(const u16*)d_in[br*5+3]; const u16* wo=(const u16*)d_in[br*5+4];
    gemm_naive<<<dim3(16,B*SQ/8),256,0,stream_>>>(x,wq,q,B*SQ,4096,D,4096);
    attn_naive<<<dim3(SQ,HQ,B),512,0,stream_>>>(q,k,v,o);
    gemm_naive<<<dim3(3,B*SQ/8),256,0,stream_>>>(o,wo,(u16*)d_out+br*D,B*SQ,D,4096,2*D);
  }
}
```

```cpp
#include <hip/hip_runtime.h>
#include <hip/hip_bf16.h>

struct Args { const char* p[2][8]; };

__global__ __launch_bounds__(256, 1) void gqa_pair_fused(Args A) {
  unsigned tA = threadIdx.x;
  asm volatile(
"; prologue\n"
"s_lshl_b32 s62, %[bid], 4\n"
"s_and_b32 s62, s62, 64\n"
"s_load_dwordx16 s[8:23], %[karg], s62\n"
"v_readfirstlane_b32 s36, %[tA]\n"
"v_and_b32 %[tA], 63, %[tA]\n"
"v_lshrrev_b32 v0, 4, %[tA]\n"
"v_bfe_u32 v1, %[tA], 2, 2\n"
"v_and_b32 v2, 3, %[tA]\n"
"s_bfe_u32 s64, %[bid], 0x10002\n"
"s_lshr_b32 s37, %[bid], 4\n"
"s_and_b32 s38, %[bid], 3\n"
"s_lshl_b32 s38, s38, 1\n"
"s_bfe_u32 s62, %[bid], 0x10003\n"
"s_or_b32 s38, s38, s62\n"
"s_lshr_b32 s36, s36, 6\n"
"s_mul_i32 s66, s36, 40960\n"
"s_mul_i32 s62, s37, 9\n"
"s_lshr_b32 s62, s62, 4\n"
"s_and_b32 s63, s38, 1\n"
"s_mul_i32 s63, s63, 4\n"
"s_add_u32 s62, s62, s63\n"
"s_lshr_b32 s63, s38, 1\n"
"s_mul_i32 s63, s63, 2\n"
"s_add_u32 s99, s62, s63\n"
"s_cmp_ge_u32 s99, 9\n"
"s_cselect_b32 s62, 9, 0\n"
"s_sub_u32 s99, s99, s62\n"
"s_cmp_ge_u32 s99, 9\n"
"s_cselect_b32 s62, 9, 0\n"
"s_sub_u32 s99, s99, s62\n"
"v_lshlrev_b32 v3, 4, v2\n"
"v_lshl_add_u32 v3, v0, 6, v3\n"
"v_lshl_add_u32 v243, v1, 13, v3\n"
"v_lshl_add_u32 v245, v1, 8, v3\n"
"v_xor_b32 v246, 32, v245\n"
"s_and_b32 s62, s36, 1\n"
"s_lshl_b32 s62, s62, 2\n"
"s_lshl_b32 s63, s36, 3\n"
"v_lshrrev_b32 v3, 3, %[tA]\n"
"v_add_u32 v3, s63, v3\n"
"v_mul_u32_u24 v244, 1152, v3\n"
"v_and_b32 v3, 7, %[tA]\n"
"v_xor_b32 v3, v3, v0\n"
"v_xor_b32 v3, s62, v3\n"
"v_lshl_add_u32 v244, v3, 4, v244\n"
"v_lshlrev_b32 v4, 6, v1\n"
"v_lshl_add_u32 v247, v2, 4, v4\n"
"v_lshl_add_u32 v247, v0, 11, v247\n"
"s_add_u32 s62, s66, 16384\n"
"v_add_u32 v247, s62, v247\n"
"v_and_b32 v3, 1, v0\n"
"v_lshl_add_u32 v4, v3, 5, v4\n"
"v_lshl_add_u32 v4, v2, 3, v4\n"
"v_lshl_add_u32 v253, v0, 9, v4\n"
"v_add_u32 v253, s66, v253\n"
"s_and_b32 s63, s36, 2\n"
"s_mul_i32 s63, s63, 40960\n"
"s_add_u32 s62, s63, 40960\n"
"v_lshl_add_u32 v250, v0, 10, v4\n"
"v_add_u32 v250, s62, v250\n"
"v_xor_b32 v251, 32, v250\n"
"v_lshrrev_b32 v4, 5, %[tA]\n"
"v_lshl_add_u32 v252, v4, 2, v1\n"
"v_mul_u32_u24 v252, 1152, v252\n"
"v_lshl_add_u32 v252, v3, 6, v252\n"
"v_lshlrev_b32 v4, 1, v4\n"
"v_xor_b32 v4, v4, v2\n"
"v_lshl_add_u32 v252, v4, 4, v252\n"
"v_and_b32 v3, 1, v1\n"
"v_lshlrev_b32 v3, 1, v3\n"
"v_xor_b32 v3, v3, v0\n"
"v_lshlrev_b32 v249, 4, v3\n"
"v_lshl_add_u32 v249, v2, 6, v249\n"
"v_lshl_add_u32 v249, v1, 10, v249\n"
"v_add_u32 v249, s63, v249\n"
"v_bfe_u32 v3, %[tA], 1, 3\n"
"v_xor_b32 v3, v3, v0\n"
"v_lshlrev_b32 v248, 4, v3\n"
"v_and_b32 v3, 7, %[tA]\n"
"v_lshl_add_u32 v248, v3, 7, v248\n"
"v_bfe_u32 v3, %[tA], 3, 1\n"
"v_mul_u32_u24 v3, 40960, v3\n"
"v_add_u32 v248, v248, v3\n"
"v_add_u32 v248, 32768, v248\n"
"v_lshlrev_b32 %[tA], 4, %[tA]\n"
"s_lshl_b32 s75, s38, 2\n"
"s_add_u32 s75, s75, s36\n"
"s_lshl_b32 s75, s75, 8\n"
"s_sub_u32 s26, 4718592, s75\n"
"s_mul_i32 s74, s37, 129024\n"
"s_mul_i32 s60, s99, 524288\n"
"s_lshl_b32 s61, s99, 7\n"
"s_mov_b32 s73, 0x40000000\n"
"s_cmp_ge_u32 s36, 2\n"
"s_cselect_b32 s63, s73, 0\n"
"v_or_b32 v224, s63, v244\n"
"s_getpc_b64 s[76:77]\n"
"Lcode0:\n"
"s_add_u32 s92, s76, Lal-Lcode0\n"
"s_addc_u32 s93, s77, 0\n"
"s_and_b32 s77, s77, 0xffff\n"
"s_mov_b32 s78, Lcode1-Lcode0\n"
"s_mov_b32 s79, 0x20000\n"
"v_lshlrev_b32 v231, 3, %[tA]\n"
"s_lshr_b32 s62, %[bid], 3\n"
"s_lshl_b32 s62, s62, 2\n"
"s_add_u32 s62, s62, s36\n"
"s_lshl_b32 s62, s62, 9\n"
"v_and_b32 v232, 384, v231\n"
"v_add_u32 v232, s62, v232\n"
"buffer_load_dword v232, v232, s[76:79], 0 offen\n"
"s_mov_b32 s59, Lh0-Lal\n"
"s_cmp_ge_u32 s36, 1\n"
"s_cselect_b32 s59, Lh1-Lal, s59\n"
"s_cmp_ge_u32 s36, 2\n"
"s_cselect_b32 s59, Lh2-Lal, s59\n"
"s_cmp_ge_u32 s36, 3\n"
"s_cselect_b32 s59, Lh3-Lal, s59\n"
"s_add_u32 s94, s92, s59\n"
"s_addc_u32 s95, s93, 0\n"
"s_mov_b64 exec, 0\n"
"s_setpc_b64 s[94:95]\n"
"Lw0:\n"
"s_mov_b64 exec, -1\n"
"s_waitcnt lgkmcnt(0)\n"
"s_add_u32 s24, s14, s75\n"
"s_addc_u32 s25, s15, 0\n"
"s_and_b32 s25, s25, 0xffff\n"
"s_mov_b32 s27, 0x20000\n"
"s_add_u32 m0, s66, 16384\n"
"s_add_u32 s62, s60, 0\n"
"buffer_load_dwordx4 v243, s[24:27], s62 offen lds\n"
"s_add_u32 s28, s8, s74\n"
"s_addc_u32 s29, s9, 0\n"
"s_and_b32 s29, s29, 0xffff\n"
"s_mov_b32 s30, 129024\n"
"s_mov_b32 s31, 0x20000\n"
"s_add_u32 m0, s66, 32768\n"
"s_add_u32 s62, s61, 0\n"
"buffer_load_dwordx4 v244, s[28:31], s62 offen lds\n"
"s_lshl_b32 s62, s37, 4\n"
"s_lshl_b32 s63, s38, 1\n"
"s_add_u32 s62, s62, s63\n"
"s_lshr_b32 s63, s36, 1\n"
"s_add_u32 s62, s62, s63\n"
"s_lshl_b32 s62, s62, 17\n"
"s_add_u32 s10, s10, s62\n"
"s_addc_u32 s11, s11, 0\n"
"s_and_b32 s11, s11, 0xffff\n"
"s_add_u32 s12, s12, s62\n"
"s_addc_u32 s13, s13, 0\n"
"s_and_b32 s13, s13, 0xffff\n"
"s_mul_i32 s62, s38, 589824\n"
"s_add_u32 s16, s16, s62\n"
"s_addc_u32 s17, s17, 0\n"
"s_and_b32 s17, s17, 0xffff\n"
"s_mul_i32 s62, s37, 258048\n"
"s_add_u32 s20, s20, s62\n"
"s_addc_u32 s21, s21, 0\n"
"s_and_b32 s21, s21, 0xffff\n"
"s_mul_i32 s62, s37, 926720\n"
"s_add_u32 s32, s18, s62\n"
"s_addc_u32 s33, s19, 0\n"
"s_and_b32 s33, s33, 0xffff\n"
"s_mov_b32 s34, 926720\n"
"s_mov_b32 s35, 0x20000\n"
"s_mul_i32 s39, s38, 5\n"
"s_add_u32 s39, s39, s37\n"
"s_lshl_b32 s63, s63, 3\n"
"s_add_u32 s39, s39, s63\n"
"s_mul_i32 s62, s64, 3\n"
"s_add_u32 s39, s39, s62\n"
"s_and_b32 s39, s39, 15\n"
"s_lshl_b32 s39, s39, 13\n"
"s_mul_i32 s62, s64, 1152\n"
"s_sub_u32 s8, 258048, s62\n"
"s_and_b32 s14, s38, 1\n"
"s_lshl_b32 s14, s14, 2\n"
"s_add_u32 s14, s14, s36\n"
"s_lshr_b32 s15, s38, 1\n"
"s_mul_i32 s100, s15, 3584\n"
"s_lshr_b32 s62, s14, 1\n"
"s_lshl_b32 s62, s62, 10\n"
"s_add_u32 s100, s100, s62\n"
"s_and_b32 s62, s14, 1\n"
"s_lshl_b32 s62, s62, 3\n"
"s_add_u32 s100, s100, s62\n"
"s_mul_i32 s101, s14, 36864\n"
"s_lshl_b32 s62, s15, 5\n"
"s_add_u32 s101, s101, s62\n"
"s_add_u32 s101, s101, 1024\n"
"s_cmp_eq_u32 s14, 7\n"
"s_cselect_b32 s100, s73, s100\n"
"s_cselect_b32 s101, s73, s101\n"
"s_cmp_eq_u32 s14, 6\n"
"s_cselect_b32 s9, 1, 0\n"
"s_bitcmp1_b32 s36, 0\n"
"s_cselect_b64 s[80:81], s[12:13], s[10:11]\n"
"s_mov_b32 s82, 131072\n"
"s_mov_b32 s83, 0x20000\n"
"s_mov_b32 s65, 0x3e0293ee\n"
"s_add_u32 s62, s38, s36\n"
"s_add_u32 s56, s62, 4\n"
"s_and_b32 s56, s56, 7\n"
"s_lshl_b32 s56, s56, 7\n"
"s_and_b32 s62, s62, 7\n"
"s_cmp_eq_u32 s36, 0\n"
"s_cselect_b32 s63, 8, s62\n"
"s_lshl_b32 s63, s63, 7\n"
"s_add_u32 s62, s36, 0\n"
"s_and_b32 s62, s62, 3\n"
"s_mul_i32 s48, s62, 40960\n"
"s_mul_i32 s44, s62, 147456\n"
"s_add_u32 s62, s36, 1\n"
"s_and_b32 s62, s62, 3\n"
"s_mul_i32 s49, s62, 40960\n"
"s_mul_i32 s45, s62, 147456\n"
"s_add_u32 s62, s36, 2\n"
"s_and_b32 s62, s62, 3\n"
"s_mul_i32 s50, s62, 40960\n"
"s_mul_i32 s46, s62, 147456\n"
"s_add_u32 s62, s36, 3\n"
"s_and_b32 s62, s62, 3\n"
"s_mul_i32 s51, s62, 40960\n"
"s_mul_i32 s47, s62, 147456\n"
"s_lshl_b32 s58, s38, 7\n"
"s_add_u32 s72, s44, s58\n"
"s_add_u32 s40, s44, s56\n"
"s_add_u32 s44, s44, s63\n"
"s_add_u32 s41, s45, s56\n"
"s_add_u32 s45, s45, s63\n"
"s_add_u32 s42, s46, s56\n"
"s_add_u32 s46, s46, s63\n"
"s_add_u32 s43, s47, s56\n"
"s_add_u32 s47, s47, s63\n"
"s_mul_i32 s57, s36, 3584\n"
"s_and_b32 s56, s36, 1\n"
"s_lshl_b32 s56, s56, 5\n"
"s_lshr_b32 s67, s36, 1\n"
"s_lshl_b32 s67, s67, 8\n"
"s_sub_u32 s67, s67, s66\n"
"s_lshl_b32 s62, s38, 2\n"
"s_add_u32 s62, s62, s36\n"
"s_mul_i32 s52, s62, 28928\n"
"s_lshl_b32 s53, s38, 7\n"
"s_add_u32 s53, s53, 925696\n"
"s_lshl_b32 s55, s38, 2\n"
"s_add_u32 s55, s55, 925696\n"
"s_add_u32 s86, s66, 8192\n"
"s_mov_b32 s87, s39\n"
"s_mov_b32 s98, 0x1ffff\n"
"s_mov_b32 s84, 6\n"
"s_mov_b32 s96, 8192\n"
"s_mov_b32 s88, 0\n"
"s_mov_b32 s90, 0\n"
"s_add_u32 s89, s66, 40960\n"
"s_cmp_eq_u32 s36, 0\n"
"s_cselect_b32 s54, s55, s73\n"
"s_cselect_b32 s74, s53, s73\n"
"v_xor_b32 v225, 64, v248\n"
"v_add_u32 v226, 81920, v248\n"
"v_add_u32 v227, 81920, v225\n"
"v_lshrrev_b32 v233, 2, %[tA]\n"
"v_and_b32 v233, 28, v233\n"
"v_mov_b32 v234, 0\n"
"s_mov_b64 s[76:77], s[16:17]\n"
"s_mov_b32 s78, 589824\n"
"s_add_u32 m0, s66, 18432\n"
"s_add_u32 s62, s60, 65536\n"
"buffer_load_dwordx4 v243, s[24:27], s62 offen lds\n"
"s_add_u32 m0, s66, 20480\n"
"s_add_u32 s62, s60, 131072\n"
"buffer_load_dwordx4 v243, s[24:27], s62 offen lds\n"
"s_add_u32 m0, s66, 22528\n"
"s_add_u32 s62, s60, 196608\n"
"buffer_load_dwordx4 v243, s[24:27], s62 offen lds\n"
"s_add_u32 m0, s66, 17408\n"
"s_add_u32 s62, s60, 32768\n"
"buffer_load_dwordx4 v243, s[24:27], s62 offen lds\n"
"s_add_u32 m0, s66, 19456\n"
"s_add_u32 s62, s60, 98304\n"
"buffer_load_dwordx4 v243, s[24:27], s62 offen lds\n"
"s_add_u32 m0, s66, 21504\n"
"s_add_u32 s62, s60, 163840\n"
"buffer_load_dwordx4 v243, s[24:27], s62 offen lds\n"
"s_add_u32 m0, s66, 23552\n"
"s_add_u32 s62, s60, 229376\n"
"buffer_load_dwordx4 v243, s[24:27], s62 offen lds\n"
"s_add_u32 m0, s66, 33792\n"
"s_add_u32 s62, s61, 36864\n"
"buffer_load_dwordx4 v244, s[28:31], s62 offen lds\n"
"s_add_u32 m0, s66, 34816\n"
"s_add_u32 s62, s61, 73728\n"
"buffer_load_dwordx4 v244, s[28:31], s62 offen lds\n"
"s_add_u32 m0, s66, 35840\n"
"s_add_u32 s62, s61, 110592\n"
"buffer_load_dwordx4 v224, s[28:31], s62 offen lds\n"
"s_add_u32 s60, s60, 262144\n"
"s_cmp_ge_u32 s60, 4718592\n"
"s_cselect_b32 s63, 4718592, 0\n"
"s_sub_u32 s60, s60, s63\n"
"s_add_u32 s61, s61, 128\n"
"s_cmp_ge_u32 s61, 1152\n"
"s_cselect_b32 s63, 1152, 0\n"
"s_sub_u32 s61, s61, s63\n"
"s_add_u32 m0, s66, 24576\n"
"s_add_u32 s62, s60, 0\n"
"buffer_load_dwordx4 v243, s[24:27], s62 offen lds\n"
"s_add_u32 m0, s66, 26624\n"
"s_add_u32 s62, s60, 65536\n"
"buffer_load_dwordx4 v243, s[24:27], s62 offen lds\n"
"s_add_u32 m0, s66, 28672\n"
"s_add_u32 s62, s60, 131072\n"
"buffer_load_dwordx4 v243, s[24:27], s62 offen lds\n"
"s_add_u32 m0, s66, 30720\n"
"s_add_u32 s62, s60, 196608\n"
"buffer_load_dwordx4 v243, s[24:27], s62 offen lds\n"
"s_add_u32 m0, s66, 25600\n"
"s_add_u32 s62, s60, 32768\n"
"buffer_load_dwordx4 v243, s[24:27], s62 offen lds\n"
"s_add_u32 m0, s66, 27648\n"
"s_add_u32 s62, s60, 98304\n"
"buffer_load_dwordx4 v243, s[24:27], s62 offen lds\n"
"s_add_u32 m0, s66, 29696\n"
"s_add_u32 s62, s60, 163840\n"
"buffer_load_dwordx4 v243, s[24:27], s62 offen lds\n"
"s_add_u32 m0, s66, 31744\n"
"s_add_u32 s62, s60, 229376\n"
"buffer_load_dwordx4 v243, s[24:27], s62 offen lds\n"
"s_add_u32 s60, s60, 262144\n"
"s_cmp_ge_u32 s60, 4718592\n"
"s_cselect_b32 s63, 4718592, 0\n"
"s_sub_u32 s60, s60, s63\n"
"s_waitcnt vmcnt(11)\n"
"ds_read_b64_tr_b16 a[0:1], v247 offset:0\n"
"ds_read_b64_tr_b16 a[2:3], v247 offset:1024\n"
"ds_read_b64_tr_b16 a[4:5], v247 offset:8\n"
"ds_read_b64_tr_b16 a[6:7], v247 offset:1032\n"
"ds_read_b64_tr_b16 a[8:9], v247 offset:256\n"
"ds_read_b64_tr_b16 a[10:11], v247 offset:1280\n"
"ds_read_b64_tr_b16 a[12:13], v247 offset:264\n"
"ds_read_b64_tr_b16 a[14:15], v247 offset:1288\n"
"ds_read_b64_tr_b16 a[16:17], v247 offset:512\n"
"ds_read_b64_tr_b16 a[18:19], v247 offset:1536\n"
"ds_read_b64_tr_b16 a[20:21], v247 offset:520\n"
"ds_read_b64_tr_b16 a[22:23], v247 offset:1544\n"
"ds_read_b64_tr_b16 a[24:25], v247 offset:768\n"
"ds_read_b64_tr_b16 a[26:27], v247 offset:1792\n"
"ds_read_b64_tr_b16 a[28:29], v247 offset:776\n"
"ds_read_b64_tr_b16 a[30:31], v247 offset:1800\n"
"s_waitcnt vmcnt(8)\n"
"s_barrier\n"
"ds_read_b128 a[128:131], v248 offset:0\n"
"ds_read_b128 a[132:135], v226 offset:0\n"
"ds_read_b128 a[136:139], v248 offset:1024\n"
"s_waitcnt lgkmcnt(2)\n"
"v_mfma_f32_16x16x32_bf16 v[0:3], a[0:3], a[128:131], 0\n"
"ds_read_b128 a[140:143], v226 offset:1024\n"
"v_mfma_f32_16x16x32_bf16 v[112:115], a[4:7], a[128:131], 0\n"
"ds_read_b128 a[144:147], v248 offset:2048\n"
"v_mfma_f32_16x16x32_bf16 v[4:7], a[8:11], a[128:131], 0\n"
"ds_read_b128 a[148:151], v226 offset:2048\n"
"v_mfma_f32_16x16x32_bf16 v[116:119], a[12:15], a[128:131], 0\n"
"s_add_u32 m0, s66, 16384\n"
"s_add_u32 s62, s60, 0\n"
"ds_read_b128 a[152:155], v248 offset:3072\n"
"v_mfma_f32_16x16x32_bf16 v[8:11], a[16:19], a[128:131], 0\n"
"buffer_load_dwordx4 v243, s[24:27], s62 offen lds\n"
"v_mfma_f32_16x16x32_bf16 v[120:123], a[20:23], a[128:131], 0\n"
"v_mfma_f32_16x16x32_bf16 v[12:15], a[24:27], a[128:131], 0\n"
"s_waitcnt lgkmcnt(5)\n"
"ds_read_b128 a[156:159], v225 offset:0\n"
"v_mfma_f32_16x16x32_bf16 v[124:127], a[28:31], a[128:131], 0\n"
"s_add_u32 m0, s66, 18432\n"
"s_add_u32 s62, s60, 65536\n"
"v_mfma_f32_16x16x32_bf16 v[16:19], a[0:3], a[132:135], 0\n"
"buffer_load_dwordx4 v243, s[24:27], s62 offen lds\n"
"v_mfma_f32_16x16x32_bf16 v[128:131], a[4:7], a[132:135], 0\n"
"ds_read_b128 a[160:163], v227 offset:0\n"
"v_mfma_f32_16x16x32_bf16 v[20:23], a[8:11], a[132:135], 0\n"
"s_add_u32 m0, s66, 20480\n"
"s_add_u32 s62, s60, 131072\n"
"s_waitcnt vmcnt(6)\n"
"ds_read_b64_tr_b16 a[32:33], v247 offset:8192\n"
"v_mfma_f32_16x16x32_bf16 v[132:135], a[12:15], a[132:135], 0\n"
"buffer_load_dwordx4 v243, s[24:27], s62 offen lds\n"
"v_mfma_f32_16x16x32_bf16 v[24:27], a[16:19], a[132:135], 0\n"
"ds_read_b128 a[164:167], v225 offset:1024\n"
"v_mfma_f32_16x16x32_bf16 v[136:139], a[20:23], a[132:135], 0\n"
"s_add_u32 m0, s66, 22528\n"
"s_add_u32 s62, s60, 196608\n"
"ds_read_b64_tr_b16 a[36:37], v247 offset:8200\n"
"v_mfma_f32_16x16x32_bf16 v[28:31], a[24:27], a[132:135], 0\n"
"s_waitcnt lgkmcnt(9)\n"
"buffer_load_dwordx4 v243, s[24:27], s62 offen lds\n"
"v_mfma_f32_16x16x32_bf16 v[140:143], a[28:31], a[132:135], 0\n"
"ds_read_b128 a[168:171], v227 offset:1024\n"
"v_mfma_f32_16x16x32_bf16 v[32:35], a[0:3], a[136:139], 0\n"
"s_add_u32 m0, s66, 17408\n"
"s_add_u32 s62, s60, 32768\n"
"ds_read_b64_tr_b16 a[40:41], v247 offset:8448\n"
"v_mfma_f32_16x16x32_bf16 v[144:147], a[4:7], a[136:139], 0\n"
"buffer_load_dwordx4 v243, s[24:27], s62 offen lds\n"
"v_mfma_f32_16x16x32_bf16 v[36:39], a[8:11], a[136:139], 0\n"
"ds_read_b128 a[172:175], v225 offset:2048\n"
"v_mfma_f32_16x16x32_bf16 v[148:151], a[12:15], a[136:139], 0\n"
"s_add_u32 m0, s66, 19456\n"
"s_add_u32 s62, s60, 98304\n"
"ds_read_b64_tr_b16 a[44:45], v247 offset:8456\n"
"v_mfma_f32_16x16x32_bf16 v[40:43], a[16:19], a[136:139], 0\n"
"buffer_load_dwordx4 v243, s[24:27], s62 offen lds\n"
"v_mfma_f32_16x16x32_bf16 v[152:155], a[20:23], a[136:139], 0\n"
"ds_read_b128 a[176:179], v227 offset:2048\n"
"v_mfma_f32_16x16x32_bf16 v[44:47], a[24:27], a[136:139], 0\n"
"s_waitcnt lgkmcnt(13)\n"
"ds_read_b64_tr_b16 a[48:49], v247 offset:8704\n"
"v_mfma_f32_16x16x32_bf16 v[156:159], a[28:31], a[136:139], 0\n"
"ds_read_b64_tr_b16 a[52:53], v247 offset:8712\n"
"v_mfma_f32_16x16x32_bf16 v[48:51], a[0:3], a[140:143], 0\n"
"s_add_u32 m0, s66, 21504\n"
"s_add_u32 s62, s60, 163840\n"
"ds_read_b128 a[180:183], v225 offset:3072\n"
"v_mfma_f32_16x16x32_bf16 v[160:163], a[4:7], a[140:143], 0\n"
"buffer_load_dwordx4 v243, s[24:27], s62 offen lds\n"
"v_mfma_f32_16x16x32_bf16 v[52:55], a[8:11], a[140:143], 0\n"
"ds_read_b64_tr_b16 a[56:57], v247 offset:8960\n"
"v_mfma_f32_16x16x32_bf16 v[164:167], a[12:15], a[140:143], 0\n"
"s_add_u32 m0, s66, 23552\n"
"s_add_u32 s62, s60, 229376\n"
"ds_read_b64_tr_b16 a[60:61], v247 offset:8968\n"
"v_mfma_f32_16x16x32_bf16 v[56:59], a[16:19], a[140:143], 0\n"
"buffer_load_dwordx4 v243, s[24:27], s62 offen lds\n"
"v_mfma_f32_16x16x32_bf16 v[168:171], a[20:23], a[140:143], 0\n"
"s_add_u32 s60, s60, 262144\n"
"s_waitcnt vmcnt(8)\n"
"ds_read_b64_tr_b16 a[34:35], v247 offset:9216\n"
"v_mfma_f32_16x16x32_bf16 v[60:63], a[24:27], a[140:143], 0\n"
"s_waitcnt lgkmcnt(15)\n"
"s_cmp_ge_u32 s60, 4718592\n"
"s_cselect_b32 s63, 4718592, 0\n"
"ds_read_b64_tr_b16 a[38:39], v247 offset:9224\n"
"v_mfma_f32_16x16x32_bf16 v[172:175], a[28:31], a[140:143], 0\n"
"s_sub_u32 s60, s60, s63\n"
"ds_read_b64_tr_b16 a[42:43], v247 offset:9472\n"
"v_mfma_f32_16x16x32_bf16 v[64:67], a[0:3], a[144:147], 0\n"
"s_add_u32 m0, s66, 36864\n"
"s_add_u32 s62, s61, 0\n"
"ds_read_b64_tr_b16 a[46:47], v247 offset:9480\n"
"v_mfma_f32_16x16x32_bf16 v[176:179], a[4:7], a[144:147], 0\n"
"buffer_load_dwordx4 v244, s[28:31], s62 offen lds\n"
"v_mfma_f32_16x16x32_bf16 v[68:71], a[8:11], a[144:147], 0\n"
"ds_read_b64_tr_b16 a[50:51], v247 offset:9728\n"
"v_mfma_f32_16x16x32_bf16 v[180:183], a[12:15], a[144:147], 0\n"
"s_add_u32 m0, s66, 37888\n"
"s_add_u32 s62, s61, 36864\n"
"ds_read_b64_tr_b16 a[54:55], v247 offset:9736\n"
"v_mfma_f32_16x16x32_bf16 v[72:75], a[16:19], a[144:147], 0\n"
"buffer_load_dwordx4 v244, s[28:31], s62 offen lds\n"
"v_mfma_f32_16x16x32_bf16 v[184:187], a[20:23], a[144:147], 0\n"
"ds_read_b64_tr_b16 a[58:59], v247 offset:9984\n"
"v_mfma_f32_16x16x32_bf16 v[76:79], a[24:27], a[144:147], 0\n"
"ds_read_b64_tr_b16 a[62:63], v247 offset:9992\n"
"v_mfma_f32_16x16x32_bf16 v[188:191], a[28:31], a[144:147], 0\n"
"s_add_u32 m0, s66, 38912\n"
"s_add_u32 s62, s61, 73728\n"
"v_mfma_f32_16x16x32_bf16 v[80:83], a[0:3], a[148:151], 0\n"
"buffer_load_dwordx4 v244, s[28:31], s62 offen lds\n"
"v_mfma_f32_16x16x32_bf16 v[192:195], a[4:7], a[148:151], 0\n"
"v_mfma_f32_16x16x32_bf16 v[84:87], a[8:11], a[148:151], 0\n"
"s_add_u32 m0, s66, 39936\n"
"s_add_u32 s62, s61, 110592\n"
"v_mfma_f32_16x16x32_bf16 v[196:199], a[12:15], a[148:151], 0\n"
"buffer_load_dwordx4 v224, s[28:31], s62 offen lds\n"
"v_mfma_f32_16x16x32_bf16 v[88:91], a[16:19], a[148:151], 0\n"
"s_add_u32 s61, s61, 128\n"
"v_mfma_f32_16x16x32_bf16 v[200:203], a[20:23], a[148:151], 0\n"
"s_cmp_ge_u32 s61, 1152\n"
"s_cselect_b32 s63, 1152, 0\n"
"v_mfma_f32_16x16x32_bf16 v[92:95], a[24:27], a[148:151], 0\n"
"s_sub_u32 s61, s61, s63\n"
"v_mfma_f32_16x16x32_bf16 v[204:207], a[28:31], a[148:151], 0\n"
"s_add_u32 m0, s66, 24576\n"
"s_add_u32 s62, s60, 0\n"
"v_mfma_f32_16x16x32_bf16 v[96:99], a[0:3], a[152:155], 0\n"
"s_waitcnt lgkmcnt(8)\n"
"buffer_load_dwordx4 v243, s[24:27], s62 offen lds\n"
"v_mfma_f32_16x16x32_bf16 v[208:211], a[4:7], a[152:155], 0\n"
"v_mfma_f32_16x16x32_bf16 v[100:103], a[8:11], a[152:155], 0\n"
"s_add_u32 m0, s66, 26624\n"
"s_add_u32 s62, s60, 65536\n"
"v_mfma_f32_16x16x32_bf16 v[212:215], a[12:15], a[152:155], 0\n"
"buffer_load_dwordx4 v243, s[24:27], s62 offen lds\n"
"v_mfma_f32_16x16x32_bf16 v[104:107], a[16:19], a[152:155], 0\n"
"v_mfma_f32_16x16x32_bf16 v[216:219], a[20:23], a[152:155], 0\n"
"s_add_u32 m0, s66, 28672\n"
"s_add_u32 s62, s60, 131072\n"
"v_mfma_f32_16x16x32_bf16 v[108:111], a[24:27], a[152:155], 0\n"
"buffer_load_dwordx4 v243, s[24:27], s62 offen lds\n"
"v_mfma_f32_16x16x32_bf16 v[220:223], a[28:31], a[152:155], 0\n"
"s_waitcnt lgkmcnt(0)\n"
"v_mfma_f32_16x16x32_bf16 v[0:3], a[32:35], a[156:159], v[0:3]\n"
"s_add_u32 m0, s66, 30720\n"
"s_add_u32 s62, s60, 196608\n"
"v_mfma_f32_16x16x32_bf16 v[112:115], a[36:39], a[156:159], v[112:115]\n"
"buffer_load_dwordx4 v243, s[24:27], s62 offen lds\n"
"v_mfma_f32_16x16x32_bf16 v[4:7], a[40:43], a[156:159], v[4:7]\n"
"v_mfma_f32_16x16x32_bf16 v[116:119], a[44:47], a[156:159], v[116:119]\n"
"v_mfma_f32_16x16x32_bf16 v[8:11], a[48:51], a[156:159], v[8:11]\n"
"s_add_u32 m0, s66, 32768\n"
"s_add_u32 s62, s61, 0\n"
"s_waitcnt vmcnt(4)\n"
"s_barrier\n"
"v_mfma_f32_16x16x32_bf16 v[120:123], a[52:55], a[156:159], v[120:123]\n"
"buffer_load_dwordx4 v244, s[28:31], s62 offen lds\n"
"v_mfma_f32_16x16x32_bf16 v[12:15], a[56:59], a[156:159], v[12:15]\n"
"ds_read_b128 a[128:131], v248 offset:4096\n"
"v_mfma_f32_16x16x32_bf16 v[124:127], a[60:63], a[156:159], v[124:127]\n"
"s_add_u32 m0, s66, 33792\n"
"s_add_u32 s62, s61, 36864\n"
"v_mfma_f32_16x16x32_bf16 v[16:19], a[32:35], a[160:163], v[16:19]\n"
"buffer_load_dwordx4 v244, s[28:31], s62 offen lds\n"
"v_mfma_f32_16x16x32_bf16 v[128:131], a[36:39], a[160:163], v[128:131]\n"
"ds_read_b128 a[132:135], v226 offset:4096\n"
"v_mfma_f32_16x16x32_bf16 v[20:23], a[40:43], a[160:163], v[20:23]\n"
"s_add_u32 m0, s66, 25600\n"
"s_add_u32 s62, s60, 32768\n"
"v_mfma_f32_16x16x32_bf16 v[132:135], a[44:47], a[160:163], v[132:135]\n"
"buffer_load_dwordx4 v243, s[24:27], s62 offen lds\n"
"v_mfma_f32_16x16x32_bf16 v[24:27], a[48:51], a[160:163], v[24:27]\n"
"ds_read_b128 a[136:139], v248 offset:5120\n"
"v_mfma_f32_16x16x32_bf16 v[136:139], a[52:55], a[160:163], v[136:139]\n"
"s_add_u32 m0, s66, 27648\n"
"s_add_u32 s62, s60, 98304\n"
"v_mfma_f32_16x16x32_bf16 v[28:31], a[56:59], a[160:163], v[28:31]\n"
"buffer_load_dwordx4 v243, s[24:27], s62 offen lds\n"
"v_mfma_f32_16x16x32_bf16 v[140:143], a[60:63], a[160:163], v[140:143]\n"
"ds_read_b128 a[140:143], v226 offset:5120\n"
"v_mfma_f32_16x16x32_bf16 v[32:35], a[32:35], a[164:167], v[32:35]\n"
"s_add_u32 m0, s66, 29696\n"
"s_add_u32 s62, s60, 163840\n"
"ds_read_b64_tr_b16 a[64:65], v247 offset:0\n"
"v_mfma_f32_16x16x32_bf16 v[144:147], a[36:39], a[164:167], v[144:147]\n"
"buffer_load_dwordx4 v243, s[24:27], s62 offen lds\n"
"v_mfma_f32_16x16x32_bf16 v[36:39], a[40:43], a[164:167], v[36:39]\n"
"ds_read_b128 a[144:147], v248 offset:6144\n"
"v_mfma_f32_16x16x32_bf16 v[148:151], a[44:47], a[164:167], v[148:151]\n"
"s_add_u32 m0, s66, 31744\n"
"s_add_u32 s62, s60, 229376\n"
"ds_read_b64_tr_b16 a[68:69], v247 offset:8\n"
"v_mfma_f32_16x16x32_bf16 v[40:43], a[48:51], a[164:167], v[40:43]\n"
"buffer_load_dwordx4 v243, s[24:27], s62 offen lds\n"
"v_mfma_f32_16x16x32_bf16 v[152:155], a[52:55], a[164:167], v[152:155]\n"
"s_add_u32 s60, s60, 262144\n"
"ds_read_b128 a[148:151], v226 offset:6144\n"
"v_mfma_f32_16x16x32_bf16 v[44:47], a[56:59], a[164:167], v[44:47]\n"
"s_cmp_ge_u32 s60, 4718592\n"
"s_cselect_b32 s63, 4718592, 0\n"
"ds_read_b64_tr_b16 a[72:73], v247 offset:256\n"
"v_mfma_f32_16x16x32_bf16 v[156:159], a[60:63], a[164:167], v[156:159]\n"
"s_sub_u32 s60, s60, s63\n"
"ds_read_b64_tr_b16 a[76:77], v247 offset:264\n"
"v_mfma_f32_16x16x32_bf16 v[48:51], a[32:35], a[168:171], v[48:51]\n"
"s_add_u32 m0, s66, 34816\n"
"s_add_u32 s62, s61, 73728\n"
"ds_read_b128 a[152:155], v248 offset:7168\n"
"v_mfma_f32_16x16x32_bf16 v[160:163], a[36:39], a[168:171], v[160:163]\n"
"buffer_load_dwordx4 v244, s[28:31], s62 offen lds\n"
"v_mfma_f32_16x16x32_bf16 v[52:55], a[40:43], a[168:171], v[52:55]\n"
"ds_read_b64_tr_b16 a[80:81], v247 offset:512\n"
"v_mfma_f32_16x16x32_bf16 v[164:167], a[44:47], a[168:171], v[164:167]\n"
"ds_read_b64_tr_b16 a[84:85], v247 offset:520\n"
"v_mfma_f32_16x16x32_bf16 v[56:59], a[48:51], a[168:171], v[56:59]\n"
"ds_read_b128 a[156:159], v225 offset:4096\n"
"v_mfma_f32_16x16x32_bf16 v[168:171], a[52:55], a[168:171], v[168:171]\n"
"ds_read_b128 a[160:163], v227 offset:4096\n"
"v_mfma_f32_16x16x32_bf16 v[60:63], a[56:59], a[168:171], v[60:63]\n"
"s_add_u32 m0, s66, 35840\n"
"s_add_u32 s62, s61, 110592\n"
"ds_read_b128 a[164:167], v225 offset:5120\n"
"v_mfma_f32_16x16x32_bf16 v[172:175], a[60:63], a[168:171], v[172:175]\n"
"buffer_load_dwordx4 v224, s[28:31], s62 offen lds\n"
"v_mfma_f32_16x16x32_bf16 v[64:67], a[32:35], a[172:175], v[64:67]\n"
"s_add_u32 s61, s61, 128\n"
"ds_read_b64_tr_b16 a[88:89], v247 offset:768\n"
"v_mfma_f32_16x16x32_bf16 v[176:179], a[36:39], a[172:175], v[176:179]\n"
"s_cmp_ge_u32 s61, 1152\n"
"s_cselect_b32 s63, 1152, 0\n"
"ds_read_b128 a[168:171], v227 offset:5120\n"
"v_mfma_f32_16x16x32_bf16 v[68:71], a[40:43], a[172:175], v[68:71]\n"
"s_sub_u32 s61, s61, s63\n"
"ds_read_b64_tr_b16 a[92:93], v247 offset:776\n"
"v_mfma_f32_16x16x32_bf16 v[180:183], a[44:47], a[172:175], v[180:183]\n"
"s_branch Lal\n"
".p2align 12\n"
"Lal:\n"
"Lh0:\n"
"s_cbranch_execz Lw0\n"
"ds_read_b64_tr_b16 a[66:67], v247 offset:1024\n"
"v_mfma_f32_16x16x32_bf16 v[72:75], a[48:51], a[172:175], v[72:75]\n"
"ds_read_b64_tr_b16 a[70:71], v247 offset:1032\n"
"v_mfma_f32_16x16x32_bf16 v[184:187], a[52:55], a[172:175], v[184:187]\n"
"ds_read_b64_tr_b16 a[74:75], v247 offset:1280\n"
"v_mfma_f32_16x16x32_bf16 v[76:79], a[56:59], a[172:175], v[76:79]\n"
"ds_read_b64_tr_b16 a[78:79], v247 offset:1288\n"
"v_mfma_f32_16x16x32_bf16 v[188:191], a[60:63], a[172:175], v[188:191]\n"
"ds_read_b64_tr_b16 a[82:83], v247 offset:1536\n"
"v_mfma_f32_16x16x32_bf16 v[80:83], a[32:35], a[176:179], v[80:83]\n"
"s_add_u32 m0, s66, 16384\n"
"s_add_u32 s62, s60, 0\n"
"ds_read_b128 a[172:175], v225 offset:6144\n"
"v_mfma_f32_16x16x32_bf16 v[192:195], a[36:39], a[176:179], v[192:195]\n"
"s_waitcnt lgkmcnt(6)\n"
"buffer_load_dwordx4 v243, s[24:27], s62 offen lds\n"
"v_mfma_f32_16x16x32_bf16 v[84:87], a[40:43], a[176:179], v[84:87]\n"
"ds_read_b64_tr_b16 a[86:87], v247 offset:1544\n"
"v_mfma_f32_16x16x32_bf16 v[196:199], a[44:47], a[176:179], v[196:199]\n"
"s_add_u32 m0, s66, 18432\n"
"s_add_u32 s62, s60, 65536\n"
"ds_read_b64_tr_b16 a[90:91], v247 offset:1792\n"
"v_mfma_f32_16x16x32_bf16 v[88:91], a[48:51], a[176:179], v[88:91]\n"
"buffer_load_dwordx4 v243, s[24:27], s62 offen lds\n"
"v_mfma_f32_16x16x32_bf16 v[200:203], a[52:55], a[176:179], v[200:203]\n"
"ds_read_b64_tr_b16 a[94:95], v247 offset:1800\n"
"v_mfma_f32_16x16x32_bf16 v[92:95], a[56:59], a[176:179], v[92:95]\n"
"s_add_u32 m0, s66, 20480\n"
"s_add_u32 s62, s60, 131072\n"
"v_mfma_f32_16x16x32_bf16 v[204:207], a[60:63], a[176:179], v[204:207]\n"
"buffer_load_dwordx4 v243, s[24:27], s62 offen lds\n"
"v_mfma_f32_16x16x32_bf16 v[96:99], a[32:35], a[180:183], v[96:99]\n"
"v_mfma_f32_16x16x32_bf16 v[208:211], a[36:39], a[180:183], v[208:211]\n"
"s_add_u32 m0, s66, 22528\n"
"s_add_u32 s62, s60, 196608\n"
"ds_read_b128 a[176:179], v227 offset:6144\n"
"v_mfma_f32_16x16x32_bf16 v[100:103], a[40:43], a[180:183], v[100:103]\n"
"buffer_load_dwordx4 v243, s[24:27], s62 offen lds\n"
"v_mfma_f32_16x16x32_bf16 v[212:215], a[44:47], a[180:183], v[212:215]\n"
"v_mfma_f32_16x16x32_bf16 v[104:107], a[48:51], a[180:183], v[104:107]\n"
"s_add_u32 m0, s66, 17408\n"
"s_add_u32 s62, s60, 32768\n"
"v_mfma_f32_16x16x32_bf16 v[216:219], a[52:55], a[180:183], v[216:219]\n"
"s_waitcnt lgkmcnt(1)\n"
"buffer_load_dwordx4 v243, s[24:27], s62 offen lds\n"
"v_mfma_f32_16x16x32_bf16 v[108:111], a[56:59], a[180:183], v[108:111]\n"
"s_mov_b32 s59, Lh4-Lal\n"
"s_cmp_ge_u32 s36, 1\n"
"s_cselect_b32 s59, Lh5-Lal, s59\n"
"s_cmp_ge_u32 s36, 2\n"
"s_cselect_b32 s59, Lh6-Lal, s59\n"
"s_cmp_ge_u32 s36, 3\n"
"s_cselect_b32 s59, Lh7-Lal, s59\n"
"s_add_u32 s94, s92, s59\n"
"s_addc_u32 s95, s93, 0\n"
"s_mov_b64 exec, 0\n"
"s_setpc_b64 s[94:95]\n"
"Lw1:\n"
"s_mov_b64 exec, -1\n"
"v_mfma_f32_16x16x32_bf16 v[220:223], a[60:63], a[180:183], v[220:223]\n"
"v_mfma_f32_16x16x32_bf16 v[0:3], a[64:67], a[128:131], v[0:3]\n"
"s_add_u32 m0, s66, 19456\n"
"s_add_u32 s62, s60, 98304\n"
"ds_read_b128 a[180:183], v225 offset:7168\n"
"v_mfma_f32_16x16x32_bf16 v[112:115], a[68:71], a[128:131], v[112:115]\n"
"buffer_load_dwordx4 v243, s[24:27], s62 offen lds\n"
"v_mfma_f32_16x16x32_bf16 v[4:7], a[72:75], a[128:131], v[4:7]\n"
"v_mfma_f32_16x16x32_bf16 v[116:119], a[76:79], a[128:131], v[116:119]\n"
"s_add_u32 m0, s66, 21504\n"
"s_add_u32 s62, s60, 163840\n"
"s_waitcnt vmcnt(14)\n"
"ds_read_b64_tr_b16 a[96:97], v247 offset:8192\n"
"v_mfma_f32_16x16x32_bf16 v[8:11], a[80:83], a[128:131], v[8:11]\n"
"buffer_load_dwordx4 v243, s[24:27], s62 offen lds\n"
"v_mfma_f32_16x16x32_bf16 v[120:123], a[84:87], a[128:131], v[120:123]\n"
"ds_read_b64_tr_b16 a[100:101], v247 offset:8200\n"
"v_mfma_f32_16x16x32_bf16 v[12:15], a[88:91], a[128:131], v[12:15]\n"
"s_add_u32 m0, s66, 23552\n"
"s_add_u32 s62, s60, 229376\n"
"ds_read_b64_tr_b16 a[104:105], v247 offset:8448\n"
"v_mfma_f32_16x16x32_bf16 v[124:127], a[92:95], a[128:131], v[124:127]\n"
"buffer_load_dwordx4 v243, s[24:27], s62 offen lds\n"
"v_mfma_f32_16x16x32_bf16 v[16:19], a[64:67], a[132:135], v[16:19]\n"
"s_add_u32 s60, s60, 262144\n"
"ds_read_b64_tr_b16 a[108:109], v247 offset:8456\n"
"v_mfma_f32_16x16x32_bf16 v[128:131], a[68:71], a[132:135], v[128:131]\n"
"s_cmp_ge_u32 s60, 4718592\n"
"s_cselect_b32 s63, 4718592, 0\n"
"ds_read_b64_tr_b16 a[112:113], v247 offset:8704\n"
"v_mfma_f32_16x16x32_bf16 v[20:23], a[72:75], a[132:135], v[20:23]\n"
"s_sub_u32 s60, s60, s63\n"
"ds_read_b64_tr_b16 a[116:117], v247 offset:8712\n"
"v_mfma_f32_16x16x32_bf16 v[132:135], a[76:79], a[132:135], v[132:135]\n"
"ds_read_b64_tr_b16 a[120:121], v247 offset:8960\n"
"v_mfma_f32_16x16x32_bf16 v[24:27], a[80:83], a[132:135], v[24:27]\n"
"ds_read_b64_tr_b16 a[124:125], v247 offset:8968\n"
"v_mfma_f32_16x16x32_bf16 v[136:139], a[84:87], a[132:135], v[136:139]\n"
"v_mfma_f32_16x16x32_bf16 v[28:31], a[88:91], a[132:135], v[28:31]\n"
"v_mfma_f32_16x16x32_bf16 v[140:143], a[92:95], a[132:135], v[140:143]\n"
"v_mfma_f32_16x16x32_bf16 v[32:35], a[64:67], a[136:139], v[32:35]\n"
"v_mfma_f32_16x16x32_bf16 v[144:147], a[68:71], a[136:139], v[144:147]\n"
"v_mfma_f32_16x16x32_bf16 v[36:39], a[72:75], a[136:139], v[36:39]\n"
"s_add_u32 m0, s66, 24576\n"
"s_add_u32 s62, s60, 0\n"
"v_mfma_f32_16x16x32_bf16 v[148:151], a[76:79], a[136:139], v[148:151]\n"
"s_waitcnt lgkmcnt(0)\n"
"buffer_load_dwordx4 v243, s[24:27], s62 offen lds\n"
"v_mfma_f32_16x16x32_bf16 v[40:43], a[80:83], a[136:139], v[40:43]\n"
"v_mfma_f32_16x16x32_bf16 v[152:155], a[84:87], a[136:139], v[152:155]\n"
"s_add_u32 m0, s66, 26624\n"
"s_add_u32 s62, s60, 65536\n"
"v_mfma_f32_16x16x32_bf16 v[44:47], a[88:91], a[136:139], v[44:47]\n"
"buffer_load_dwordx4 v243, s[24:27], s62 offen lds\n"
"v_mfma_f32_16x16x32_bf16 v[156:159], a[92:95], a[136:139], v[156:159]\n"
"s_waitcnt vmcnt(12)\n"
"ds_read_b64_tr_b16 a[98:99], v247 offset:9216\n"
"v_mfma_f32_16x16x32_bf16 v[48:51], a[64:67], a[140:143], v[48:51]\n"
"s_add_u32 m0, s66, 28672\n"
"s_add_u32 s62, s60, 131072\n"
"ds_read_b64_tr_b16 a[102:103], v247 offset:9224\n"
"v_mfma_f32_16x16x32_bf16 v[160:163], a[68:71], a[140:143], v[160:163]\n"
"buffer_load_dwordx4 v243, s[24:27], s62 offen lds\n"
"v_mfma_f32_16x16x32_bf16 v[52:55], a[72:75], a[140:143], v[52:55]\n"
"ds_read_b64_tr_b16 a[106:107], v247 offset:9472\n"
"v_mfma_f32_16x16x32_bf16 v[164:167], a[76:79], a[140:143], v[164:167]\n"
"s_add_u32 m0, s66, 30720\n"
"s_add_u32 s62, s60, 196608\n"
"ds_read_b64_tr_b16 a[110:111], v247 offset:9480\n"
"v_mfma_f32_16x16x32_bf16 v[56:59], a[80:83], a[140:143], v[56:59]\n"
"buffer_load_dwordx4 v243, s[24:27], s62 offen lds\n"
"v_mfma_f32_16x16x32_bf16 v[168:171], a[84:87], a[140:143], v[168:171]\n"
"ds_read_b64_tr_b16 a[114:115], v247 offset:9728\n"
"v_mfma_f32_16x16x32_bf16 v[60:63], a[88:91], a[140:143], v[60:63]\n"
"ds_read_b64_tr_b16 a[118:119], v247 offset:9736\n"
"v_mfma_f32_16x16x32_bf16 v[172:175], a[92:95], a[140:143], v[172:175]\n"
"ds_read_b64_tr_b16 a[122:123], v247 offset:9984\n"
"v_mfma_f32_16x16x32_bf16 v[64:67], a[64:67], a[144:147], v[64:67]\n"
"ds_read_b64_tr_b16 a[126:127], v247 offset:9992\n"
"v_mfma_f32_16x16x32_bf16 v[176:179], a[68:71], a[144:147], v[176:179]\n"
"v_mfma_f32_16x16x32_bf16 v[68:71], a[72:75], a[144:147], v[68:71]\n"
"v_mfma_f32_16x16x32_bf16 v[180:183], a[76:79], a[144:147], v[180:183]\n"
"v_mfma_f32_16x16x32_bf16 v[72:75], a[80:83], a[144:147], v[72:75]\n"
"v_mfma_f32_16x16x32_bf16 v[184:187], a[84:87], a[144:147], v[184:187]\n"
"v_mfma_f32_16x16x32_bf16 v[76:79], a[88:91], a[144:147], v[76:79]\n"
"s_add_u32 m0, s66, 25600\n"
"s_add_u32 s62, s60, 32768\n"
"v_mfma_f32_16x16x32_bf16 v[188:191], a[92:95], a[144:147], v[188:191]\n"
"s_waitcnt lgkmcnt(0)\n"
"buffer_load_dwordx4 v243, s[24:27], s62 offen lds\n"
"v_mfma_f32_16x16x32_bf16 v[80:83], a[64:67], a[148:151], v[80:83]\n"
"v_mfma_f32_16x16x32_bf16 v[192:195], a[68:71], a[148:151], v[192:195]\n"
"s_add_u32 m0, s66, 27648\n"
"s_add_u32 s62, s60, 98304\n"
"v_mfma_f32_16x16x32_bf16 v[84:87], a[72:75], a[148:151], v[84:87]\n"
"buffer_load_dwordx4 v243, s[24:27], s62 offen lds\n"
"v_mfma_f32_16x16x32_bf16 v[196:199], a[76:79], a[148:151], v[196:199]\n"
"v_mfma_f32_16x16x32_bf16 v[88:91], a[80:83], a[148:151], v[88:91]\n"
"s_add_u32 m0, s66, 29696\n"
"s_add_u32 s62, s60, 163840\n"
"v_mfma_f32_16x16x32_bf16 v[200:203], a[84:87], a[148:151], v[200:203]\n"
"buffer_load_dwordx4 v243, s[24:27], s62 offen lds\n"
"v_mfma_f32_16x16x32_bf16 v[92:95], a[88:91], a[148:151], v[92:95]\n"
"s_mov_b32 s59, Lh8-Lal\n"
"s_cmp_ge_u32 s36, 1\n"
"s_cselect_b32 s59, Lh9-Lal, s59\n"
"s_cmp_ge_u32 s36, 2\n"
"s_cselect_b32 s59, Lh10-Lal, s59\n"
"s_cmp_ge_u32 s36, 3\n"
"s_cselect_b32 s59, Lh11-Lal, s59\n"
"s_add_u32 s94, s92, s59\n"
"s_addc_u32 s95, s93, 0\n"
"s_mov_b64 exec, 0\n"
"s_setpc_b64 s[94:95]\n"
"Lw2:\n"
"s_mov_b64 exec, -1\n"
"v_mfma_f32_16x16x32_bf16 v[204:207], a[92:95], a[148:151], v[204:207]\n"
"s_add_u32 m0, s66, 31744\n"
"s_add_u32 s62, s60, 229376\n"
"v_mfma_f32_16x16x32_bf16 v[96:99], a[64:67], a[152:155], v[96:99]\n"
"buffer_load_dwordx4 v243, s[24:27], s62 offen lds\n"
"v_mfma_f32_16x16x32_bf16 v[208:211], a[68:71], a[152:155], v[208:211]\n"
"s_add_u32 s60, s60, 262144\n"
"s_waitcnt vmcnt(12)\n"
"ds_read_b64_tr_b16 a[0:1], v247 offset:0\n"
"v_mfma_f32_16x16x32_bf16 v[100:103], a[72:75], a[152:155], v[100:103]\n"
"s_cmp_ge_u32 s60, 4718592\n"
"s_cselect_b32 s63, 4718592, 0\n"
"s_barrier\n"
"v_mfma_f32_16x16x32_bf16 v[212:215], a[76:79], a[152:155], v[212:215]\n"
"s_sub_u32 s60, s60, s63\n"
"ds_read_b64_tr_b16 a[4:5], v247 offset:8\n"
"v_mfma_f32_16x16x32_bf16 v[104:107], a[80:83], a[152:155], v[104:107]\n"
"s_add_u32 m0, s66, 36864\n"
"s_add_u32 s62, s61, 0\n"
"ds_read_b64_tr_b16 a[8:9], v247 offset:256\n"
"v_mfma_f32_16x16x32_bf16 v[216:219], a[84:87], a[152:155], v[216:219]\n"
"buffer_load_dwordx4 v244, s[28:31], s62 offen lds\n"
"v_mfma_f32_16x16x32_bf16 v[108:111], a[88:91], a[152:155], v[108:111]\n"
"ds_read_b64_tr_b16 a[12:13], v247 offset:264\n"
"v_mfma_f32_16x16x32_bf16 v[220:223], a[92:95], a[152:155], v[220:223]\n"
"s_add_u32 m0, s66, 37888\n"
"s_add_u32 s62, s61, 36864\n"
"ds_read_b64_tr_b16 a[16:17], v247 offset:512\n"
"v_mfma_f32_16x16x32_bf16 v[0:3], a[96:99], a[156:159], v[0:3]\n"
"buffer_load_dwordx4 v244, s[28:31], s62 offen lds\n"
"v_mfma_f32_16x16x32_bf16 v[112:115], a[100:103], a[156:159], v[112:115]\n"
"ds_read_b64_tr_b16 a[20:21], v247 offset:520\n"
"v_mfma_f32_16x16x32_bf16 v[4:7], a[104:107], a[156:159], v[4:7]\n"
"ds_read_b128 a[128:131], v248 offset:0\n"
"v_mfma_f32_16x16x32_bf16 v[116:119], a[108:111], a[156:159], v[116:119]\n"
"ds_read_b64_tr_b16 a[24:25], v247 offset:768\n"
"v_mfma_f32_16x16x32_bf16 v[8:11], a[112:115], a[156:159], v[8:11]\n"
"ds_read_b64_tr_b16 a[28:29], v247 offset:776\n"
"v_mfma_f32_16x16x32_bf16 v[120:123], a[116:119], a[156:159], v[120:123]\n"
"ds_read_b128 a[132:135], v226 offset:0\n"
"v_mfma_f32_16x16x32_bf16 v[12:15], a[120:123], a[156:159], v[12:15]\n"
"v_mfma_f32_16x16x32_bf16 v[124:127], a[124:127], a[156:159], v[124:127]\n"
"s_waitcnt vmcnt(10)\n"
"ds_read_b64_tr_b16 a[2:3], v247 offset:1024\n"
"v_mfma_f32_16x16x32_bf16 v[16:19], a[96:99], a[160:163], v[16:19]\n"
"ds_read_b128 a[136:139], v248 offset:1024\n"
"v_mfma_f32_16x16x32_bf16 v[128:131], a[100:103], a[160:163], v[128:131]\n"
"ds_read_b64_tr_b16 a[6:7], v247 offset:1032\n"
"v_mfma_f32_16x16x32_bf16 v[20:23], a[104:107], a[160:163], v[20:23]\n"
"ds_read_b64_tr_b16 a[10:11], v247 offset:1280\n"
"v_mfma_f32_16x16x32_bf16 v[132:135], a[108:111], a[160:163], v[132:135]\n"
"s_add_u32 m0, s66, 16384\n"
"s_add_u32 s62, s60, 0\n"
"ds_read_b128 a[140:143], v226 offset:1024\n"
"v_mfma_f32_16x16x32_bf16 v[24:27], a[112:115], a[160:163], v[24:27]\n"
"s_waitcnt lgkmcnt(6)\n"
"buffer_load_dwordx4 v243, s[24:27], s62 offen lds\n"
"v_mfma_f32_16x16x32_bf16 v[136:139], a[116:119], a[160:163], v[136:139]\n"
"ds_read_b64_tr_b16 a[14:15], v247 offset:1288\n"
"v_mfma_f32_16x16x32_bf16 v[28:31], a[120:123], a[160:163], v[28:31]\n"
"s_add_u32 m0, s66, 18432\n"
"s_add_u32 s62, s60, 65536\n"
"ds_read_b128 a[144:147], v248 offset:2048\n"
"v_mfma_f32_16x16x32_bf16 v[140:143], a[124:127], a[160:163], v[140:143]\n"
"buffer_load_dwordx4 v243, s[24:27], s62 offen lds\n"
"v_mfma_f32_16x16x32_bf16 v[32:35], a[96:99], a[164:167], v[32:35]\n"
"ds_read_b64_tr_b16 a[18:19], v247 offset:1536\n"
"v_mfma_f32_16x16x32_bf16 v[144:147], a[100:103], a[164:167], v[144:147]\n"
"s_add_u32 m0, s66, 20480\n"
"s_add_u32 s62, s60, 131072\n"
"ds_read_b128 a[148:151], v226 offset:2048\n"
"v_mfma_f32_16x16x32_bf16 v[36:39], a[104:107], a[164:167], v[36:39]\n"
"buffer_load_dwordx4 v243, s[24:27], s62 offen lds\n"
"v_mfma_f32_16x16x32_bf16 v[148:151], a[108:111], a[164:167], v[148:151]\n"
"ds_read_b64_tr_b16 a[22:23], v247 offset:1544\n"
"v_mfma_f32_16x16x32_bf16 v[40:43], a[112:115], a[164:167], v[40:43]\n"
"s_add_u32 m0, s66, 22528\n"
"s_add_u32 s62, s60, 196608\n"
"ds_read_b128 a[152:155], v248 offset:3072\n"
"v_mfma_f32_16x16x32_bf16 v[152:155], a[116:119], a[164:167], v[152:155]\n"
"buffer_load_dwordx4 v243, s[24:27], s62 offen lds\n"
"v_mfma_f32_16x16x32_bf16 v[44:47], a[120:123], a[164:167], v[44:47]\n"
"ds_read_b64_tr_b16 a[26:27], v247 offset:1792\n"
"v_mfma_f32_16x16x32_bf16 v[156:159], a[124:127], a[164:167], v[156:159]\n"
"ds_read_b64_tr_b16 a[30:31], v247 offset:1800\n"
"v_mfma_f32_16x16x32_bf16 v[48:51], a[96:99], a[168:171], v[48:51]\n"
"ds_read_b128 a[156:159], v225 offset:0\n"
"v_mfma_f32_16x16x32_bf16 v[160:163], a[100:103], a[168:171], v[160:163]\n"
"ds_read_b128 a[160:163], v227 offset:0\n"
"v_mfma_f32_16x16x32_bf16 v[52:55], a[104:107], a[168:171], v[52:55]\n"
"s_add_u32 m0, s66, 38912\n"
"s_add_u32 s62, s61, 73728\n"
"ds_read_b128 a[164:167], v225 offset:1024\n"
"v_mfma_f32_16x16x32_bf16 v[164:167], a[108:111], a[168:171], v[164:167]\n"
"buffer_load_dwordx4 v244, s[28:31], s62 offen lds\n"
"v_mfma_f32_16x16x32_bf16 v[56:59], a[112:115], a[168:171], v[56:59]\n"
"s_waitcnt vmcnt(11)\n"
"ds_read_b64_tr_b16 a[32:33], v247 offset:8192\n"
"v_mfma_f32_16x16x32_bf16 v[168:171], a[116:119], a[168:171], v[168:171]\n"
"s_add_u32 m0, s66, 39936\n"
"s_add_u32 s62, s61, 110592\n"
"ds_read_b64_tr_b16 a[36:37], v247 offset:8200\n"
"v_mfma_f32_16x16x32_bf16 v[60:63], a[120:123], a[168:171], v[60:63]\n"
"buffer_load_dwordx4 v224, s[28:31], s62 offen lds\n"
"v_mfma_f32_16x16x32_bf16 v[172:175], a[124:127], a[168:171], v[172:175]\n"
"s_add_u32 s61, s61, 128\n"
"ds_read_b64_tr_b16 a[40:41], v247 offset:8448\n"
"v_mfma_f32_16x16x32_bf16 v[64:67], a[96:99], a[172:175], v[64:67]\n"
"s_cmp_ge_u32 s61, 1152\n"
"s_cselect_b32 s63, 1152, 0\n"
"ds_read_b64_tr_b16 a[44:45], v247 offset:8456\n"
"v_mfma_f32_16x16x32_bf16 v[176:179], a[100:103], a[172:175], v[176:179]\n"
"s_sub_u32 s61, s61, s63\n"
"ds_read_b128 a[168:171], v227 offset:1024\n"
"v_mfma_f32_16x16x32_bf16 v[68:71], a[104:107], a[172:175], v[68:71]\n"
"s_add_u32 m0, s66, 17408\n"
"s_add_u32 s62, s60, 32768\n"
"ds_read_b64_tr_b16 a[48:49], v247 offset:8704\n"
"v_mfma_f32_16x16x32_bf16 v[180:183], a[108:111], a[172:175], v[180:183]\n"
"s_waitcnt lgkmcnt(9)\n"
"buffer_load_dwordx4 v243, s[24:27], s62 offen lds\n"
"v_mfma_f32_16x16x32_bf16 v[72:75], a[112:115], a[172:175], v[72:75]\n"
"ds_read_b64_tr_b16 a[52:53], v247 offset:8712\n"
"v_mfma_f32_16x16x32_bf16 v[184:187], a[116:119], a[172:175], v[184:187]\n"
"s_add_u32 m0, s66, 19456\n"
"s_add_u32 s62, s60, 98304\n"
"ds_read_b64_tr_b16 a[56:57], v247 offset:8960\n"
"v_mfma_f32_16x16x32_bf16 v[76:79], a[120:123], a[172:175], v[76:79]\n"
"buffer_load_dwordx4 v243, s[24:27], s62 offen lds\n"
"v_mfma_f32_16x16x32_bf16 v[188:191], a[124:127], a[172:175], v[188:191]\n"
"ds_read_b64_tr_b16 a[60:61], v247 offset:8968\n"
"v_mfma_f32_16x16x32_bf16 v[80:83], a[96:99], a[176:179], v[80:83]\n"
"s_add_u32 m0, s66, 21504\n"
"s_add_u32 s62, s60, 163840\n"
"ds_read_b128 a[172:175], v225 offset:2048\n"
"v_mfma_f32_16x16x32_bf16 v[192:195], a[100:103], a[176:179], v[192:195]\n"
"buffer_load_dwordx4 v243, s[24:27], s62 offen lds\n"
"v_mfma_f32_16x16x32_bf16 v[84:87], a[104:107], a[176:179], v[84:87]\n"
"s_waitcnt vmcnt(11)\n"
"ds_read_b64_tr_b16 a[34:35], v247 offset:9216\n"
"v_mfma_f32_16x16x32_bf16 v[196:199], a[108:111], a[176:179], v[196:199]\n"
"s_add_u32 m0, s66, 23552\n"
"s_add_u32 s62, s60, 229376\n"
"ds_read_b64_tr_b16 a[38:39], v247 offset:9224\n"
"v_mfma_f32_16x16x32_bf16 v[88:91], a[112:115], a[176:179], v[88:91]\n"
"buffer_load_dwordx4 v243, s[24:27], s62 offen lds\n"
"v_mfma_f32_16x16x32_bf16 v[200:203], a[116:119], a[176:179], v[200:203]\n"
"s_add_u32 s60, s60, 262144\n"
"ds_read_b64_tr_b16 a[42:43], v247 offset:9472\n"
"v_mfma_f32_16x16x32_bf16 v[92:95], a[120:123], a[176:179], v[92:95]\n"
"s_cmp_ge_u32 s60, 4718592\n"
"s_cselect_b32 s63, 4718592, 0\n"
"ds_read_b64_tr_b16 a[46:47], v247 offset:9480\n"
"v_mfma_f32_16x16x32_bf16 v[204:207], a[124:127], a[176:179], v[204:207]\n"
"s_sub_u32 s60, s60, s63\n"
"ds_read_b64_tr_b16 a[50:51], v247 offset:9728\n"
"v_mfma_f32_16x16x32_bf16 v[96:99], a[96:99], a[180:183], v[96:99]\n"
"ds_read_b64_tr_b16 a[54:55], v247 offset:9736\n"
"v_mfma_f32_16x16x32_bf16 v[208:211], a[100:103], a[180:183], v[208:211]\n"
"s_add_u32 m0, s66, 24576\n"
"s_add_u32 s62, s60, 0\n"
"ds_read_b128 a[176:179], v227 offset:2048\n"
"v_mfma_f32_16x16x32_bf16 v[100:103], a[104:107], a[180:183], v[100:103]\n"
"s_waitcnt lgkmcnt(8)\n"
"buffer_load_dwordx4 v243, s[24:27], s62 offen lds\n"
"v_mfma_f32_16x16x32_bf16 v[212:215], a[108:111], a[180:183], v[212:215]\n"
"ds_read_b64_tr_b16 a[58:59], v247 offset:9984\n"
"v_mfma_f32_16x16x32_bf16 v[104:107], a[112:115], a[180:183], v[104:107]\n"
"s_add_u32 m0, s66, 26624\n"
"s_add_u32 s62, s60, 65536\n"
"ds_read_b64_tr_b16 a[62:63], v247 offset:9992\n"
"v_mfma_f32_16x16x32_bf16 v[216:219], a[116:119], a[180:183], v[216:219]\n"
"buffer_load_dwordx4 v243, s[24:27], s62 offen lds\n"
"v_mfma_f32_16x16x32_bf16 v[108:111], a[120:123], a[180:183], v[108:111]\n"
"s_mov_b32 s59, Lh12-Lal\n"
"s_cmp_ge_u32 s36, 1\n"
"s_cselect_b32 s59, Lh13-Lal, s59\n"
"s_add_u32 s94, s92, s59\n"
"s_addc_u32 s95, s93, 0\n"
"s_mov_b64 exec, 0\n"
"s_setpc_b64 s[94:95]\n"
"Lw3:\n"
"s_mov_b64 exec, -1\n"
"v_mfma_f32_16x16x32_bf16 v[220:223], a[124:127], a[180:183], v[220:223]\n"
"; qproj iteration 2\n"
"s_waitcnt lgkmcnt(0)\n"
"v_mfma_f32_16x16x32_bf16 v[0:3], a[0:3], a[128:131], v[0:3]\n"
"s_add_u32 m0, s66, 28672\n"
"s_add_u32 s62, s60, 131072\n"
"ds_read_b128 a[180:183], v225 offset:3072\n"
"v_mfma_f32_16x16x32_bf16 v[0:3], a[32:35], a[156:159], v[0:3]\n"
"buffer_load_dwordx4 v243, s[24:27], s62 offen lds\n"
"v_mfma_f32_16x16x32_bf16 v[112:115], a[4:7], a[128:131], v[112:115]\n"
"v_mfma_f32_16x16x32_bf16 v[112:115], a[36:39], a[156:159], v[112:115]\n"
"s_add_u32 m0, s66, 30720\n"
"s_add_u32 s62, s60, 196608\n"
"v_mfma_f32_16x16x32_bf16 v[4:7], a[8:11], a[128:131], v[4:7]\n"
"buffer_load_dwordx4 v243, s[24:27], s62 offen lds\n"
"v_mfma_f32_16x16x32_bf16 v[4:7], a[40:43], a[156:159], v[4:7]\n"
"v_mfma_f32_16x16x32_bf16 v[116:119], a[12:15], a[128:131], v[116:119]\n"
"s_add_u32 m0, s66, 25600\n"
"s_add_u32 s62, s60, 32768\n"
"v_mfma_f32_16x16x32_bf16 v[116:119], a[44:47], a[156:159], v[116:119]\n"
"buffer_load_dwordx4 v243, s[24:27], s62 offen lds\n"
"v_mfma_f32_16x16x32_bf16 v[8:11], a[16:19], a[128:131], v[8:11]\n"
"v_mfma_f32_16x16x32_bf16 v[8:11], a[48:51], a[156:159], v[8:11]\n"
"s_add_u32 m0, s66, 27648\n"
"s_add_u32 s62, s60, 98304\n"
"v_mfma_f32_16x16x32_bf16 v[120:123], a[20:23], a[128:131], v[120:123]\n"
"buffer_load_dwordx4 v243, s[24:27], s62 offen lds\n"
"v_mfma_f32_16x16x32_bf16 v[120:123], a[52:55], a[156:159], v[120:123]\n"
"v_mfma_f32_16x16x32_bf16 v[12:15], a[24:27], a[128:131], v[12:15]\n"
"s_add_u32 m0, s66, 29696\n"
"s_add_u32 s62, s60, 163840\n"
"v_mfma_f32_16x16x32_bf16 v[12:15], a[56:59], a[156:159], v[12:15]\n"
"buffer_load_dwordx4 v243, s[24:27], s62 offen lds\n"
"v_mfma_f32_16x16x32_bf16 v[124:127], a[28:31], a[128:131], v[124:127]\n"
"s_waitcnt vmcnt(11) lgkmcnt(0)\n"
"s_barrier\n"
"v_mfma_f32_16x16x32_bf16 v[124:127], a[60:63], a[156:159], v[124:127]\n"
"v_mfma_f32_16x16x32_bf16 v[16:19], a[0:3], a[132:135], v[16:19]\n"
"s_add_u32 m0, s66, 31744\n"
"s_add_u32 s62, s60, 229376\n"
"ds_read_b128 a[128:131], v248 offset:4096\n"
"v_mfma_f32_16x16x32_bf16 v[16:19], a[32:35], a[160:163], v[16:19]\n"
"buffer_load_dwordx4 v243, s[24:27], s62 offen lds\n"
"v_mfma_f32_16x16x32_bf16 v[128:131], a[4:7], a[132:135], v[128:131]\n"
"s_add_u32 s60, s60, 262144\n"
"ds_read_b128 a[156:159], v225 offset:4096\n"
"v_mfma_f32_16x16x32_bf16 v[128:131], a[36:39], a[160:163], v[128:131]\n"
"s_cmp_ge_u32 s60, 4718592\n"
"s_cselect_b32 s63, 4718592, 0\n"
"v_mfma_f32_16x16x32_bf16 v[20:23], a[8:11], a[132:135], v[20:23]\n"
"s_sub_u32 s60, s60, s63\n"
"v_mfma_f32_16x16x32_bf16 v[20:23], a[40:43], a[160:163], v[20:23]\n"
"s_add_u32 m0, s66, 32768\n"
"s_add_u32 s62, s61, 0\n"
"v_mfma_f32_16x16x32_bf16 v[132:135], a[12:15], a[132:135], v[132:135]\n"
"buffer_load_dwordx4 v244, s[28:31], s62 offen lds\n"
"v_mfma_f32_16x16x32_bf16 v[132:135], a[44:47], a[160:163], v[132:135]\n"
"ds_read_b64_tr_b16 a[64:65], v247 offset:0\n"
"v_mfma_f32_16x16x32_bf16 v[24:27], a[16:19], a[132:135], v[24:27]\n"
"s_add_u32 m0, s66, 33792\n"
"s_add_u32 s62, s61, 36864\n"
"ds_read_b64_tr_b16 a[68:69], v247 offset:8\n"
"v_mfma_f32_16x16x32_bf16 v[24:27], a[48:51], a[160:163], v[24:27]\n"
"buffer_load_dwordx4 v244, s[28:31], s62 offen lds\n"
"v_mfma_f32_16x16x32_bf16 v[136:139], a[20:23], a[132:135], v[136:139]\n"
"ds_read_b64_tr_b16 a[72:73], v247 offset:256\n"
"v_mfma_f32_16x16x32_bf16 v[136:139], a[52:55], a[160:163], v[136:139]\n"
"ds_read_b64_tr_b16 a[76:77], v247 offset:264\n"
"v_mfma_f32_16x16x32_bf16 v[28:31], a[24:27], a[132:135], v[28:31]\n"
"ds_read_b64_tr_b16 a[80:81], v247 offset:512\n"
"v_mfma_f32_16x16x32_bf16 v[28:31], a[56:59], a[160:163], v[28:31]\n"
"ds_read_b64_tr_b16 a[84:85], v247 offset:520\n"
"v_mfma_f32_16x16x32_bf16 v[140:143], a[28:31], a[132:135], v[140:143]\n"
"ds_read_b64_tr_b16 a[88:89], v247 offset:768\n"
"v_mfma_f32_16x16x32_bf16 v[140:143], a[60:63], a[160:163], v[140:143]\n"
"ds_read_b64_tr_b16 a[92:93], v247 offset:776\n"
"v_mfma_f32_16x16x32_bf16 v[32:35], a[0:3], a[136:139], v[32:35]\n"
"ds_read_b128 a[132:135], v226 offset:4096\n"
"v_mfma_f32_16x16x32_bf16 v[32:35], a[32:35], a[164:167], v[32:35]\n"
"v_mfma_f32_16x16x32_bf16 v[144:147], a[4:7], a[136:139], v[144:147]\n"
"ds_read_b128 a[160:163], v227 offset:4096\n"
"v_mfma_f32_16x16x32_bf16 v[144:147], a[36:39], a[164:167], v[144:147]\n"
"v_mfma_f32_16x16x32_bf16 v[36:39], a[8:11], a[136:139], v[36:39]\n"
"v_mfma_f32_16x16x32_bf16 v[36:39], a[40:43], a[164:167], v[36:39]\n"
"s_add_u32 m0, s66, 16384\n"
"s_add_u32 s62, s60, 0\n"
"v_mfma_f32_16x16x32_bf16 v[148:151], a[12:15], a[136:139], v[148:151]\n"
"s_waitcnt lgkmcnt(2)\n"
"buffer_load_dwordx4 v243, s[24:27], s62 offen lds\n"
"v_mfma_f32_16x16x32_bf16 v[148:151], a[44:47], a[164:167], v[148:151]\n"
"v_mfma_f32_16x16x32_bf16 v[40:43], a[16:19], a[136:139], v[40:43]\n"
"s_add_u32 m0, s66, 18432\n"
"s_add_u32 s62, s60, 65536\n"
"v_mfma_f32_16x16x32_bf16 v[40:43], a[48:51], a[164:167], v[40:43]\n"
"buffer_load_dwordx4 v243, s[24:27], s62 offen lds\n"
"v_mfma_f32_16x16x32_bf16 v[152:155], a[20:23], a[136:139], v[152:155]\n"
"v_mfma_f32_16x16x32_bf16 v[152:155], a[52:55], a[164:167], v[152:155]\n"
"s_add_u32 m0, s66, 20480\n"
"s_add_u32 s62, s60, 131072\n"
"v_mfma_f32_16x16x32_bf16 v[44:47], a[24:27], a[136:139], v[44:47]\n"
"buffer_load_dwordx4 v243, s[24:27], s62 offen lds\n"
"v_mfma_f32_16x16x32_bf16 v[44:47], a[56:59], a[164:167], v[44:47]\n"
"v_mfma_f32_16x16x32_bf16 v[156:159], a[28:31], a[136:139], v[156:159]\n"
"s_add_u32 m0, s66, 22528\n"
"s_add_u32 s62, s60, 196608\n"
"s_waitcnt vmcnt(13)\n"
"ds_read_b64_tr_b16 a[66:67], v247 offset:1024\n"
"v_mfma_f32_16x16x32_bf16 v[156:159], a[60:63], a[164:167], v[156:159]\n"
"buffer_load_dwordx4 v243, s[24:27], s62 offen lds\n"
"v_mfma_f32_16x16x32_bf16 v[48:51], a[0:3], a[140:143], v[48:51]\n"
"ds_read_b128 a[136:139], v248 offset:5120\n"
"v_mfma_f32_16x16x32_bf16 v[48:51], a[32:35], a[168:171], v[48:51]\n"
"ds_read_b64_tr_b16 a[70:71], v247 offset:1032\n"
"v_mfma_f32_16x16x32_bf16 v[160:163], a[4:7], a[140:143], v[160:163]\n"
"s_add_u32 m0, s66, 34816\n"
"s_add_u32 s62, s61, 73728\n"
"ds_read_b128 a[164:167], v225 offset:5120\n"
"v_mfma_f32_16x16x32_bf16 v[160:163], a[36:39], a[168:171], v[160:163]\n"
"buffer_load_dwordx4 v244, s[28:31], s62 offen lds\n"
"v_mfma_f32_16x16x32_bf16 v[52:55], a[8:11], a[140:143], v[52:55]\n"
"ds_read_b64_tr_b16 a[74:75], v247 offset:1280\n"
"v_mfma_f32_16x16x32_bf16 v[52:55], a[40:43], a[168:171], v[52:55]\n"
"s_add_u32 m0, s66, 35840\n"
"s_add_u32 s62, s61, 110592\n"
"ds_read_b64_tr_b16 a[78:79], v247 offset:1288\n"
"v_mfma_f32_16x16x32_bf16 v[164:167], a[12:15], a[140:143], v[164:167]\n"
"buffer_load_dwordx4 v224, s[28:31], s62 offen lds\n"
"v_mfma_f32_16x16x32_bf16 v[164:167], a[44:47], a[168:171], v[164:167]\n"
"s_add_u32 s61, s61, 128\n"
"ds_read_b64_tr_b16 a[82:83], v247 offset:1536\n"
"v_mfma_f32_16x16x32_bf16 v[56:59], a[16:19], a[140:143], v[56:59]\n"
"s_cmp_ge_u32 s61, 1152\n"
"s_cselect_b32 s63, 1152, 0\n"
"ds_read_b64_tr_b16 a[86:87], v247 offset:1544\n"
"v_mfma_f32_16x16x32_bf16 v[56:59], a[48:51], a[168:171], v[56:59]\n"
"s_sub_u32 s61, s61, s63\n"
"ds_read_b64_tr_b16 a[90:91], v247 offset:1792\n"
"v_mfma_f32_16x16x32_bf16 v[168:171], a[20:23], a[140:143], v[168:171]\n"
"ds_read_b64_tr_b16 a[94:95], v247 offset:1800\n"
"v_mfma_f32_16x16x32_bf16 v[168:171], a[52:55], a[168:171], v[168:171]\n"
"v_mfma_f32_16x16x32_bf16 v[60:63], a[24:27], a[140:143], v[60:63]\n"
"v_mfma_f32_16x16x32_bf16 v[60:63], a[56:59], a[168:171], v[60:63]\n"
"v_mfma_f32_16x16x32_bf16 v[172:175], a[28:31], a[140:143], v[172:175]\n"
"s_waitcnt vmcnt(12)\n"
"ds_read_b64_tr_b16 a[96:97], v247 offset:8192\n"
"v_mfma_f32_16x16x32_bf16 v[172:175], a[60:63], a[168:171], v[172:175]\n"
"ds_read_b64_tr_b16 a[100:101], v247 offset:8200\n"
"v_mfma_f32_16x16x32_bf16 v[64:67], a[0:3], a[144:147], v[64:67]\n"
"s_add_u32 m0, s66, 17408\n"
"s_add_u32 s62, s60, 32768\n"
"ds_read_b128 a[140:143], v226 offset:5120\n"
"v_mfma_f32_16x16x32_bf16 v[64:67], a[32:35], a[172:175], v[64:67]\n"
"s_waitcnt lgkmcnt(3)\n"
"buffer_load_dwordx4 v243, s[24:27], s62 offen lds\n"
"v_mfma_f32_16x16x32_bf16 v[176:179], a[4:7], a[144:147], v[176:179]\n"
"ds_read_b128 a[168:171], v227 offset:5120\n"
"v_mfma_f32_16x16x32_bf16 v[176:179], a[36:39], a[172:175], v[176:179]\n"
"s_add_u32 m0, s66, 19456\n"
"s_add_u32 s62, s60, 98304\n"
"ds_read_b64_tr_b16 a[104:105], v247 offset:8448\n"
"v_mfma_f32_16x16x32_bf16 v[68:71], a[8:11], a[144:147], v[68:71]\n"
"buffer_load_dwordx4 v243, s[24:27], s62 offen lds\n"
"v_mfma_f32_16x16x32_bf16 v[68:71], a[40:43], a[172:175], v[68:71]\n"
"ds_read_b64_tr_b16 a[108:109], v247 offset:8456\n"
"v_mfma_f32_16x16x32_bf16 v[180:183], a[12:15], a[144:147], v[180:183]\n"
"s_add_u32 m0, s66, 21504\n"
"s_add_u32 s62, s60, 163840\n"
"ds_read_b64_tr_b16 a[112:113], v247 offset:8704\n"
"v_mfma_f32_16x16x32_bf16 v[180:183], a[44:47], a[172:175], v[180:183]\n"
"buffer_load_dwordx4 v243, s[24:27], s62 offen lds\n"
"v_mfma_f32_16x16x32_bf16 v[72:75], a[16:19], a[144:147], v[72:75]\n"
"ds_read_b64_tr_b16 a[116:117], v247 offset:8712\n"
"v_mfma_f32_16x16x32_bf16 v[72:75], a[48:51], a[172:175], v[72:75]\n"
"s_add_u32 m0, s66, 23552\n"
"s_add_u32 s62, s60, 229376\n"
"ds_read_b64_tr_b16 a[120:121], v247 offset:8960\n"
"v_mfma_f32_16x16x32_bf16 v[184:187], a[20:23], a[144:147], v[184:187]\n"
"buffer_load_dwordx4 v243, s[24:27], s62 offen lds\n"
"v_mfma_f32_16x16x32_bf16 v[184:187], a[52:55], a[172:175], v[184:187]\n"
"s_add_u32 s60, s60, 262144\n"
"ds_read_b64_tr_b16 a[124:125], v247 offset:8968\n"
"v_mfma_f32_16x16x32_bf16 v[76:79], a[24:27], a[144:147], v[76:79]\n"
"s_cmp_ge_u32 s60, 4718592\n"
"s_cselect_b32 s63, 4718592, 0\n"
"v_mfma_f32_16x16x32_bf16 v[76:79], a[56:59], a[172:175], v[76:79]\n"
"s_sub_u32 s60, s60, s63\n"
"Lh1:\n"
"s_cbranch_execz Lw0\n"
"v_mfma_f32_16x16x32_bf16 v[188:191], a[28:31], a[144:147], v[188:191]\n"
"s_mov_b32 m0, s86\n"
"v_mfma_f32_16x16x32_bf16 v[188:191], a[60:63], a[172:175], v[188:191]\n"
"buffer_load_dwordx4 v245, s[80:83], s87 offen nt lds\n"
"v_mfma_f32_16x16x32_bf16 v[80:83], a[0:3], a[148:151], v[80:83]\n"
"ds_read_b128 a[144:147], v248 offset:6144\n"
"v_mfma_f32_16x16x32_bf16 v[80:83], a[32:35], a[176:179], v[80:83]\n"
"v_mfma_f32_16x16x32_bf16 v[192:195], a[4:7], a[148:151], v[192:195]\n"
"s_mov_b32 m0, s86\n"
"ds_read_b128 a[172:175], v225 offset:6144\n"
"v_mfma_f32_16x16x32_bf16 v[192:195], a[36:39], a[176:179], v[192:195]\n"
"buffer_load_dwordx4 v246, s[80:83], s87 offen offset:1024 lds\n"
"v_mfma_f32_16x16x32_bf16 v[84:87], a[8:11], a[148:151], v[84:87]\n"
"s_add_u32 s87, s87, 2048\n"
"s_and_b32 s87, s87, s98\n"
"s_waitcnt vmcnt(14)\n"
"ds_read_b64_tr_b16 a[98:99], v247 offset:9216\n"
"v_mfma_f32_16x16x32_bf16 v[84:87], a[40:43], a[176:179], v[84:87]\n"
"s_add_u32 s86, s86, 2048\n"
"ds_read_b64_tr_b16 a[102:103], v247 offset:9224\n"
"v_mfma_f32_16x16x32_bf16 v[196:199], a[12:15], a[148:151], v[196:199]\n"
"s_mov_b32 m0, s86\n"
"ds_read_b64_tr_b16 a[106:107], v247 offset:9472\n"
"v_mfma_f32_16x16x32_bf16 v[196:199], a[44:47], a[176:179], v[196:199]\n"
"buffer_load_dwordx4 v245, s[80:83], s87 offen nt lds\n"
"v_mfma_f32_16x16x32_bf16 v[88:91], a[16:19], a[148:151], v[88:91]\n"
"ds_read_b64_tr_b16 a[110:111], v247 offset:9480\n"
"v_mfma_f32_16x16x32_bf16 v[88:91], a[48:51], a[176:179], v[88:91]\n"
"s_mov_b32 m0, s86\n"
"ds_read_b64_tr_b16 a[114:115], v247 offset:9728\n"
"v_mfma_f32_16x16x32_bf16 v[200:203], a[20:23], a[148:151], v[200:203]\n"
"buffer_load_dwordx4 v246, s[80:83], s87 offen offset:1024 lds\n"
"v_mfma_f32_16x16x32_bf16 v[200:203], a[52:55], a[176:179], v[200:203]\n"
"s_add_u32 s87, s87, 2048\n"
"s_and_b32 s87, s87, s98\n"
"ds_read_b64_tr_b16 a[118:119], v247 offset:9736\n"
"v_mfma_f32_16x16x32_bf16 v[92:95], a[24:27], a[148:151], v[92:95]\n"
"s_add_u32 s86, s86, 2048\n"
"ds_read_b64_tr_b16 a[122:123], v247 offset:9984\n"
"v_mfma_f32_16x16x32_bf16 v[92:95], a[56:59], a[176:179], v[92:95]\n"
"ds_read_b64_tr_b16 a[126:127], v247 offset:9992\n"
"v_mfma_f32_16x16x32_bf16 v[204:207], a[28:31], a[148:151], v[204:207]\n"
"s_add_u32 m0, s66, 24576\n"
"s_add_u32 s62, s60, 0\n"
"v_mfma_f32_16x16x32_bf16 v[204:207], a[60:63], a[176:179], v[204:207]\n"
"s_waitcnt lgkmcnt(10)\n"
"buffer_load_dwordx4 v243, s[24:27], s62 offen lds\n"
"v_mfma_f32_16x16x32_bf16 v[96:99], a[0:3], a[152:155], v[96:99]\n"
"ds_read_b128 a[148:151], v226 offset:6144\n"
"v_mfma_f32_16x16x32_bf16 v[96:99], a[32:35], a[180:183], v[96:99]\n"
"v_mfma_f32_16x16x32_bf16 v[208:211], a[4:7], a[152:155], v[208:211]\n"
"s_add_u32 m0, s66, 26624\n"
"s_add_u32 s62, s60, 65536\n"
"ds_read_b128 a[176:179], v227 offset:6144\n"
"v_mfma_f32_16x16x32_bf16 v[208:211], a[36:39], a[180:183], v[208:211]\n"
"buffer_load_dwordx4 v243, s[24:27], s62 offen lds\n"
"v_mfma_f32_16x16x32_bf16 v[100:103], a[8:11], a[152:155], v[100:103]\n"
"v_mfma_f32_16x16x32_bf16 v[100:103], a[40:43], a[180:183], v[100:103]\n"
"s_add_u32 m0, s66, 28672\n"
"s_add_u32 s62, s60, 131072\n"
"v_mfma_f32_16x16x32_bf16 v[212:215], a[12:15], a[152:155], v[212:215]\n"
"buffer_load_dwordx4 v243, s[24:27], s62 offen lds\n"
"v_mfma_f32_16x16x32_bf16 v[212:215], a[44:47], a[180:183], v[212:215]\n"
"v_mfma_f32_16x16x32_bf16 v[104:107], a[16:19], a[152:155], v[104:107]\n"
"s_add_u32 m0, s66, 30720\n"
"s_add_u32 s62, s60, 196608\n"
"v_mfma_f32_16x16x32_bf16 v[104:107], a[48:51], a[180:183], v[104:107]\n"
"buffer_load_dwordx4 v243, s[24:27], s62 offen lds\n"
"v_mfma_f32_16x16x32_bf16 v[216:219], a[20:23], a[152:155], v[216:219]\n"
"v_mfma_f32_16x16x32_bf16 v[216:219], a[52:55], a[180:183], v[216:219]\n"
"v_mfma_f32_16x16x32_bf16 v[108:111], a[24:27], a[152:155], v[108:111]\n"
"v_mfma_f32_16x16x32_bf16 v[108:111], a[56:59], a[180:183], v[108:111]\n"
"s_add_u32 m0, s66, 25600\n"
"s_add_u32 s62, s60, 32768\n"
"v_mfma_f32_16x16x32_bf16 v[220:223], a[28:31], a[152:155], v[220:223]\n"
"s_waitcnt lgkmcnt(2)\n"
"buffer_load_dwordx4 v243, s[24:27], s62 offen lds\n"
"v_mfma_f32_16x16x32_bf16 v[220:223], a[60:63], a[180:183], v[220:223]\n"
"; qproj iteration 3\n"
"v_mfma_f32_16x16x32_bf16 v[0:3], a[64:67], a[128:131], v[0:3]\n"
"ds_read_b128 a[152:155], v248 offset:7168\n"
"v_mfma_f32_16x16x32_bf16 v[0:3], a[96:99], a[156:159], v[0:3]\n"
"s_waitcnt vmcnt(15)\n"
"ds_read_b64_tr_b16 a[0:1], v247 offset:0\n"
"v_mfma_f32_16x16x32_bf16 v[112:115], a[68:71], a[128:131], v[112:115]\n"
"s_add_u32 m0, s66, 27648\n"
"s_add_u32 s62, s60, 98304\n"
"ds_read_b128 a[180:183], v225 offset:7168\n"
"v_mfma_f32_16x16x32_bf16 v[112:115], a[100:103], a[156:159], v[112:115]\n"
"buffer_load_dwordx4 v243, s[24:27], s62 offen lds\n"
"v_mfma_f32_16x16x32_bf16 v[4:7], a[72:75], a[128:131], v[4:7]\n"
"ds_read_b64_tr_b16 a[4:5], v247 offset:8\n"
"v_mfma_f32_16x16x32_bf16 v[4:7], a[104:107], a[156:159], v[4:7]\n"
"ds_read_b64_tr_b16 a[8:9], v247 offset:256\n"
"v_mfma_f32_16x16x32_bf16 v[116:119], a[76:79], a[128:131], v[116:119]\n"
"ds_read_b64_tr_b16 a[12:13], v247 offset:264\n"
"v_mfma_f32_16x16x32_bf16 v[116:119], a[108:111], a[156:159], v[116:119]\n"
"s_add_u32 m0, s66, 29696\n"
"s_add_u32 s62, s60, 163840\n"
"ds_read_b64_tr_b16 a[16:17], v247 offset:512\n"
"v_mfma_f32_16x16x32_bf16 v[8:11], a[80:83], a[128:131], v[8:11]\n"
"buffer_load_dwordx4 v243, s[24:27], s62 offen lds\n"
"v_mfma_f32_16x16x32_bf16 v[8:11], a[112:115], a[156:159], v[8:11]\n"
"ds_read_b64_tr_b16 a[20:21], v247 offset:520\n"
"v_mfma_f32_16x16x32_bf16 v[120:123], a[84:87], a[128:131], v[120:123]\n"
"ds_read_b64_tr_b16 a[24:25], v247 offset:768\n"
"v_mfma_f32_16x16x32_bf16 v[120:123], a[116:119], a[156:159], v[120:123]\n"
"ds_read_b64_tr_b16 a[28:29], v247 offset:776\n"
"v_mfma_f32_16x16x32_bf16 v[12:15], a[88:91], a[128:131], v[12:15]\n"
"s_add_u32 m0, s66, 31744\n"
"s_add_u32 s62, s60, 229376\n"
"v_mfma_f32_16x16x32_bf16 v[12:15], a[120:123], a[156:159], v[12:15]\n"
"s_waitcnt lgkmcnt(10)\n"
"buffer_load_dwordx4 v243, s[24:27], s62 offen lds\n"
"v_mfma_f32_16x16x32_bf16 v[124:127], a[92:95], a[128:131], v[124:127]\n"
"s_add_u32 s60, s60, 262144\n"
"s_waitcnt vmcnt(16) lgkmcnt(7)\n"
"s_barrier\n"
"v_mfma_f32_16x16x32_bf16 v[124:127], a[124:127], a[156:159], v[124:127]\n"
"s_cmp_ge_u32 s60, 4718592\n"
"s_cselect_b32 s63, 4718592, 0\n"
"v_mfma_f32_16x16x32_bf16 v[16:19], a[64:67], a[132:135], v[16:19]\n"
"s_sub_u32 s60, s60, s63\n"
"ds_read_b128 a[128:131], v248 offset:0\n"
"v_mfma_f32_16x16x32_bf16 v[16:19], a[96:99], a[160:163], v[16:19]\n"
"v_mfma_f32_16x16x32_bf16 v[128:131], a[68:71], a[132:135], v[128:131]\n"
"ds_read_b128 a[156:159], v225 offset:0\n"
"v_mfma_f32_16x16x32_bf16 v[128:131], a[100:103], a[160:163], v[128:131]\n"
"s_add_u32 m0, s66, 36864\n"
"s_add_u32 s62, s61, 0\n"
"v_mfma_f32_16x16x32_bf16 v[20:23], a[72:75], a[132:135], v[20:23]\n"
"buffer_load_dwordx4 v244, s[28:31], s62 offen lds\n"
"v_mfma_f32_16x16x32_bf16 v[20:23], a[104:107], a[160:163], v[20:23]\n"
"v_mfma_f32_16x16x32_bf16 v[132:135], a[76:79], a[132:135], v[132:135]\n"
"v_mfma_f32_16x16x32_bf16 v[132:135], a[108:111], a[160:163], v[132:135]\n"
"v_mfma_f32_16x16x32_bf16 v[24:27], a[80:83], a[132:135], v[24:27]\n"
"s_add_u32 m0, s66, 37888\n"
"s_add_u32 s62, s61, 36864\n"
"v_mfma_f32_16x16x32_bf16 v[24:27], a[112:115], a[160:163], v[24:27]\n"
"buffer_load_dwordx4 v244, s[28:31], s62 offen lds\n"
"v_mfma_f32_16x16x32_bf16 v[136:139], a[84:87], a[132:135], v[136:139]\n"
"v_mfma_f32_16x16x32_bf16 v[136:139], a[116:119], a[160:163], v[136:139]\n"
"s_add_u32 m0, s66, 16384\n"
"s_add_u32 s62, s60, 0\n"
"v_mfma_f32_16x16x32_bf16 v[28:31], a[88:91], a[132:135], v[28:31]\n"
"s_waitcnt lgkmcnt(2)\n"
"buffer_load_dwordx4 v243, s[24:27], s62 offen lds\n"
"v_mfma_f32_16x16x32_bf16 v[28:31], a[120:123], a[160:163], v[28:31]\n"
"s_waitcnt vmcnt(15)\n"
"ds_read_b64_tr_b16 a[2:3], v247 offset:1024\n"
"v_mfma_f32_16x16x32_bf16 v[140:143], a[92:95], a[132:135], v[140:143]\n"
"s_add_u32 m0, s66, 18432\n"
"s_add_u32 s62, s60, 65536\n"
"ds_read_b64_tr_b16 a[6:7], v247 offset:1032\n"
"v_mfma_f32_16x16x32_bf16 v[140:143], a[124:127], a[160:163], v[140:143]\n"
"buffer_load_dwordx4 v243, s[24:27], s62 offen lds\n"
"v_mfma_f32_16x16x32_bf16 v[32:35], a[64:67], a[136:139], v[32:35]\n"
"ds_read_b128 a[132:135], v226 offset:0\n"
"v_mfma_f32_16x16x32_bf16 v[32:35], a[96:99], a[164:167], v[32:35]\n"
"ds_read_b64_tr_b16 a[10:11], v247 offset:1280\n"
"v_mfma_f32_16x16x32_bf16 v[144:147], a[68:71], a[136:139], v[144:147]\n"
"ds_read_b128 a[160:163], v227 offset:0\n"
"v_mfma_f32_16x16x32_bf16 v[144:147], a[100:103], a[164:167], v[144:147]\n"
"s_add_u32 m0, s66, 20480\n"
"s_add_u32 s62, s60, 131072\n"
"ds_read_b64_tr_b16 a[14:15], v247 offset:1288\n"
"v_mfma_f32_16x16x32_bf16 v[36:39], a[72:75], a[136:139], v[36:39]\n"
"buffer_load_dwordx4 v243, s[24:27], s62 offen lds\n"
"v_mfma_f32_16x16x32_bf16 v[36:39], a[104:107], a[164:167], v[36:39]\n"
"ds_read_b64_tr_b16 a[18:19], v247 offset:1536\n"
"v_mfma_f32_16x16x32_bf16 v[148:151], a[76:79], a[136:139], v[148:151]\n"
"ds_read_b64_tr_b16 a[22:23], v247 offset:1544\n"
"v_mfma_f32_16x16x32_bf16 v[148:151], a[108:111], a[164:167], v[148:151]\n"
"ds_read_b64_tr_b16 a[26:27], v247 offset:1792\n"
"v_mfma_f32_16x16x32_bf16 v[40:43], a[80:83], a[136:139], v[40:43]\n"
"s_add_u32 m0, s66, 22528\n"
"s_add_u32 s62, s60, 196608\n"
"ds_read_b64_tr_b16 a[30:31], v247 offset:1800\n"
"v_mfma_f32_16x16x32_bf16 v[40:43], a[112:115], a[164:167], v[40:43]\n"
"buffer_load_dwordx4 v243, s[24:27], s62 offen lds\n"
"v_mfma_f32_16x16x32_bf16 v[152:155], a[84:87], a[136:139], v[152:155]\n"
"v_mfma_f32_16x16x32_bf16 v[152:155], a[116:119], a[164:167], v[152:155]\n"
"v_mfma_f32_16x16x32_bf16 v[44:47], a[88:91], a[136:139], v[44:47]\n"
"v_mfma_f32_16x16x32_bf16 v[44:47], a[120:123], a[164:167], v[44:47]\n"
"s_add_u32 m0, s66, 38912\n"
"s_add_u32 s62, s61, 73728\n"
"v_mfma_f32_16x16x32_bf16 v[156:159], a[92:95], a[136:139], v[156:159]\n"
"buffer_load_dwordx4 v244, s[28:31], s62 offen lds\n"
"v_mfma_f32_16x16x32_bf16 v[156:159], a[124:127], a[164:167], v[156:159]\n"
"v_mfma_f32_16x16x32_bf16 v[48:51], a[64:67], a[140:143], v[48:51]\n"
"ds_read_b128 a[136:139], v248 offset:1024\n"
"v_mfma_f32_16x16x32_bf16 v[48:51], a[96:99], a[168:171], v[48:51]\n"
"v_mfma_f32_16x16x32_bf16 v[160:163], a[68:71], a[140:143], v[160:163]\n"
"s_add_u32 m0, s66, 39936\n"
"s_add_u32 s62, s61, 110592\n"
"ds_read_b128 a[164:167], v225 offset:1024\n"
"v_mfma_f32_16x16x32_bf16 v[160:163], a[100:103], a[168:171], v[160:163]\n"
"buffer_load_dwordx4 v224, s[28:31], s62 offen lds\n"
"v_mfma_f32_16x16x32_bf16 v[52:55], a[72:75], a[140:143], v[52:55]\n"
"s_add_u32 s61, s61, 128\n"
"v_mfma_f32_16x16x32_bf16 v[52:55], a[104:107], a[168:171], v[52:55]\n"
"s_cmp_ge_u32 s61, 1152\n"
"s_cselect_b32 s63, 1152, 0\n"
"v_mfma_f32_16x16x32_bf16 v[164:167], a[76:79], a[140:143], v[164:167]\n"
"s_sub_u32 s61, s61, s63\n"
"v_mfma_f32_16x16x32_bf16 v[164:167], a[108:111], a[168:171], v[164:167]\n"
"s_add_u32 m0, s66, 17408\n"
"s_add_u32 s62, s60, 32768\n"
"v_mfma_f32_16x16x32_bf16 v[56:59], a[80:83], a[140:143], v[56:59]\n"
"s_waitcnt lgkmcnt(2)\n"
"buffer_load_dwordx4 v243, s[24:27], s62 offen lds\n"
"v_mfma_f32_16x16x32_bf16 v[56:59], a[112:115], a[168:171], v[56:59]\n"
"s_waitcnt vmcnt(13)\n"
"ds_read_b64_tr_b16 a[32:33], v247 offset:8192\n"
"v_mfma_f32_16x16x32_bf16 v[168:171], a[84:87], a[140:143], v[168:171]\n"
"s_add_u32 m0, s66, 19456\n"
"s_add_u32 s62, s60, 98304\n"
"ds_read_b64_tr_b16 a[36:37], v247 offset:8200\n"
"v_mfma_f32_16x16x32_bf16 v[168:171], a[116:119], a[168:171], v[168:171]\n"
"buffer_load_dwordx4 v243, s[24:27], s62 offen lds\n"
"v_mfma_f32_16x16x32_bf16 v[60:63], a[88:91], a[140:143], v[60:63]\n"
"ds_read_b64_tr_b16 a[40:41], v247 offset:8448\n"
"v_mfma_f32_16x16x32_bf16 v[60:63], a[120:123], a[168:171], v[60:63]\n"
"ds_read_b64_tr_b16 a[44:45], v247 offset:8456\n"
"v_mfma_f32_16x16x32_bf16 v[172:175], a[92:95], a[140:143], v[172:175]\n"
"ds_read_b64_tr_b16 a[48:49], v247 offset:8704\n"
"v_mfma_f32_16x16x32_bf16 v[172:175], a[124:127], a[168:171], v[172:175]\n"
"ds_read_b64_tr_b16 a[52:53], v247 offset:8712\n"
"v_mfma_f32_16x16x32_bf16 v[64:67], a[64:67], a[144:147], v[64:67]\n"
"s_add_u32 m0, s66, 21504\n"
"s_add_u32 s62, s60, 163840\n"
"ds_read_b128 a[140:143], v226 offset:1024\n"
"v_mfma_f32_16x16x32_bf16 v[64:67], a[96:99], a[172:175], v[64:67]\n"
"buffer_load_dwordx4 v243, s[24:27], s62 offen lds\n"
"v_mfma_f32_16x16x32_bf16 v[176:179], a[68:71], a[144:147], v[176:179]\n"
"ds_read_b128 a[168:171], v227 offset:1024\n"
"v_mfma_f32_16x16x32_bf16 v[176:179], a[100:103], a[172:175], v[176:179]\n"
"ds_read_b64_tr_b16 a[56:57], v247 offset:8960\n"
"v_mfma_f32_16x16x32_bf16 v[68:71], a[72:75], a[144:147], v[68:71]\n"
"ds_read_b64_tr_b16 a[60:61], v247 offset:8968\n"
"v_mfma_f32_16x16x32_bf16 v[68:71], a[104:107], a[172:175], v[68:71]\n"
"s_add_u32 m0, s66, 23552\n"
"s_add_u32 s62, s60, 229376\n"
"v_mfma_f32_16x16x32_bf16 v[180:183], a[76:79], a[144:147], v[180:183]\n"
"buffer_load_dwordx4 v243, s[24:27], s62 offen lds\n"
"v_mfma_f32_16x16x32_bf16 v[180:183], a[108:111], a[172:175], v[180:183]\n"
"s_add_u32 s60, s60, 262144\n"
"v_mfma_f32_16x16x32_bf16 v[72:75], a[80:83], a[144:147], v[72:75]\n"
"s_cmp_ge_u32 s60, 4718592\n"
"s_cselect_b32 s63, 4718592, 0\n"
"v_mfma_f32_16x16x32_bf16 v[72:75], a[112:115], a[172:175], v[72:75]\n"
"s_sub_u32 s60, s60, s63\n"
"v_mfma_f32_16x16x32_bf16 v[184:187], a[84:87], a[144:147], v[184:187]\n"
"v_mfma_f32_16x16x32_bf16 v[184:187], a[116:119], a[172:175], v[184:187]\n"
"v_mfma_f32_16x16x32_bf16 v[76:79], a[88:91], a[144:147], v[76:79]\n"
"s_mov_b32 m0, s86\n"
"v_mfma_f32_16x16x32_bf16 v[76:79], a[120:123], a[172:175], v[76:79]\n"
"buffer_load_dwordx4 v245, s[80:83], s87 offen nt lds\n"
"v_mfma_f32_16x16x32_bf16 v[188:191], a[92:95], a[144:147], v[188:191]\n"
"v_mfma_f32_16x16x32_bf16 v[188:191], a[124:127], a[172:175], v[188:191]\n"
"v_mfma_f32_16x16x32_bf16 v[80:83], a[64:67], a[148:151], v[80:83]\n"
"ds_read_b128 a[144:147], v248 offset:2048\n"
"v_mfma_f32_16x16x32_bf16 v[80:83], a[96:99], a[176:179], v[80:83]\n"
"v_mfma_f32_16x16x32_bf16 v[192:195], a[68:71], a[148:151], v[192:195]\n"
"s_mov_b32 m0, s86\n"
"ds_read_b128 a[172:175], v225 offset:2048\n"
"v_mfma_f32_16x16x32_bf16 v[192:195], a[100:103], a[176:179], v[192:195]\n"
"buffer_load_dwordx4 v246, s[80:83], s87 offen offset:1024 lds\n"
"v_mfma_f32_16x16x32_bf16 v[84:87], a[72:75], a[148:151], v[84:87]\n"
"s_add_u32 s87, s87, 2048\n"
"s_and_b32 s87, s87, s98\n"
"s_waitcnt vmcnt(14)\n"
"ds_read_b64_tr_b16 a[34:35], v247 offset:9216\n"
"v_mfma_f32_16x16x32_bf16 v[84:87], a[104:107], a[176:179], v[84:87]\n"
"s_add_u32 s86, s86, 2048\n"
"ds_read_b64_tr_b16 a[38:39], v247 offset:9224\n"
"v_mfma_f32_16x16x32_bf16 v[196:199], a[76:79], a[148:151], v[196:199]\n"
"ds_read_b64_tr_b16 a[42:43], v247 offset:9472\n"
"v_mfma_f32_16x16x32_bf16 v[196:199], a[108:111], a[176:179], v[196:199]\n"
"ds_read_b64_tr_b16 a[46:47], v247 offset:9480\n"
"v_mfma_f32_16x16x32_bf16 v[88:91], a[80:83], a[148:151], v[88:91]\n"
"s_mov_b32 m0, s86\n"
"ds_read_b64_tr_b16 a[50:51], v247 offset:9728\n"
"v_mfma_f32_16x16x32_bf16 v[88:91], a[112:115], a[176:179], v[88:91]\n"
"buffer_load_dwordx4 v245, s[80:83], s87 offen nt lds\n"
"v_mfma_f32_16x16x32_bf16 v[200:203], a[84:87], a[148:151], v[200:203]\n"
"ds_read_b64_tr_b16 a[54:55], v247 offset:9736\n"
"v_mfma_f32_16x16x32_bf16 v[200:203], a[116:119], a[176:179], v[200:203]\n"
"ds_read_b64_tr_b16 a[58:59], v247 offset:9984\n"
"v_mfma_f32_16x16x32_bf16 v[92:95], a[88:91], a[148:151], v[92:95]\n"
"ds_read_b64_tr_b16 a[62:63], v247 offset:9992\n"
"v_mfma_f32_16x16x32_bf16 v[92:95], a[120:123], a[176:179], v[92:95]\n"
"v_mfma_f32_16x16x32_bf16 v[204:207], a[92:95], a[148:151], v[204:207]\n"
"s_mov_b32 m0, s86\n"
"v_mfma_f32_16x16x32_bf16 v[204:207], a[124:127], a[176:179], v[204:207]\n"
"buffer_load_dwordx4 v246, s[80:83], s87 offen offset:1024 lds\n"
"v_mfma_f32_16x16x32_bf16 v[96:99], a[64:67], a[152:155], v[96:99]\n"
"s_add_u32 s87, s87, 2048\n"
"s_and_b32 s87, s87, s98\n"
"ds_read_b128 a[148:151], v226 offset:2048\n"
"v_mfma_f32_16x16x32_bf16 v[96:99], a[96:99], a[180:183], v[96:99]\n"
"s_mov_b32 s86, s66\n"
"v_mfma_f32_16x16x32_bf16 v[208:211], a[68:71], a[152:155], v[208:211]\n"
"s_add_u32 m0, s66, 24576\n"
"s_add_u32 s62, s60, 0\n"
"ds_read_b128 a[176:179], v227 offset:2048\n"
"v_mfma_f32_16x16x32_bf16 v[208:211], a[100:103], a[180:183], v[208:211]\n"
"s_waitcnt lgkmcnt(12)\n"
"buffer_load_dwordx4 v243, s[24:27], s62 offen lds\n"
"v_mfma_f32_16x16x32_bf16 v[100:103], a[72:75], a[152:155], v[100:103]\n"
"v_mfma_f32_16x16x32_bf16 v[100:103], a[104:107], a[180:183], v[100:103]\n"
"s_add_u32 m0, s66, 26624\n"
"s_add_u32 s62, s60, 65536\n"
"v_mfma_f32_16x16x32_bf16 v[212:215], a[76:79], a[152:155], v[212:215]\n"
"buffer_load_dwordx4 v243, s[24:27], s62 offen lds\n"
"v_mfma_f32_16x16x32_bf16 v[212:215], a[108:111], a[180:183], v[212:215]\n"
"v_mfma_f32_16x16x32_bf16 v[104:107], a[80:83], a[152:155], v[104:107]\n"
"s_add_u32 m0, s66, 28672\n"
"s_add_u32 s62, s60, 131072\n"
"v_mfma_f32_16x16x32_bf16 v[104:107], a[112:115], a[180:183], v[104:107]\n"
"buffer_load_dwordx4 v243, s[24:27], s62 offen lds\n"
"v_mfma_f32_16x16x32_bf16 v[216:219], a[84:87], a[152:155], v[216:219]\n"
"v_mfma_f32_16x16x32_bf16 v[216:219], a[116:119], a[180:183], v[216:219]\n"
"s_add_u32 m0, s66, 30720\n"
"s_add_u32 s62, s60, 196608\n"
"v_mfma_f32_16x16x32_bf16 v[108:111], a[88:91], a[152:155], v[108:111]\n"
"buffer_load_dwordx4 v243, s[24:27], s62 offen lds\n"
"v_mfma_f32_16x16x32_bf16 v[108:111], a[120:123], a[180:183], v[108:111]\n"
"v_mfma_f32_16x16x32_bf16 v[220:223], a[92:95], a[152:155], v[220:223]\n"
"s_add_u32 m0, s66, 25600\n"
"s_add_u32 s62, s60, 32768\n"
"v_mfma_f32_16x16x32_bf16 v[220:223], a[124:127], a[180:183], v[220:223]\n"
"s_waitcnt lgkmcnt(2)\n"
"buffer_load_dwordx4 v243, s[24:27], s62 offen lds\n"
"; qproj iteration 4\n"
"v_mfma_f32_16x16x32_bf16 v[0:3], a[0:3], a[128:131], v[0:3]\n"
"ds_read_b128 a[152:155], v248 offset:3072\n"
"v_mfma_f32_16x16x32_bf16 v[0:3], a[32:35], a[156:159], v[0:3]\n"
"s_waitcnt vmcnt(15)\n"
"ds_read_b64_tr_b16 a[64:65], v247 offset:0\n"
"v_mfma_f32_16x16x32_bf16 v[112:115], a[4:7], a[128:131], v[112:115]\n"
"ds_read_b128 a[180:183], v225 offset:3072\n"
"v_mfma_f32_16x16x32_bf16 v[112:115], a[36:39], a[156:159], v[112:115]\n"
"s_add_u32 m0, s66, 27648\n"
"s_add_u32 s62, s60, 98304\n"
"ds_read_b64_tr_b16 a[68:69], v247 offset:8\n"
"v_mfma_f32_16x16x32_bf16 v[4:7], a[8:11], a[128:131], v[4:7]\n"
"buffer_load_dwordx4 v243, s[24:27], s62 offen lds\n"
"v_mfma_f32_16x16x32_bf16 v[4:7], a[40:43], a[156:159], v[4:7]\n"
"ds_read_b64_tr_b16 a[72:73], v247 offset:256\n"
"v_mfma_f32_16x16x32_bf16 v[116:119], a[12:15], a[128:131], v[116:119]\n"
"ds_read_b64_tr_b16 a[76:77], v247 offset:264\n"
"v_mfma_f32_16x16x32_bf16 v[116:119], a[44:47], a[156:159], v[116:119]\n"
"ds_read_b64_tr_b16 a[80:81], v247 offset:512\n"
"v_mfma_f32_16x16x32_bf16 v[8:11], a[16:19], a[128:131], v[8:11]\n"
"s_add_u32 m0, s66, 29696\n"
"s_add_u32 s62, s60, 163840\n"
"ds_read_b64_tr_b16 a[84:85], v247 offset:520\n"
"v_mfma_f32_16x16x32_bf16 v[8:11], a[48:51], a[156:159], v[8:11]\n"
"buffer_load_dwordx4 v243, s[24:27], s62 offen lds\n"
"v_mfma_f32_16x16x32_bf16 v[120:123], a[20:23], a[128:131], v[120:123]\n"
"ds_read_b64_tr_b16 a[88:89], v247 offset:768\n"
"v_mfma_f32_16x16x32_bf16 v[120:123], a[52:55], a[156:159], v[120:123]\n"
"ds_read_b64_tr_b16 a[92:93], v247 offset:776\n"
"v_mfma_f32_16x16x32_bf16 v[12:15], a[24:27], a[128:131], v[12:15]\n"
"v_mfma_f32_16x16x32_bf16 v[12:15], a[56:59], a[156:159], v[12:15]\n"
"s_waitcnt lgkmcnt(10)\n"
"v_mfma_f32_16x16x32_bf16 v[124:127], a[28:31], a[128:131], v[124:127]\n"
"s_add_u32 m0, s66, 31744\n"
"s_add_u32 s62, s60, 229376\n"
"s_waitcnt vmcnt(15) lgkmcnt(7)\n"
"s_barrier\n"
"v_mfma_f32_16x16x32_bf16 v[124:127], a[60:63], a[156:159], v[124:127]\n"
"buffer_load_dwordx4 v243, s[24:27], s62 offen lds\n"
"v_mfma_f32_16x16x32_bf16 v[16:19], a[0:3], a[132:135], v[16:19]\n"
"s_add_u32 s60, s60, 262144\n"
"ds_read_b128 a[128:131], v248 offset:4096\n"
"v_mfma_f32_16x16x32_bf16 v[16:19], a[32:35], a[160:163], v[16:19]\n"
"s_cmp_ge_u32 s60, 4718592\n"
"s_cselect_b32 s63, 4718592, 0\n"
"v_mfma_f32_16x16x32_bf16 v[128:131], a[4:7], a[132:135], v[128:131]\n"
"s_sub_u32 s60, s60, s63\n"
"ds_read_b128 a[156:159], v225 offset:4096\n"
"v_mfma_f32_16x16x32_bf16 v[128:131], a[36:39], a[160:163], v[128:131]\n"
"v_mfma_f32_16x16x32_bf16 v[20:23], a[8:11], a[132:135], v[20:23]\n"
"v_mfma_f32_16x16x32_bf16 v[20:23], a[40:43], a[160:163], v[20:23]\n"
"s_add_u32 m0, s66, 32768\n"
"s_add_u32 s62, s61, 0\n"
"v_mfma_f32_16x16x32_bf16 v[132:135], a[12:15], a[132:135], v[132:135]\n"
"buffer_load_dwordx4 v244, s[28:31], s62 offen lds\n"
"v_mfma_f32_16x16x32_bf16 v[132:135], a[44:47], a[160:163], v[132:135]\n"
"v_mfma_f32_16x16x32_bf16 v[24:27], a[16:19], a[132:135], v[24:27]\n"
"v_mfma_f32_16x16x32_bf16 v[24:27], a[48:51], a[160:163], v[24:27]\n"
"v_mfma_f32_16x16x32_bf16 v[136:139], a[20:23], a[132:135], v[136:139]\n"
"s_add_u32 m0, s66, 33792\n"
"s_add_u32 s62, s61, 36864\n"
"v_mfma_f32_16x16x32_bf16 v[136:139], a[52:55], a[160:163], v[136:139]\n"
"buffer_load_dwordx4 v244, s[28:31], s62 offen lds\n"
"v_mfma_f32_16x16x32_bf16 v[28:31], a[24:27], a[132:135], v[28:31]\n"
"s_waitcnt vmcnt(14)\n"
"ds_read_b64_tr_b16 a[66:67], v247 offset:1024\n"
"v_mfma_f32_16x16x32_bf16 v[28:31], a[56:59], a[160:163], v[28:31]\n"
"s_add_u32 m0, s66, 16384\n"
"s_add_u32 s62, s60, 0\n"
"ds_read_b64_tr_b16 a[70:71], v247 offset:1032\n"
"v_mfma_f32_16x16x32_bf16 v[140:143], a[28:31], a[132:135], v[140:143]\n"
"s_waitcnt lgkmcnt(4)\n"
"buffer_load_dwordx4 v243, s[24:27], s62 offen lds\n"
"v_mfma_f32_16x16x32_bf16 v[140:143], a[60:63], a[160:163], v[140:143]\n"
"ds_read_b64_tr_b16 a[74:75], v247 offset:1280\n"
"v_mfma_f32_16x16x32_bf16 v[32:35], a[0:3], a[136:139], v[32:35]\n"
"s_add_u32 m0, s66, 18432\n"
"s_add_u32 s62, s60, 65536\n"
"ds_read_b128 a[132:135], v226 offset:4096\n"
"v_mfma_f32_16x16x32_bf16 v[32:35], a[32:35], a[164:167], v[32:35]\n"
"buffer_load_dwordx4 v243, s[24:27], s62 offen lds\n"
"v_mfma_f32_16x16x32_bf16 v[144:147], a[4:7], a[136:139], v[144:147]\n"
"ds_read_b128 a[160:163], v227 offset:4096\n"
"v_mfma_f32_16x16x32_bf16 v[144:147], a[36:39], a[164:167], v[144:147]\n"
"s_add_u32 m0, s66, 20480\n"
"s_add_u32 s62, s60, 131072\n"
"ds_read_b64_tr_b16 a[78:79], v247 offset:1288\n"
"v_mfma_f32_16x16x32_bf16 v[36:39], a[8:11], a[136:139], v[36:39]\n"
"buffer_load_dwordx4 v243, s[24:27], s62 offen lds\n"
"v_mfma_f32_16x16x32_bf16 v[36:39], a[40:43], a[164:167], v[36:39]\n"
"ds_read_b64_tr_b16 a[82:83], v247 offset:1536\n"
"v_mfma_f32_16x16x32_bf16 v[148:151], a[12:15], a[136:139], v[148:151]\n"
"ds_read_b64_tr_b16 a[86:87], v247 offset:1544\n"
"v_mfma_f32_16x16x32_bf16 v[148:151], a[44:47], a[164:167], v[148:151]\n"
"ds_read_b64_tr_b16 a[90:91], v247 offset:1792\n"
"v_mfma_f32_16x16x32_bf16 v[40:43], a[16:19], a[136:139], v[40:43]\n"
"s_add_u32 m0, s66, 22528\n"
"s_add_u32 s62, s60, 196608\n"
"ds_read_b64_tr_b16 a[94:95], v247 offset:1800\n"
"v_mfma_f32_16x16x32_bf16 v[40:43], a[48:51], a[164:167], v[40:43]\n"
"buffer_load_dwordx4 v243, s[24:27], s62 offen lds\n"
"v_mfma_f32_16x16x32_bf16 v[152:155], a[20:23], a[136:139], v[152:155]\n"
"v_mfma_f32_16x16x32_bf16 v[152:155], a[52:55], a[164:167], v[152:155]\n"
"v_mfma_f32_16x16x32_bf16 v[44:47], a[24:27], a[136:139], v[44:47]\n"
"v_mfma_f32_16x16x32_bf16 v[44:47], a[56:59], a[164:167], v[44:47]\n"
"s_add_u32 m0, s66, 34816\n"
"s_add_u32 s62, s61, 73728\n"
"v_mfma_f32_16x16x32_bf16 v[156:159], a[28:31], a[136:139], v[156:159]\n"
"buffer_load_dwordx4 v244, s[28:31], s62 offen lds\n"
"v_mfma_f32_16x16x32_bf16 v[156:159], a[60:63], a[164:167], v[156:159]\n"
"v_mfma_f32_16x16x32_bf16 v[48:51], a[0:3], a[140:143], v[48:51]\n"
"ds_read_b128 a[136:139], v248 offset:5120\n"
"v_mfma_f32_16x16x32_bf16 v[48:51], a[32:35], a[168:171], v[48:51]\n"
"v_mfma_f32_16x16x32_bf16 v[160:163], a[4:7], a[140:143], v[160:163]\n"
"s_add_u32 m0, s66, 35840\n"
"s_add_u32 s62, s61, 110592\n"
"ds_read_b128 a[164:167], v225 offset:5120\n"
"v_mfma_f32_16x16x32_bf16 v[160:163], a[36:39], a[168:171], v[160:163]\n"
"buffer_load_dwordx4 v224, s[28:31], s62 offen lds\n"
"v_mfma_f32_16x16x32_bf16 v[52:55], a[8:11], a[140:143], v[52:55]\n"
"s_add_u32 s61, s61, 128\n"
"v_mfma_f32_16x16x32_bf16 v[52:55], a[40:43], a[168:171], v[52:55]\n"
"s_cmp_ge_u32 s61, 1152\n"
"s_cselect_b32 s63, 1152, 0\n"
"v_mfma_f32_16x16x32_bf16 v[164:167], a[12:15], a[140:143], v[164:167]\n"
"s_sub_u32 s61, s61, s63\n"
"v_mfma_f32_16x16x32_bf16 v[164:167], a[44:47], a[168:171], v[164:167]\n"
"s_add_u32 m0, s66, 17408\n"
"s_add_u32 s62, s60, 32768\n"
"v_mfma_f32_16x16x32_bf16 v[56:59], a[16:19], a[140:143], v[56:59]\n"
"s_waitcnt lgkmcnt(2)\n"
"buffer_load_dwordx4 v243, s[24:27], s62 offen lds\n"
"v_mfma_f32_16x16x32_bf16 v[56:59], a[48:51], a[168:171], v[56:59]\n"
"s_waitcnt vmcnt(13)\n"
"ds_read_b64_tr_b16 a[96:97], v247 offset:8192\n"
"v_mfma_f32_16x16x32_bf16 v[168:171], a[20:23], a[140:143], v[168:171]\n"
"s_add_u32 m0, s66, 19456\n"
"s_add_u32 s62, s60, 98304\n"
"ds_read_b64_tr_b16 a[100:101], v247 offset:8200\n"
"v_mfma_f32_16x16x32_bf16 v[168:171], a[52:55], a[168:171], v[168:171]\n"
"buffer_load_dwordx4 v243, s[24:27], s62 offen lds\n"
"v_mfma_f32_16x16x32_bf16 v[60:63], a[24:27], a[140:143], v[60:63]\n"
"ds_read_b64_tr_b16 a[104:105], v247 offset:8448\n"
"v_mfma_f32_16x16x32_bf16 v[60:63], a[56:59], a[168:171], v[60:63]\n"
"ds_read_b64_tr_b16 a[108:109], v247 offset:8456\n"
"v_mfma_f32_16x16x32_bf16 v[172:175], a[28:31], a[140:143], v[172:175]\n"
"ds_read_b64_tr_b16 a[112:113], v247 offset:8704\n"
"v_mfma_f32_16x16x32_bf16 v[172:175], a[60:63], a[168:171], v[172:175]\n"
"ds_read_b64_tr_b16 a[116:117], v247 offset:8712\n"
"v_mfma_f32_16x16x32_bf16 v[64:67], a[0:3], a[144:147], v[64:67]\n"
"s_add_u32 m0, s66, 21504\n"
"s_add_u32 s62, s60, 163840\n"
"ds_read_b128 a[140:143], v226 offset:5120\n"
"v_mfma_f32_16x16x32_bf16 v[64:67], a[32:35], a[172:175], v[64:67]\n"
"buffer_load_dwordx4 v243, s[24:27], s62 offen lds\n"
"v_mfma_f32_16x16x32_bf16 v[176:179], a[4:7], a[144:147], v[176:179]\n"
"ds_read_b128 a[168:171], v227 offset:5120\n"
"v_mfma_f32_16x16x32_bf16 v[176:179], a[36:39], a[172:175], v[176:179]\n"
"ds_read_b64_tr_b16 a[120:121], v247 offset:8960\n"
"v_mfma_f32_16x16x32_bf16 v[68:71], a[8:11], a[144:147], v[68:71]\n"
"ds_read_b64_tr_b16 a[124:125], v247 offset:8968\n"
"v_mfma_f32_16x16x32_bf16 v[68:71], a[40:43], a[172:175], v[68:71]\n"
"s_add_u32 m0, s66, 23552\n"
"s_add_u32 s62, s60, 229376\n"
"v_mfma_f32_16x16x32_bf16 v[180:183], a[12:15], a[144:147], v[180:183]\n"
"buffer_load_dwordx4 v243, s[24:27], s62 offen lds\n"
"v_mfma_f32_16x16x32_bf16 v[180:183], a[44:47], a[172:175], v[180:183]\n"
"s_add_u32 s60, s60, 262144\n"
"v_mfma_f32_16x16x32_bf16 v[72:75], a[16:19], a[144:147], v[72:75]\n"
"s_cmp_ge_u32 s60, 4718592\n"
"s_cselect_b32 s63, 4718592, 0\n"
"v_mfma_f32_16x16x32_bf16 v[72:75], a[48:51], a[172:175], v[72:75]\n"
"s_sub_u32 s60, s60, s63\n"
"v_mfma_f32_16x16x32_bf16 v[184:187], a[20:23], a[144:147], v[184:187]\n"
"v_mfma_f32_16x16x32_bf16 v[184:187], a[52:55], a[172:175], v[184:187]\n"
"v_mfma_f32_16x16x32_bf16 v[76:79], a[24:27], a[144:147], v[76:79]\n"
"s_mov_b32 m0, s86\n"
"v_mfma_f32_16x16x32_bf16 v[76:79], a[56:59], a[172:175], v[76:79]\n"
"buffer_load_dwordx4 v245, s[80:83], s87 offen nt lds\n"
"v_mfma_f32_16x16x32_bf16 v[188:191], a[28:31], a[144:147], v[188:191]\n"
"v_mfma_f32_16x16x32_bf16 v[188:191], a[60:63], a[172:175], v[188:191]\n"
"v_mfma_f32_16x16x32_bf16 v[80:83], a[0:3], a[148:151], v[80:83]\n"
"ds_read_b128 a[144:147], v248 offset:6144\n"
"v_mfma_f32_16x16x32_bf16 v[80:83], a[32:35], a[176:179], v[80:83]\n"
"v_mfma_f32_16x16x32_bf16 v[192:195], a[4:7], a[148:151], v[192:195]\n"
"s_mov_b32 m0, s86\n"
"ds_read_b128 a[172:175], v225 offset:6144\n"
"v_mfma_f32_16x16x32_bf16 v[192:195], a[36:39], a[176:179], v[192:195]\n"
"buffer_load_dwordx4 v246, s[80:83], s87 offen offset:1024 lds\n"
"v_mfma_f32_16x16x32_bf16 v[84:87], a[8:11], a[148:151], v[84:87]\n"
"s_add_u32 s87, s87, 2048\n"
"s_and_b32 s87, s87, s98\n"
"s_waitcnt vmcnt(14)\n"
"ds_read_b64_tr_b16 a[98:99], v247 offset:9216\n"
"v_mfma_f32_16x16x32_bf16 v[84:87], a[40:43], a[176:179], v[84:87]\n"
"s_add_u32 s86, s86, 2048\n"
"ds_read_b64_tr_b16 a[102:103], v247 offset:9224\n"
"v_mfma_f32_16x16x32_bf16 v[196:199], a[12:15], a[148:151], v[196:199]\n"
"ds_read_b64_tr_b16 a[106:107], v247 offset:9472\n"
"v_mfma_f32_16x16x32_bf16 v[196:199], a[44:47], a[176:179], v[196:199]\n"
"ds_read_b64_tr_b16 a[110:111], v247 offset:9480\n"
"v_mfma_f32_16x16x32_bf16 v[88:91], a[16:19], a[148:151], v[88:91]\n"
"s_mov_b32 m0, s86\n"
"ds_read_b64_tr_b16 a[114:115], v247 offset:9728\n"
"s_nop 0\n"
"Lh2:\n"
"s_cbranch_execz Lw0\n"
"v_mfma_f32_16x16x32_bf16 v[88:91], a[48:51], a[176:179], v[88:91]\n"
"buffer_load_dwordx4 v245, s[80:83], s87 offen nt lds\n"
"v_mfma_f32_16x16x32_bf16 v[200:203], a[20:23], a[148:151], v[200:203]\n"
"ds_read_b64_tr_b16 a[118:119], v247 offset:9736\n"
"v_mfma_f32_16x16x32_bf16 v[200:203], a[52:55], a[176:179], v[200:203]\n"
"ds_read_b64_tr_b16 a[122:123], v247 offset:9984\n"
"v_mfma_f32_16x16x32_bf16 v[92:95], a[24:27], a[148:151], v[92:95]\n"
"ds_read_b64_tr_b16 a[126:127], v247 offset:9992\n"
"v_mfma_f32_16x16x32_bf16 v[92:95], a[56:59], a[176:179], v[92:95]\n"
"v_mfma_f32_16x16x32_bf16 v[204:207], a[28:31], a[148:151], v[204:207]\n"
"s_mov_b32 m0, s86\n"
"v_mfma_f32_16x16x32_bf16 v[204:207], a[60:63], a[176:179], v[204:207]\n"
"buffer_load_dwordx4 v246, s[80:83], s87 offen offset:1024 lds\n"
"v_mfma_f32_16x16x32_bf16 v[96:99], a[0:3], a[152:155], v[96:99]\n"
"s_add_u32 s87, s87, 2048\n"
"s_and_b32 s87, s87, s98\n"
"ds_read_b128 a[148:151], v226 offset:6144\n"
"v_mfma_f32_16x16x32_bf16 v[96:99], a[32:35], a[180:183], v[96:99]\n"
"s_add_u32 s86, s86, 2048\n"
"v_mfma_f32_16x16x32_bf16 v[208:211], a[4:7], a[152:155], v[208:211]\n"
"s_add_u32 m0, s66, 24576\n"
"s_add_u32 s62, s60, 0\n"
"ds_read_b128 a[176:179], v227 offset:6144\n"
"v_mfma_f32_16x16x32_bf16 v[208:211], a[36:39], a[180:183], v[208:211]\n"
"s_waitcnt lgkmcnt(12)\n"
"buffer_load_dwordx4 v243, s[24:27], s62 offen lds\n"
"v_mfma_f32_16x16x32_bf16 v[100:103], a[8:11], a[152:155], v[100:103]\n"
"v_mfma_f32_16x16x32_bf16 v[100:103], a[40:43], a[180:183], v[100:103]\n"
"s_add_u32 m0, s66, 26624\n"
"s_add_u32 s62, s60, 65536\n"
"v_mfma_f32_16x16x32_bf16 v[212:215], a[12:15], a[152:155], v[212:215]\n"
"buffer_load_dwordx4 v243, s[24:27], s62 offen lds\n"
"v_mfma_f32_16x16x32_bf16 v[212:215], a[44:47], a[180:183], v[212:215]\n"
"v_mfma_f32_16x16x32_bf16 v[104:107], a[16:19], a[152:155], v[104:107]\n"
"s_add_u32 m0, s66, 28672\n"
"s_add_u32 s62, s60, 131072\n"
"v_mfma_f32_16x16x32_bf16 v[104:107], a[48:51], a[180:183], v[104:107]\n"
"buffer_load_dwordx4 v243, s[24:27], s62 offen lds\n"
"v_mfma_f32_16x16x32_bf16 v[216:219], a[20:23], a[152:155], v[216:219]\n"
"v_mfma_f32_16x16x32_bf16 v[216:219], a[52:55], a[180:183], v[216:219]\n"
"s_add_u32 m0, s66, 30720\n"
"s_add_u32 s62, s60, 196608\n"
"v_mfma_f32_16x16x32_bf16 v[108:111], a[24:27], a[152:155], v[108:111]\n"
"buffer_load_dwordx4 v243, s[24:27], s62 offen lds\n"
"v_mfma_f32_16x16x32_bf16 v[108:111], a[56:59], a[180:183], v[108:111]\n"
"v_mfma_f32_16x16x32_bf16 v[220:223], a[28:31], a[152:155], v[220:223]\n"
"s_add_u32 m0, s66, 25600\n"
"s_add_u32 s62, s60, 32768\n"
"v_mfma_f32_16x16x32_bf16 v[220:223], a[60:63], a[180:183], v[220:223]\n"
"s_waitcnt lgkmcnt(2)\n"
"buffer_load_dwordx4 v243, s[24:27], s62 offen lds\n"
"; qproj iteration 5\n"
"v_mfma_f32_16x16x32_bf16 v[0:3], a[64:67], a[128:131], v[0:3]\n"
"ds_read_b128 a[152:155], v248 offset:7168\n"
"v_mfma_f32_16x16x32_bf16 v[0:3], a[96:99], a[156:159], v[0:3]\n"
"s_waitcnt vmcnt(15)\n"
"ds_read_b64_tr_b16 a[0:1], v247 offset:0\n"
"v_mfma_f32_16x16x32_bf16 v[112:115], a[68:71], a[128:131], v[112:115]\n"
"ds_read_b128 a[180:183], v225 offset:7168\n"
"v_mfma_f32_16x16x32_bf16 v[112:115], a[100:103], a[156:159], v[112:115]\n"
"s_add_u32 m0, s66, 27648\n"
"s_add_u32 s62, s60, 98304\n"
"ds_read_b64_tr_b16 a[4:5], v247 offset:8\n"
"v_mfma_f32_16x16x32_bf16 v[4:7], a[72:75], a[128:131], v[4:7]\n"
"buffer_load_dwordx4 v243, s[24:27], s62 offen lds\n"
"v_mfma_f32_16x16x32_bf16 v[4:7], a[104:107], a[156:159], v[4:7]\n"
"ds_read_b64_tr_b16 a[8:9], v247 offset:256\n"
"v_mfma_f32_16x16x32_bf16 v[116:119], a[76:79], a[128:131], v[116:119]\n"
"ds_read_b64_tr_b16 a[12:13], v247 offset:264\n"
"v_mfma_f32_16x16x32_bf16 v[116:119], a[108:111], a[156:159], v[116:119]\n"
"ds_read_b64_tr_b16 a[16:17], v247 offset:512\n"
"v_mfma_f32_16x16x32_bf16 v[8:11], a[80:83], a[128:131], v[8:11]\n"
"s_add_u32 m0, s66, 29696\n"
"s_add_u32 s62, s60, 163840\n"
"ds_read_b64_tr_b16 a[20:21], v247 offset:520\n"
"v_mfma_f32_16x16x32_bf16 v[8:11], a[112:115], a[156:159], v[8:11]\n"
"buffer_load_dwordx4 v243, s[24:27], s62 offen lds\n"
"v_mfma_f32_16x16x32_bf16 v[120:123], a[84:87], a[128:131], v[120:123]\n"
"ds_read_b64_tr_b16 a[24:25], v247 offset:768\n"
"v_mfma_f32_16x16x32_bf16 v[120:123], a[116:119], a[156:159], v[120:123]\n"
"ds_read_b64_tr_b16 a[28:29], v247 offset:776\n"
"v_mfma_f32_16x16x32_bf16 v[12:15], a[88:91], a[128:131], v[12:15]\n"
"v_mfma_f32_16x16x32_bf16 v[12:15], a[120:123], a[156:159], v[12:15]\n"
"s_waitcnt lgkmcnt(10)\n"
"v_mfma_f32_16x16x32_bf16 v[124:127], a[92:95], a[128:131], v[124:127]\n"
"s_add_u32 m0, s66, 31744\n"
"s_add_u32 s62, s60, 229376\n"
"s_waitcnt vmcnt(15) lgkmcnt(7)\n"
"s_barrier\n"
"v_mfma_f32_16x16x32_bf16 v[124:127], a[124:127], a[156:159], v[124:127]\n"
"buffer_load_dwordx4 v243, s[24:27], s62 offen lds\n"
"v_mfma_f32_16x16x32_bf16 v[16:19], a[64:67], a[132:135], v[16:19]\n"
"s_add_u32 s60, s60, 262144\n"
"ds_read_b128 a[128:131], v248 offset:0\n"
"v_mfma_f32_16x16x32_bf16 v[16:19], a[96:99], a[160:163], v[16:19]\n"
"s_cmp_ge_u32 s60, 4718592\n"
"s_cselect_b32 s63, 4718592, 0\n"
"ds_read_b128 a[224:227], v249 offset:8192\n"
"v_mfma_f32_16x16x32_bf16 v[128:131], a[68:71], a[132:135], v[128:131]\n"
"s_sub_u32 s60, s60, s63\n"
"ds_read_b128 a[156:159], v225 offset:0\n"
"v_mfma_f32_16x16x32_bf16 v[128:131], a[100:103], a[160:163], v[128:131]\n"
"ds_read_b128 a[228:231], v249 offset:8448\n"
"v_mfma_f32_16x16x32_bf16 v[20:23], a[72:75], a[132:135], v[20:23]\n"
"v_mfma_f32_16x16x32_bf16 v[20:23], a[104:107], a[160:163], v[20:23]\n"
"s_add_u32 m0, s66, 36864\n"
"s_add_u32 s62, s61, 0\n"
"ds_read_b128 a[232:235], v249 offset:8704\n"
"v_mfma_f32_16x16x32_bf16 v[132:135], a[76:79], a[132:135], v[132:135]\n"
"buffer_load_dwordx4 v244, s[28:31], s62 offen lds\n"
"v_mfma_f32_16x16x32_bf16 v[132:135], a[108:111], a[160:163], v[132:135]\n"
"v_mfma_f32_16x16x32_bf16 v[24:27], a[80:83], a[132:135], v[24:27]\n"
"ds_read_b128 a[236:239], v249 offset:8960\n"
"v_mfma_f32_16x16x32_bf16 v[24:27], a[112:115], a[160:163], v[24:27]\n"
"v_mfma_f32_16x16x32_bf16 v[136:139], a[84:87], a[132:135], v[136:139]\n"
"s_add_u32 m0, s66, 37888\n"
"s_add_u32 s62, s61, 36864\n"
"v_mfma_f32_16x16x32_bf16 v[136:139], a[116:119], a[160:163], v[136:139]\n"
"buffer_load_dwordx4 v244, s[28:31], s62 offen lds\n"
"v_mfma_f32_16x16x32_bf16 v[28:31], a[88:91], a[132:135], v[28:31]\n"
"s_waitcnt vmcnt(14)\n"
"ds_read_b64_tr_b16 a[2:3], v247 offset:1024\n"
"v_mfma_f32_16x16x32_bf16 v[28:31], a[120:123], a[160:163], v[28:31]\n"
"s_add_u32 m0, s66, 16384\n"
"s_add_u32 s62, s60, 0\n"
"ds_read_b64_tr_b16 a[6:7], v247 offset:1032\n"
"v_mfma_f32_16x16x32_bf16 v[140:143], a[92:95], a[132:135], v[140:143]\n"
"s_waitcnt lgkmcnt(8)\n"
"buffer_load_dwordx4 v243, s[24:27], s62 offen lds\n"
"v_mfma_f32_16x16x32_bf16 v[140:143], a[124:127], a[160:163], v[140:143]\n"
"ds_read_b64_tr_b16 a[10:11], v247 offset:1280\n"
"v_mfma_f32_16x16x32_bf16 v[32:35], a[64:67], a[136:139], v[32:35]\n"
"s_add_u32 m0, s66, 18432\n"
"s_add_u32 s62, s60, 65536\n"
"ds_read_b128 a[132:135], v226 offset:0\n"
"v_mfma_f32_16x16x32_bf16 v[32:35], a[96:99], a[164:167], v[32:35]\n"
"buffer_load_dwordx4 v243, s[24:27], s62 offen lds\n"
"v_mfma_f32_16x16x32_bf16 v[144:147], a[68:71], a[136:139], v[144:147]\n"
"ds_read_b128 a[160:163], v227 offset:0\n"
"v_mfma_f32_16x16x32_bf16 v[144:147], a[100:103], a[164:167], v[144:147]\n"
"s_add_u32 m0, s66, 20480\n"
"s_add_u32 s62, s60, 131072\n"
"ds_read_b64_tr_b16 a[14:15], v247 offset:1288\n"
"v_mfma_f32_16x16x32_bf16 v[36:39], a[72:75], a[136:139], v[36:39]\n"
"buffer_load_dwordx4 v243, s[24:27], s62 offen lds\n"
"v_mfma_f32_16x16x32_bf16 v[36:39], a[104:107], a[164:167], v[36:39]\n"
"ds_read_b64_tr_b16 a[18:19], v247 offset:1536\n"
"v_mfma_f32_16x16x32_bf16 v[148:151], a[76:79], a[136:139], v[148:151]\n"
"ds_read_b64_tr_b16 a[22:23], v247 offset:1544\n"
"v_mfma_f32_16x16x32_bf16 v[148:151], a[108:111], a[164:167], v[148:151]\n"
"ds_read_b64_tr_b16 a[26:27], v247 offset:1792\n"
"v_mfma_f32_16x16x32_bf16 v[40:43], a[80:83], a[136:139], v[40:43]\n"
"s_add_u32 m0, s66, 22528\n"
"s_add_u32 s62, s60, 196608\n"
"ds_read_b64_tr_b16 a[30:31], v247 offset:1800\n"
"v_mfma_f32_16x16x32_bf16 v[40:43], a[112:115], a[164:167], v[40:43]\n"
"buffer_load_dwordx4 v243, s[24:27], s62 offen lds\n"
"v_mfma_f32_16x16x32_bf16 v[152:155], a[84:87], a[136:139], v[152:155]\n"
"ds_read_b128 a[240:243], v249 offset:12288\n"
"v_mfma_f32_16x16x32_bf16 v[152:155], a[116:119], a[164:167], v[152:155]\n"
"ds_read_b128 a[244:247], v249 offset:12544\n"
"v_mfma_f32_16x16x32_bf16 v[44:47], a[88:91], a[136:139], v[44:47]\n"
"ds_read_b128 a[248:251], v249 offset:12800\n"
"v_mfma_f32_16x16x32_bf16 v[44:47], a[120:123], a[164:167], v[44:47]\n"
"s_add_u32 m0, s66, 38912\n"
"s_add_u32 s62, s61, 73728\n"
"ds_read_b128 a[252:255], v249 offset:13056\n"
"v_mfma_f32_16x16x32_bf16 v[156:159], a[92:95], a[136:139], v[156:159]\n"
"buffer_load_dwordx4 v244, s[28:31], s62 offen lds\n"
"v_mfma_f32_16x16x32_bf16 v[156:159], a[124:127], a[164:167], v[156:159]\n"
"ds_read_b64_tr_b16 a[192:193], v250 offset:8192\n"
"v_mfma_f32_16x16x32_bf16 v[48:51], a[64:67], a[140:143], v[48:51]\n"
"ds_read_b128 a[136:139], v248 offset:1024\n"
"v_mfma_f32_16x16x32_bf16 v[48:51], a[96:99], a[168:171], v[48:51]\n"
"ds_read_b64_tr_b16 a[194:195], v250 offset:12288\n"
"v_mfma_f32_16x16x32_bf16 v[160:163], a[68:71], a[140:143], v[160:163]\n"
"s_add_u32 m0, s66, 39936\n"
"s_add_u32 s62, s61, 110592\n"
"ds_read_b128 a[164:167], v225 offset:1024\n"
"v_mfma_f32_16x16x32_bf16 v[160:163], a[100:103], a[168:171], v[160:163]\n"
"buffer_load_dwordx4 v224, s[28:31], s62 offen lds\n"
"v_mfma_f32_16x16x32_bf16 v[52:55], a[72:75], a[140:143], v[52:55]\n"
"s_add_u32 s61, s61, 128\n"
"ds_read_b64_tr_b16 a[196:197], v251 offset:8192\n"
"v_mfma_f32_16x16x32_bf16 v[52:55], a[104:107], a[168:171], v[52:55]\n"
"s_cmp_ge_u32 s61, 1152\n"
"s_cselect_b32 s63, 1152, 0\n"
"ds_read_b64_tr_b16 a[198:199], v251 offset:12288\n"
"v_mfma_f32_16x16x32_bf16 v[164:167], a[76:79], a[140:143], v[164:167]\n"
"s_sub_u32 s61, s61, s63\n"
"ds_read_b64_tr_b16 a[200:201], v250 offset:8448\n"
"v_mfma_f32_16x16x32_bf16 v[164:167], a[108:111], a[168:171], v[164:167]\n"
"s_add_u32 m0, s66, 17408\n"
"s_add_u32 s62, s60, 32768\n"
"ds_read_b64_tr_b16 a[202:203], v250 offset:12544\n"
"v_mfma_f32_16x16x32_bf16 v[56:59], a[80:83], a[140:143], v[56:59]\n"
"s_waitcnt lgkmcnt(12)\n"
"buffer_load_dwordx4 v243, s[24:27], s62 offen lds\n"
"v_mfma_f32_16x16x32_bf16 v[56:59], a[112:115], a[168:171], v[56:59]\n"
"s_waitcnt vmcnt(13)\n"
"ds_read_b64_tr_b16 a[32:33], v247 offset:8192\n"
"v_mfma_f32_16x16x32_bf16 v[168:171], a[84:87], a[140:143], v[168:171]\n"
"s_add_u32 m0, s66, 19456\n"
"s_add_u32 s62, s60, 98304\n"
"ds_read_b64_tr_b16 a[36:37], v247 offset:8200\n"
"v_mfma_f32_16x16x32_bf16 v[168:171], a[116:119], a[168:171], v[168:171]\n"
"buffer_load_dwordx4 v243, s[24:27], s62 offen lds\n"
"v_mfma_f32_16x16x32_bf16 v[60:63], a[88:91], a[140:143], v[60:63]\n"
"ds_read_b64_tr_b16 a[40:41], v247 offset:8448\n"
"v_mfma_f32_16x16x32_bf16 v[60:63], a[120:123], a[168:171], v[60:63]\n"
"ds_read_b64_tr_b16 a[44:45], v247 offset:8456\n"
"v_mfma_f32_16x16x32_bf16 v[172:175], a[92:95], a[140:143], v[172:175]\n"
"ds_read_b64_tr_b16 a[48:49], v247 offset:8704\n"
"v_mfma_f32_16x16x32_bf16 v[172:175], a[124:127], a[168:171], v[172:175]\n"
"ds_read_b64_tr_b16 a[52:53], v247 offset:8712\n"
"v_mfma_f32_16x16x32_bf16 v[64:67], a[64:67], a[144:147], v[64:67]\n"
"s_add_u32 m0, s66, 21504\n"
"s_add_u32 s62, s60, 163840\n"
"ds_read_b128 a[140:143], v226 offset:1024\n"
"v_mfma_f32_16x16x32_bf16 v[64:67], a[96:99], a[172:175], v[64:67]\n"
"buffer_load_dwordx4 v243, s[24:27], s62 offen lds\n"
"v_mfma_f32_16x16x32_bf16 v[176:179], a[68:71], a[144:147], v[176:179]\n"
"ds_read_b128 a[168:171], v227 offset:1024\n"
"v_mfma_f32_16x16x32_bf16 v[176:179], a[100:103], a[172:175], v[176:179]\n"
"ds_read_b64_tr_b16 a[56:57], v247 offset:8960\n"
"v_mfma_f32_16x16x32_bf16 v[68:71], a[72:75], a[144:147], v[68:71]\n"
"ds_read_b64_tr_b16 a[60:61], v247 offset:8968\n"
"v_mfma_f32_16x16x32_bf16 v[68:71], a[104:107], a[172:175], v[68:71]\n"
"s_add_u32 m0, s66, 23552\n"
"s_add_u32 s62, s60, 229376\n"
"ds_read_b64_tr_b16 a[204:205], v251 offset:8448\n"
"v_mfma_f32_16x16x32_bf16 v[180:183], a[76:79], a[144:147], v[180:183]\n"
"buffer_load_dwordx4 v243, s[24:27], s62 offen lds\n"
"v_mfma_f32_16x16x32_bf16 v[180:183], a[108:111], a[172:175], v[180:183]\n"
"s_add_u32 s60, s60, 262144\n"
"ds_read_b64_tr_b16 a[206:207], v251 offset:12544\n"
"v_mfma_f32_16x16x32_bf16 v[72:75], a[80:83], a[144:147], v[72:75]\n"
"s_cmp_ge_u32 s60, 4718592\n"
"s_cselect_b32 s63, 4718592, 0\n"
"ds_read_b64_tr_b16 a[208:209], v250 offset:8704\n"
"v_mfma_f32_16x16x32_bf16 v[72:75], a[112:115], a[172:175], v[72:75]\n"
"s_sub_u32 s60, s60, s63\n"
"ds_read_b64_tr_b16 a[210:211], v250 offset:12800\n"
"v_mfma_f32_16x16x32_bf16 v[184:187], a[84:87], a[144:147], v[184:187]\n"
"ds_read_b64_tr_b16 a[212:213], v251 offset:8704\n"
"v_mfma_f32_16x16x32_bf16 v[184:187], a[116:119], a[172:175], v[184:187]\n"
"ds_read_b64_tr_b16 a[214:215], v251 offset:12800\n"
"v_mfma_f32_16x16x32_bf16 v[76:79], a[88:91], a[144:147], v[76:79]\n"
"s_mov_b32 m0, s86\n"
"ds_read_b64_tr_b16 a[216:217], v250 offset:8960\n"
"v_mfma_f32_16x16x32_bf16 v[76:79], a[120:123], a[172:175], v[76:79]\n"
"buffer_load_dwordx4 v245, s[80:83], s87 offen nt lds\n"
"v_mfma_f32_16x16x32_bf16 v[188:191], a[92:95], a[144:147], v[188:191]\n"
"ds_read_b64_tr_b16 a[218:219], v250 offset:13056\n"
"v_mfma_f32_16x16x32_bf16 v[188:191], a[124:127], a[172:175], v[188:191]\n"
"v_mfma_f32_16x16x32_bf16 v[80:83], a[64:67], a[148:151], v[80:83]\n"
"ds_read_b128 a[144:147], v248 offset:2048\n"
"v_mfma_f32_16x16x32_bf16 v[80:83], a[96:99], a[176:179], v[80:83]\n"
"ds_read_b64_tr_b16 a[220:221], v251 offset:8960\n"
"v_mfma_f32_16x16x32_bf16 v[192:195], a[68:71], a[148:151], v[192:195]\n"
"s_mov_b32 m0, s86\n"
"ds_read_b128 a[172:175], v225 offset:2048\n"
"v_mfma_f32_16x16x32_bf16 v[192:195], a[100:103], a[176:179], v[192:195]\n"
"buffer_load_dwordx4 v246, s[80:83], s87 offen offset:1024 lds\n"
"v_mfma_f32_16x16x32_bf16 v[84:87], a[72:75], a[148:151], v[84:87]\n"
"s_add_u32 s87, s87, 2048\n"
"s_and_b32 s87, s87, s98\n"
"s_waitcnt vmcnt(14)\n"
"ds_read_b64_tr_b16 a[34:35], v247 offset:9216\n"
"v_mfma_f32_16x16x32_bf16 v[84:87], a[104:107], a[176:179], v[84:87]\n"
"s_add_u32 s86, s86, 2048\n"
"ds_read_b64_tr_b16 a[38:39], v247 offset:9224\n"
"v_mfma_f32_16x16x32_bf16 v[196:199], a[76:79], a[148:151], v[196:199]\n"
"ds_read_b64_tr_b16 a[42:43], v247 offset:9472\n"
"v_mfma_f32_16x16x32_bf16 v[196:199], a[108:111], a[176:179], v[196:199]\n"
"ds_read_b64_tr_b16 a[46:47], v247 offset:9480\n"
"v_mfma_f32_16x16x32_bf16 v[88:91], a[80:83], a[148:151], v[88:91]\n"
"s_mov_b32 m0, s86\n"
"ds_read_b64_tr_b16 a[50:51], v247 offset:9728\n"
"v_mfma_f32_16x16x32_bf16 v[88:91], a[112:115], a[176:179], v[88:91]\n"
"buffer_load_dwordx4 v245, s[80:83], s87 offen nt lds\n"
"v_mfma_f32_16x16x32_bf16 v[200:203], a[84:87], a[148:151], v[200:203]\n"
"ds_read_b64_tr_b16 a[54:55], v247 offset:9736\n"
"v_mfma_f32_16x16x32_bf16 v[200:203], a[116:119], a[176:179], v[200:203]\n"
"ds_read_b64_tr_b16 a[58:59], v247 offset:9984\n"
"v_mfma_f32_16x16x32_bf16 v[92:95], a[88:91], a[148:151], v[92:95]\n"
"ds_read_b64_tr_b16 a[62:63], v247 offset:9992\n"
"v_mfma_f32_16x16x32_bf16 v[92:95], a[120:123], a[176:179], v[92:95]\n"
"ds_read_b64_tr_b16 a[222:223], v251 offset:13056\n"
"v_mfma_f32_16x16x32_bf16 v[204:207], a[92:95], a[148:151], v[204:207]\n"
"s_mov_b32 m0, s86\n"
"v_mfma_f32_16x16x32_bf16 v[204:207], a[124:127], a[176:179], v[204:207]\n"
"buffer_load_dwordx4 v246, s[80:83], s87 offen offset:1024 lds\n"
"v_mfma_f32_16x16x32_bf16 v[96:99], a[64:67], a[152:155], v[96:99]\n"
"s_add_u32 s87, s87, 2048\n"
"s_and_b32 s87, s87, s98\n"
"ds_read_b128 a[148:151], v226 offset:2048\n"
"v_mfma_f32_16x16x32_bf16 v[96:99], a[96:99], a[180:183], v[96:99]\n"
"s_add_u32 s86, s86, 2048\n"
"v_mfma_f32_16x16x32_bf16 v[208:211], a[68:71], a[152:155], v[208:211]\n"
"s_add_u32 m0, s66, 24576\n"
"s_add_u32 s62, s60, 0\n"
"ds_read_b128 a[176:179], v227 offset:2048\n"
"v_mfma_f32_16x16x32_bf16 v[208:211], a[100:103], a[180:183], v[208:211]\n"
"s_waitcnt lgkmcnt(15)\n"
"buffer_load_dwordx4 v243, s[24:27], s62 offen lds\n"
"v_mfma_f32_16x16x32_bf16 v[100:103], a[72:75], a[152:155], v[100:103]\n"
"v_mfma_f32_16x16x32_bf16 v[100:103], a[104:107], a[180:183], v[100:103]\n"
"s_add_u32 m0, s66, 26624\n"
"s_add_u32 s62, s60, 65536\n"
"v_mfma_f32_16x16x32_bf16 v[212:215], a[76:79], a[152:155], v[212:215]\n"
"buffer_load_dwordx4 v243, s[24:27], s62 offen lds\n"
"v_mfma_f32_16x16x32_bf16 v[212:215], a[108:111], a[180:183], v[212:215]\n"
"v_mfma_f32_16x16x32_bf16 v[104:107], a[80:83], a[152:155], v[104:107]\n"
"s_add_u32 m0, s66, 28672\n"
"s_add_u32 s62, s60, 131072\n"
"v_mfma_f32_16x16x32_bf16 v[104:107], a[112:115], a[180:183], v[104:107]\n"
"buffer_load_dwordx4 v243, s[24:27], s62 offen lds\n"
"v_mfma_f32_16x16x32_bf16 v[216:219], a[84:87], a[152:155], v[216:219]\n"
"v_mfma_f32_16x16x32_bf16 v[216:219], a[116:119], a[180:183], v[216:219]\n"
"s_add_u32 m0, s66, 30720\n"
"s_add_u32 s62, s60, 196608\n"
"v_mfma_f32_16x16x32_bf16 v[108:111], a[88:91], a[152:155], v[108:111]\n"
"buffer_load_dwordx4 v243, s[24:27], s62 offen lds\n"
"v_mfma_f32_16x16x32_bf16 v[108:111], a[120:123], a[180:183], v[108:111]\n"
"v_mfma_f32_16x16x32_bf16 v[220:223], a[92:95], a[152:155], v[220:223]\n"
"s_add_u32 m0, s66, 25600\n"
"s_add_u32 s62, s60, 32768\n"
"v_mfma_f32_16x16x32_bf16 v[220:223], a[124:127], a[180:183], v[220:223]\n"
"s_waitcnt lgkmcnt(3)\n"
"buffer_load_dwordx4 v243, s[24:27], s62 offen lds\n"
"; qproj iteration 6\n"
"v_mfma_f32_16x16x32_bf16 v[0:3], a[0:3], a[128:131], v[0:3]\n"
"ds_read_b128 a[152:155], v248 offset:3072\n"
"v_mfma_f32_16x16x32_bf16 v[0:3], a[32:35], a[156:159], v[0:3]\n"
"s_waitcnt vmcnt(15)\n"
"ds_read_b64_tr_b16 a[64:65], v247 offset:0\n"
"v_mfma_f32_16x16x32_bf16 v[112:115], a[4:7], a[128:131], v[112:115]\n"
"ds_read_b128 a[180:183], v225 offset:3072\n"
"v_mfma_f32_16x16x32_bf16 v[112:115], a[36:39], a[156:159], v[112:115]\n"
"s_add_u32 m0, s66, 27648\n"
"s_add_u32 s62, s60, 98304\n"
"ds_read_b64_tr_b16 a[68:69], v247 offset:8\n"
"v_mfma_f32_16x16x32_bf16 v[4:7], a[8:11], a[128:131], v[4:7]\n"
"buffer_load_dwordx4 v243, s[24:27], s62 offen lds\n"
"v_mfma_f32_16x16x32_bf16 v[4:7], a[40:43], a[156:159], v[4:7]\n"
"ds_read_b64_tr_b16 a[72:73], v247 offset:256\n"
"v_mfma_f32_16x16x32_bf16 v[116:119], a[12:15], a[128:131], v[116:119]\n"
"ds_read_b64_tr_b16 a[76:77], v247 offset:264\n"
"v_mfma_f32_16x16x32_bf16 v[116:119], a[44:47], a[156:159], v[116:119]\n"
"ds_read_b64_tr_b16 a[80:81], v247 offset:512\n"
"v_mfma_f32_16x16x32_bf16 v[8:11], a[16:19], a[128:131], v[8:11]\n"
"s_add_u32 m0, s66, 29696\n"
"s_add_u32 s62, s60, 163840\n"
"ds_read_b64_tr_b16 a[84:85], v247 offset:520\n"
"v_mfma_f32_16x16x32_bf16 v[8:11], a[48:51], a[156:159], v[8:11]\n"
"buffer_load_dwordx4 v243, s[24:27], s62 offen lds\n"
"v_mfma_f32_16x16x32_bf16 v[120:123], a[20:23], a[128:131], v[120:123]\n"
"ds_read_b64_tr_b16 a[88:89], v247 offset:768\n"
"v_mfma_f32_16x16x32_bf16 v[120:123], a[52:55], a[156:159], v[120:123]\n"
"ds_read_b64_tr_b16 a[92:93], v247 offset:776\n"
"v_mfma_f32_16x16x32_bf16 v[12:15], a[24:27], a[128:131], v[12:15]\n"
"v_mfma_f32_16x16x32_bf16 v[12:15], a[56:59], a[156:159], v[12:15]\n"
"s_waitcnt lgkmcnt(10)\n"
"v_mfma_f32_16x16x32_bf16 v[124:127], a[28:31], a[128:131], v[124:127]\n"
"s_add_u32 m0, s66, 31744\n"
"s_add_u32 s62, s60, 229376\n"
"s_waitcnt vmcnt(15) lgkmcnt(7)\n"
"s_barrier\n"
"v_mfma_f32_16x16x32_bf16 v[124:127], a[60:63], a[156:159], v[124:127]\n"
"buffer_load_dwordx4 v243, s[24:27], s62 offen lds\n"
"v_mfma_f32_16x16x32_bf16 v[16:19], a[0:3], a[132:135], v[16:19]\n"
"s_add_u32 s60, s60, 262144\n"
"ds_read_b128 a[128:131], v248 offset:4096\n"
"v_mfma_f32_16x16x32_bf16 v[16:19], a[32:35], a[160:163], v[16:19]\n"
"s_cmp_ge_u32 s60, 4718592\n"
"s_cselect_b32 s63, 4718592, 0\n"
"v_mfma_f32_16x16x32_bf16 v[128:131], a[4:7], a[132:135], v[128:131]\n"
"s_sub_u32 s60, s60, s63\n"
"ds_read_b128 a[156:159], v225 offset:4096\n"
"v_mfma_f32_16x16x32_bf16 v[128:131], a[36:39], a[160:163], v[128:131]\n"
"v_mfma_f32_16x16x32_bf16 v[20:23], a[8:11], a[132:135], v[20:23]\n"
"v_mfma_f32_16x16x32_bf16 v[20:23], a[40:43], a[160:163], v[20:23]\n"
"s_add_u32 m0, s66, 32768\n"
"s_add_u32 s62, s61, 0\n"
"v_mfma_f32_16x16x32_bf16 v[132:135], a[12:15], a[132:135], v[132:135]\n"
"buffer_load_dwordx4 v244, s[28:31], s62 offen lds\n"
"v_mfma_f32_16x16x32_bf16 v[132:135], a[44:47], a[160:163], v[132:135]\n"
"v_mfma_f32_16x16x32_bf16 v[24:27], a[16:19], a[132:135], v[24:27]\n"
"v_mfma_f32_16x16x32_bf16 v[24:27], a[48:51], a[160:163], v[24:27]\n"
"v_mfma_f32_16x16x32_bf16 v[136:139], a[20:23], a[132:135], v[136:139]\n"
"s_add_u32 m0, s66, 33792\n"
"s_add_u32 s62, s61, 36864\n"
"v_mfma_f32_16x16x32_bf16 v[136:139], a[52:55], a[160:163], v[136:139]\n"
"buffer_load_dwordx4 v244, s[28:31], s62 offen lds\n"
"v_mfma_f32_16x16x32_bf16 v[28:31], a[24:27], a[132:135], v[28:31]\n"
"s_waitcnt vmcnt(14)\n"
"ds_read_b64_tr_b16 a[66:67], v247 offset:1024\n"
"v_mfma_f32_16x16x32_bf16 v[28:31], a[56:59], a[160:163], v[28:31]\n"
"s_add_u32 m0, s66, 16384\n"
"s_add_u32 s62, s60, 0\n"
"ds_read_b64_tr_b16 a[70:71], v247 offset:1032\n"
"v_mfma_f32_16x16x32_bf16 v[140:143], a[28:31], a[132:135], v[140:143]\n"
"s_waitcnt lgkmcnt(4)\n"
"buffer_load_dwordx4 v243, s[24:27], s62 offen lds\n"
"v_mfma_f32_16x16x32_bf16 v[140:143], a[60:63], a[160:163], v[140:143]\n"
"ds_read_b64_tr_b16 a[74:75], v247 offset:1280\n"
"v_mfma_f32_16x16x32_bf16 v[32:35], a[0:3], a[136:139], v[32:35]\n"
"s_add_u32 m0, s66, 18432\n"
"s_add_u32 s62, s60, 65536\n"
"ds_read_b128 a[132:135], v226 offset:4096\n"
"v_mfma_f32_16x16x32_bf16 v[32:35], a[32:35], a[164:167], v[32:35]\n"
"buffer_load_dwordx4 v243, s[24:27], s62 offen lds\n"
"v_mfma_f32_16x16x32_bf16 v[144:147], a[4:7], a[136:139], v[144:147]\n"
"ds_read_b128 a[160:163], v227 offset:4096\n"
"v_mfma_f32_16x16x32_bf16 v[144:147], a[36:39], a[164:167], v[144:147]\n"
"s_add_u32 m0, s66, 20480\n"
"s_add_u32 s62, s60, 131072\n"
"ds_read_b64_tr_b16 a[78:79], v247 offset:1288\n"
"v_mfma_f32_16x16x32_bf16 v[36:39], a[8:11], a[136:139], v[36:39]\n"
"buffer_load_dwordx4 v243, s[24:27], s62 offen lds\n"
"v_mfma_f32_16x16x32_bf16 v[36:39], a[40:43], a[164:167], v[36:39]\n"
"ds_read_b64_tr_b16 a[82:83], v247 offset:1536\n"
"v_mfma_f32_16x16x32_bf16 v[148:151], a[12:15], a[136:139], v[148:151]\n"
"ds_read_b64_tr_b16 a[86:87], v247 offset:1544\n"
"v_mfma_f32_16x16x32_bf16 v[148:151], a[44:47], a[164:167], v[148:151]\n"
"ds_read_b64_tr_b16 a[90:91], v247 offset:1792\n"
"v_mfma_f32_16x16x32_bf16 v[40:43], a[16:19], a[136:139], v[40:43]\n"
"s_add_u32 m0, s66, 22528\n"
"s_add_u32 s62, s60, 196608\n"
"ds_read_b64_tr_b16 a[94:95], v247 offset:1800\n"
"v_mfma_f32_16x16x32_bf16 v[40:43], a[48:51], a[164:167], v[40:43]\n"
"buffer_load_dwordx4 v243, s[24:27], s62 offen lds\n"
"v_mfma_f32_16x16x32_bf16 v[152:155], a[20:23], a[136:139], v[152:155]\n"
"v_mfma_f32_16x16x32_bf16 v[152:155], a[52:55], a[164:167], v[152:155]\n"
"v_mfma_f32_16x16x32_bf16 v[44:47], a[24:27], a[136:139], v[44:47]\n"
"v_mfma_f32_16x16x32_bf16 v[44:47], a[56:59], a[164:167], v[44:47]\n"
"s_add_u32 m0, s66, 34816\n"
"s_add_u32 s62, s61, 73728\n"
"v_mfma_f32_16x16x32_bf16 v[156:159], a[28:31], a[136:139], v[156:159]\n"
"buffer_load_dwordx4 v244, s[28:31], s62 offen lds\n"
"v_mfma_f32_16x16x32_bf16 v[156:159], a[60:63], a[164:167], v[156:159]\n"
"v_mfma_f32_16x16x32_bf16 v[48:51], a[0:3], a[140:143], v[48:51]\n"
"ds_read_b128 a[136:139], v248 offset:5120\n"
"v_mfma_f32_16x16x32_bf16 v[48:51], a[32:35], a[168:171], v[48:51]\n"
"v_mfma_f32_16x16x32_bf16 v[160:163], a[4:7], a[140:143], v[160:163]\n"
"s_add_u32 m0, s66, 35840\n"
"s_add_u32 s62, s61, 110592\n"
"ds_read_b128 a[164:167], v225 offset:5120\n"
"v_mfma_f32_16x16x32_bf16 v[160:163], a[36:39], a[168:171], v[160:163]\n"
"buffer_load_dwordx4 v224, s[28:31], s62 offen lds\n"
"v_mfma_f32_16x16x32_bf16 v[52:55], a[8:11], a[140:143], v[52:55]\n"
"s_add_u32 s61, s61, 128\n"
"v_mfma_f32_16x16x32_bf16 v[52:55], a[40:43], a[168:171], v[52:55]\n"
"s_cmp_ge_u32 s61, 1152\n"
"s_cselect_b32 s63, 1152, 0\n"
"v_mfma_f32_16x16x32_bf16 v[164:167], a[12:15], a[140:143], v[164:167]\n"
"s_sub_u32 s61, s61, s63\n"
"v_mfma_f32_16x16x32_bf16 v[164:167], a[44:47], a[168:171], v[164:167]\n"
"s_add_u32 m0, s66, 17408\n"
"s_add_u32 s62, s60, 32768\n"
"v_mfma_f32_16x16x32_bf16 v[56:59], a[16:19], a[140:143], v[56:59]\n"
"s_waitcnt lgkmcnt(2)\n"
"buffer_load_dwordx4 v243, s[24:27], s62 offen lds\n"
"v_mfma_f32_16x16x32_bf16 v[56:59], a[48:51], a[168:171], v[56:59]\n"
"s_waitcnt vmcnt(13)\n"
"ds_read_b64_tr_b16 a[96:97], v247 offset:8192\n"
"v_mfma_f32_16x16x32_bf16 v[168:171], a[20:23], a[140:143], v[168:171]\n"
"s_add_u32 m0, s66, 19456\n"
"s_add_u32 s62, s60, 98304\n"
"ds_read_b64_tr_b16 a[100:101], v247 offset:8200\n"
"v_mfma_f32_16x16x32_bf16 v[168:171], a[52:55], a[168:171], v[168:171]\n"
"buffer_load_dwordx4 v243, s[24:27], s62 offen lds\n"
"v_mfma_f32_16x16x32_bf16 v[60:63], a[24:27], a[140:143], v[60:63]\n"
"ds_read_b64_tr_b16 a[104:105], v247 offset:8448\n"
"v_mfma_f32_16x16x32_bf16 v[60:63], a[56:59], a[168:171], v[60:63]\n"
"ds_read_b64_tr_b16 a[108:109], v247 offset:8456\n"
"v_mfma_f32_16x16x32_bf16 v[172:175], a[28:31], a[140:143], v[172:175]\n"
"ds_read_b64_tr_b16 a[112:113], v247 offset:8704\n"
"v_mfma_f32_16x16x32_bf16 v[172:175], a[60:63], a[168:171], v[172:175]\n"
"ds_read_b64_tr_b16 a[116:117], v247 offset:8712\n"
"v_mfma_f32_16x16x32_bf16 v[64:67], a[0:3], a[144:147], v[64:67]\n"
"s_add_u32 m0, s66, 21504\n"
"s_add_u32 s62, s60, 163840\n"
"ds_read_b128 a[140:143], v226 offset:5120\n"
"v_mfma_f32_16x16x32_bf16 v[64:67], a[32:35], a[172:175], v[64:67]\n"
"buffer_load_dwordx4 v243, s[24:27], s62 offen lds\n"
"v_mfma_f32_16x16x32_bf16 v[176:179], a[4:7], a[144:147], v[176:179]\n"
"ds_read_b128 a[168:171], v227 offset:5120\n"
"v_mfma_f32_16x16x32_bf16 v[176:179], a[36:39], a[172:175], v[176:179]\n"
"ds_read_b64_tr_b16 a[120:121], v247 offset:8960\n"
"v_mfma_f32_16x16x32_bf16 v[68:71], a[8:11], a[144:147], v[68:71]\n"
"ds_read_b64_tr_b16 a[124:125], v247 offset:8968\n"
"v_mfma_f32_16x16x32_bf16 v[68:71], a[40:43], a[172:175], v[68:71]\n"
"s_add_u32 m0, s66, 23552\n"
"s_add_u32 s62, s60, 229376\n"
"v_mfma_f32_16x16x32_bf16 v[180:183], a[12:15], a[144:147], v[180:183]\n"
"buffer_load_dwordx4 v243, s[24:27], s62 offen lds\n"
"v_mfma_f32_16x16x32_bf16 v[180:183], a[44:47], a[172:175], v[180:183]\n"
"s_add_u32 s60, s60, 262144\n"
"v_mfma_f32_16x16x32_bf16 v[72:75], a[16:19], a[144:147], v[72:75]\n"
"s_cmp_ge_u32 s60, 4718592\n"
"s_cselect_b32 s63, 4718592, 0\n"
"v_mfma_f32_16x16x32_bf16 v[72:75], a[48:51], a[172:175], v[72:75]\n"
"s_sub_u32 s60, s60, s63\n"
"v_mfma_f32_16x16x32_bf16 v[184:187], a[20:23], a[144:147], v[184:187]\n"
"v_mfma_f32_16x16x32_bf16 v[184:187], a[52:55], a[172:175], v[184:187]\n"
"v_mfma_f32_16x16x32_bf16 v[76:79], a[24:27], a[144:147], v[76:79]\n"
"s_mov_b32 m0, s86\n"
"v_mfma_f32_16x16x32_bf16 v[76:79], a[56:59], a[172:175], v[76:79]\n"
"buffer_load_dwordx4 v245, s[80:83], s87 offen nt lds\n"
"v_mfma_f32_16x16x32_bf16 v[188:191], a[28:31], a[144:147], v[188:191]\n"
"v_mfma_f32_16x16x32_bf16 v[188:191], a[60:63], a[172:175], v[188:191]\n"
"v_mfma_f32_16x16x32_bf16 v[80:83], a[0:3], a[148:151], v[80:83]\n"
"ds_read_b128 a[144:147], v248 offset:6144\n"
"v_mfma_f32_16x16x32_bf16 v[80:83], a[32:35], a[176:179], v[80:83]\n"
"v_mfma_f32_16x16x32_bf16 v[192:195], a[4:7], a[148:151], v[192:195]\n"
"s_mov_b32 m0, s86\n"
"ds_read_b128 a[172:175], v225 offset:6144\n"
"v_mfma_f32_16x16x32_bf16 v[192:195], a[36:39], a[176:179], v[192:195]\n"
"buffer_load_dwordx4 v246, s[80:83], s87 offen offset:1024 lds\n"
"v_mfma_f32_16x16x32_bf16 v[84:87], a[8:11], a[148:151], v[84:87]\n"
"s_add_u32 s87, s87, 2048\n"
"s_and_b32 s87, s87, s98\n"
"s_waitcnt vmcnt(14)\n"
"ds_read_b64_tr_b16 a[98:99], v247 offset:9216\n"
"v_mfma_f32_16x16x32_bf16 v[84:87], a[40:43], a[176:179], v[84:87]\n"
"s_add_u32 s86, s86, 2048\n"
"ds_read_b64_tr_b16 a[102:103], v247 offset:9224\n"
"v_mfma_f32_16x16x32_bf16 v[196:199], a[12:15], a[148:151], v[196:199]\n"
"ds_read_b64_tr_b16 a[106:107], v247 offset:9472\n"
"v_mfma_f32_16x16x32_bf16 v[196:199], a[44:47], a[176:179], v[196:199]\n"
"ds_read_b64_tr_b16 a[110:111], v247 offset:9480\n"
"v_mfma_f32_16x16x32_bf16 v[88:91], a[16:19], a[148:151], v[88:91]\n"
"s_mov_b32 m0, s86\n"
"ds_read_b64_tr_b16 a[114:115], v247 offset:9728\n"
"s_nop 0\n"
"Lh3:\n"
"s_cbranch_execz Lw0\n"
"v_mfma_f32_16x16x32_bf16 v[88:91], a[48:51], a[176:179], v[88:91]\n"
"buffer_load_dwordx4 v245, s[80:83], s87 offen nt lds\n"
"v_mfma_f32_16x16x32_bf16 v[200:203], a[20:23], a[148:151], v[200:203]\n"
"ds_read_b64_tr_b16 a[118:119], v247 offset:9736\n"
"v_mfma_f32_16x16x32_bf16 v[200:203], a[52:55], a[176:179], v[200:203]\n"
"ds_read_b64_tr_b16 a[122:123], v247 offset:9984\n"
"v_mfma_f32_16x16x32_bf16 v[92:95], a[24:27], a[148:151], v[92:95]\n"
"ds_read_b64_tr_b16 a[126:127], v247 offset:9992\n"
"v_mfma_f32_16x16x32_bf16 v[92:95], a[56:59], a[176:179], v[92:95]\n"
"v_mfma_f32_16x16x32_bf16 v[204:207], a[28:31], a[148:151], v[204:207]\n"
"s_mov_b32 m0, s86\n"
"v_mfma_f32_16x16x32_bf16 v[204:207], a[60:63], a[176:179], v[204:207]\n"
"buffer_load_dwordx4 v246, s[80:83], s87 offen offset:1024 lds\n"
"v_mfma_f32_16x16x32_bf16 v[96:99], a[0:3], a[152:155], v[96:99]\n"
"s_add_u32 s87, s87, 2048\n"
"s_and_b32 s87, s87, s98\n"
"ds_read_b128 a[148:151], v226 offset:6144\n"
"v_mfma_f32_16x16x32_bf16 v[96:99], a[32:35], a[180:183], v[96:99]\n"
"s_add_u32 s86, s86, 2048\n"
"v_mfma_f32_16x16x32_bf16 v[208:211], a[4:7], a[152:155], v[208:211]\n"
"s_add_u32 m0, s66, 24576\n"
"s_add_u32 s62, s60, 0\n"
"ds_read_b128 a[176:179], v227 offset:6144\n"
"v_mfma_f32_16x16x32_bf16 v[208:211], a[36:39], a[180:183], v[208:211]\n"
"s_waitcnt lgkmcnt(12)\n"
"buffer_load_dwordx4 v243, s[24:27], s62 offen lds\n"
"v_mfma_f32_16x16x32_bf16 v[100:103], a[8:11], a[152:155], v[100:103]\n"
"v_mfma_f32_16x16x32_bf16 v[100:103], a[40:43], a[180:183], v[100:103]\n"
"s_add_u32 m0, s66, 26624\n"
"s_add_u32 s62, s60, 65536\n"
"v_mfma_f32_16x16x32_bf16 v[212:215], a[12:15], a[152:155], v[212:215]\n"
"buffer_load_dwordx4 v243, s[24:27], s62 offen lds\n"
"v_mfma_f32_16x16x32_bf16 v[212:215], a[44:47], a[180:183], v[212:215]\n"
"v_mfma_f32_16x16x32_bf16 v[104:107], a[16:19], a[152:155], v[104:107]\n"
"s_add_u32 m0, s66, 28672\n"
"s_add_u32 s62, s60, 131072\n"
"v_mfma_f32_16x16x32_bf16 v[104:107], a[48:51], a[180:183], v[104:107]\n"
"buffer_load_dwordx4 v243, s[24:27], s62 offen lds\n"
"v_mfma_f32_16x16x32_bf16 v[216:219], a[20:23], a[152:155], v[216:219]\n"
"v_mfma_f32_16x16x32_bf16 v[216:219], a[52:55], a[180:183], v[216:219]\n"
"s_add_u32 m0, s66, 30720\n"
"s_add_u32 s62, s60, 196608\n"
"v_mfma_f32_16x16x32_bf16 v[108:111], a[24:27], a[152:155], v[108:111]\n"
"buffer_load_dwordx4 v243, s[24:27], s62 offen lds\n"
"v_mfma_f32_16x16x32_bf16 v[108:111], a[56:59], a[180:183], v[108:111]\n"
"v_mfma_f32_16x16x32_bf16 v[220:223], a[28:31], a[152:155], v[220:223]\n"
"s_add_u32 m0, s66, 25600\n"
"s_add_u32 s62, s60, 32768\n"
"v_mfma_f32_16x16x32_bf16 v[220:223], a[60:63], a[180:183], v[220:223]\n"
"s_waitcnt lgkmcnt(2)\n"
"buffer_load_dwordx4 v243, s[24:27], s62 offen lds\n"
"; qproj iteration 7\n"
"v_mfma_f32_16x16x32_bf16 v[0:3], a[64:67], a[128:131], v[0:3]\n"
"ds_read_b128 a[152:155], v248 offset:7168\n"
"v_mfma_f32_16x16x32_bf16 v[0:3], a[96:99], a[156:159], v[0:3]\n"
"s_waitcnt vmcnt(15)\n"
"ds_read_b64_tr_b16 a[0:1], v247 offset:0\n"
"v_mfma_f32_16x16x32_bf16 v[112:115], a[68:71], a[128:131], v[112:115]\n"
"ds_read_b128 a[180:183], v225 offset:7168\n"
"v_mfma_f32_16x16x32_bf16 v[112:115], a[100:103], a[156:159], v[112:115]\n"
"s_add_u32 m0, s66, 27648\n"
"s_add_u32 s62, s60, 98304\n"
"ds_read_b64_tr_b16 a[4:5], v247 offset:8\n"
"v_mfma_f32_16x16x32_bf16 v[4:7], a[72:75], a[128:131], v[4:7]\n"
"buffer_load_dwordx4 v243, s[24:27], s62 offen lds\n"
"v_mfma_f32_16x16x32_bf16 v[4:7], a[104:107], a[156:159], v[4:7]\n"
"ds_read_b64_tr_b16 a[8:9], v247 offset:256\n"
"v_mfma_f32_16x16x32_bf16 v[116:119], a[76:79], a[128:131], v[116:119]\n"
"ds_read_b64_tr_b16 a[12:13], v247 offset:264\n"
"v_mfma_f32_16x16x32_bf16 v[116:119], a[108:111], a[156:159], v[116:119]\n"
"ds_read_b64_tr_b16 a[16:17], v247 offset:512\n"
"v_mfma_f32_16x16x32_bf16 v[8:11], a[80:83], a[128:131], v[8:11]\n"
"s_add_u32 m0, s66, 29696\n"
"s_add_u32 s62, s60, 163840\n"
"ds_read_b64_tr_b16 a[20:21], v247 offset:520\n"
"v_mfma_f32_16x16x32_bf16 v[8:11], a[112:115], a[156:159], v[8:11]\n"
"buffer_load_dwordx4 v243, s[24:27], s62 offen lds\n"
"v_mfma_f32_16x16x32_bf16 v[120:123], a[84:87], a[128:131], v[120:123]\n"
"ds_read_b64_tr_b16 a[24:25], v247 offset:768\n"
"v_mfma_f32_16x16x32_bf16 v[120:123], a[116:119], a[156:159], v[120:123]\n"
"ds_read_b64_tr_b16 a[28:29], v247 offset:776\n"
"v_mfma_f32_16x16x32_bf16 v[12:15], a[88:91], a[128:131], v[12:15]\n"
"v_mfma_f32_16x16x32_bf16 v[12:15], a[120:123], a[156:159], v[12:15]\n"
"s_waitcnt lgkmcnt(10)\n"
"v_mfma_f32_16x16x32_bf16 v[124:127], a[92:95], a[128:131], v[124:127]\n"
"s_add_u32 m0, s66, 31744\n"
"s_add_u32 s62, s60, 229376\n"
"s_waitcnt vmcnt(15) lgkmcnt(7)\n"
"s_barrier\n"
"v_mfma_f32_16x16x32_bf16 v[124:127], a[124:127], a[156:159], v[124:127]\n"
"buffer_load_dwordx4 v243, s[24:27], s62 offen lds\n"
"v_mfma_f32_16x16x32_bf16 v[16:19], a[64:67], a[132:135], v[16:19]\n"
"ds_read_b128 a[128:131], v248 offset:0\n"
"v_mfma_f32_16x16x32_bf16 v[16:19], a[96:99], a[160:163], v[16:19]\n"
"s_waitcnt vmcnt(12)\n"
"ds_read_b64_tr_b16 a[2:3], v247 offset:1024\n"
"v_mfma_f32_16x16x32_bf16 v[128:131], a[68:71], a[132:135], v[128:131]\n"
"ds_read_b128 a[156:159], v225 offset:0\n"
"v_mfma_f32_16x16x32_bf16 v[128:131], a[100:103], a[160:163], v[128:131]\n"
"ds_read_b64_tr_b16 a[6:7], v247 offset:1032\n"
"v_mfma_f32_16x16x32_bf16 v[20:23], a[72:75], a[132:135], v[20:23]\n"
"ds_read_b64_tr_b16 a[10:11], v247 offset:1280\n"
"v_mfma_f32_16x16x32_bf16 v[20:23], a[104:107], a[160:163], v[20:23]\n"
"ds_read_b64_tr_b16 a[14:15], v247 offset:1288\n"
"v_mfma_f32_16x16x32_bf16 v[132:135], a[76:79], a[132:135], v[132:135]\n"
"ds_read_b64_tr_b16 a[18:19], v247 offset:1536\n"
"v_mfma_f32_16x16x32_bf16 v[132:135], a[108:111], a[160:163], v[132:135]\n"
"ds_read_b64_tr_b16 a[22:23], v247 offset:1544\n"
"v_mfma_f32_16x16x32_bf16 v[24:27], a[80:83], a[132:135], v[24:27]\n"
"ds_read_b64_tr_b16 a[26:27], v247 offset:1792\n"
"v_mfma_f32_16x16x32_bf16 v[24:27], a[112:115], a[160:163], v[24:27]\n"
"s_mov_b32 m0, s86\n"
"ds_read_b64_tr_b16 a[30:31], v247 offset:1800\n"
"v_mfma_f32_16x16x32_bf16 v[136:139], a[84:87], a[132:135], v[136:139]\n"
"buffer_load_dwordx4 v245, s[80:83], s87 offen nt lds\n"
"buffer_load_dwordx4 v246, s[80:83], s87 offen offset:1024 lds\n"
"v_mfma_f32_16x16x32_bf16 v[136:139], a[116:119], a[160:163], v[136:139]\n"
"s_add_u32 s87, s87, 2048\n"
"v_mfma_f32_16x16x32_bf16 v[28:31], a[88:91], a[132:135], v[28:31]\n"
"s_and_b32 s87, s87, s98\n"
"v_mfma_f32_16x16x32_bf16 v[28:31], a[120:123], a[160:163], v[28:31]\n"
"s_add_u32 s86, s86, 2048\n"
"v_mfma_f32_16x16x32_bf16 v[140:143], a[92:95], a[132:135], v[140:143]\n"
"v_mfma_f32_16x16x32_bf16 v[140:143], a[124:127], a[160:163], v[140:143]\n"
"v_mfma_f32_16x16x32_bf16 v[32:35], a[64:67], a[136:139], v[32:35]\n"
"ds_read_b128 a[132:135], v226 offset:0\n"
"v_mfma_f32_16x16x32_bf16 v[32:35], a[96:99], a[164:167], v[32:35]\n"
"v_mfma_f32_16x16x32_bf16 v[144:147], a[68:71], a[136:139], v[144:147]\n"
"ds_read_b128 a[160:163], v227 offset:0\n"
"v_mfma_f32_16x16x32_bf16 v[144:147], a[100:103], a[164:167], v[144:147]\n"
"v_mfma_f32_16x16x32_bf16 v[36:39], a[72:75], a[136:139], v[36:39]\n"
"v_mfma_f32_16x16x32_bf16 v[36:39], a[104:107], a[164:167], v[36:39]\n"
"v_mfma_f32_16x16x32_bf16 v[148:151], a[76:79], a[136:139], v[148:151]\n"
"v_mfma_f32_16x16x32_bf16 v[148:151], a[108:111], a[164:167], v[148:151]\n"
"v_mfma_f32_16x16x32_bf16 v[40:43], a[80:83], a[136:139], v[40:43]\n"
"v_mfma_f32_16x16x32_bf16 v[40:43], a[112:115], a[164:167], v[40:43]\n"
"s_mov_b32 m0, s86\n"
"v_mfma_f32_16x16x32_bf16 v[152:155], a[84:87], a[136:139], v[152:155]\n"
"buffer_load_dwordx4 v245, s[80:83], s87 offen nt lds\n"
"buffer_load_dwordx4 v246, s[80:83], s87 offen offset:1024 lds\n"
"v_mfma_f32_16x16x32_bf16 v[152:155], a[116:119], a[164:167], v[152:155]\n"
"s_add_u32 s87, s87, 2048\n"
"v_mfma_f32_16x16x32_bf16 v[44:47], a[88:91], a[136:139], v[44:47]\n"
"s_and_b32 s87, s87, s98\n"
"v_mfma_f32_16x16x32_bf16 v[44:47], a[120:123], a[164:167], v[44:47]\n"
"s_add_u32 s86, s86, 2048\n"
"v_mfma_f32_16x16x32_bf16 v[156:159], a[92:95], a[136:139], v[156:159]\n"
"v_mfma_f32_16x16x32_bf16 v[156:159], a[124:127], a[164:167], v[156:159]\n"
"v_mfma_f32_16x16x32_bf16 v[48:51], a[64:67], a[140:143], v[48:51]\n"
"ds_read_b128 a[136:139], v248 offset:1024\n"
"v_mfma_f32_16x16x32_bf16 v[48:51], a[96:99], a[168:171], v[48:51]\n"
"v_mfma_f32_16x16x32_bf16 v[160:163], a[68:71], a[140:143], v[160:163]\n"
"ds_read_b128 a[164:167], v225 offset:1024\n"
"v_mfma_f32_16x16x32_bf16 v[160:163], a[100:103], a[168:171], v[160:163]\n"
"v_mfma_f32_16x16x32_bf16 v[52:55], a[72:75], a[140:143], v[52:55]\n"
"v_mfma_f32_16x16x32_bf16 v[52:55], a[104:107], a[168:171], v[52:55]\n"
"v_mfma_f32_16x16x32_bf16 v[164:167], a[76:79], a[140:143], v[164:167]\n"
"s_waitcnt vmcnt(8)\n"
"ds_read_b64_tr_b16 a[32:33], v247 offset:8192\n"
"v_mfma_f32_16x16x32_bf16 v[164:167], a[108:111], a[168:171], v[164:167]\n"
"ds_read_b64_tr_b16 a[36:37], v247 offset:8200\n"
"v_mfma_f32_16x16x32_bf16 v[56:59], a[80:83], a[140:143], v[56:59]\n"
"ds_read_b64_tr_b16 a[40:41], v247 offset:8448\n"
"v_mfma_f32_16x16x32_bf16 v[56:59], a[112:115], a[168:171], v[56:59]\n"
"ds_read_b64_tr_b16 a[44:45], v247 offset:8456\n"
"v_mfma_f32_16x16x32_bf16 v[168:171], a[84:87], a[140:143], v[168:171]\n"
"ds_read_b64_tr_b16 a[48:49], v247 offset:8704\n"
"v_mfma_f32_16x16x32_bf16 v[168:171], a[116:119], a[168:171], v[168:171]\n"
"ds_read_b64_tr_b16 a[52:53], v247 offset:8712\n"
"v_mfma_f32_16x16x32_bf16 v[60:63], a[88:91], a[140:143], v[60:63]\n"
"ds_read_b64_tr_b16 a[56:57], v247 offset:8960\n"
"v_mfma_f32_16x16x32_bf16 v[60:63], a[120:123], a[168:171], v[60:63]\n"
"ds_read_b64_tr_b16 a[60:61], v247 offset:8968\n"
"v_mfma_f32_16x16x32_bf16 v[172:175], a[92:95], a[140:143], v[172:175]\n"
"v_mfma_f32_16x16x32_bf16 v[172:175], a[124:127], a[168:171], v[172:175]\n"
"v_mfma_f32_16x16x32_bf16 v[64:67], a[64:67], a[144:147], v[64:67]\n"
"ds_read_b128 a[140:143], v226 offset:1024\n"
"v_mfma_f32_16x16x32_bf16 v[64:67], a[96:99], a[172:175], v[64:67]\n"
"v_mfma_f32_16x16x32_bf16 v[176:179], a[68:71], a[144:147], v[176:179]\n"
"s_mov_b32 m0, s86\n"
"ds_read_b128 a[168:171], v227 offset:1024\n"
"v_mfma_f32_16x16x32_bf16 v[176:179], a[100:103], a[172:175], v[176:179]\n"
"s_waitcnt lgkmcnt(14)\n"
"buffer_load_dwordx4 v245, s[80:83], s87 offen nt lds\n"
"buffer_load_dwordx4 v246, s[80:83], s87 offen offset:1024 lds\n"
"v_mfma_f32_16x16x32_bf16 v[68:71], a[72:75], a[144:147], v[68:71]\n"
"s_add_u32 s87, s87, 2048\n"
"v_mfma_f32_16x16x32_bf16 v[68:71], a[104:107], a[172:175], v[68:71]\n"
"s_and_b32 s87, s87, s98\n"
"v_mfma_f32_16x16x32_bf16 v[180:183], a[76:79], a[144:147], v[180:183]\n"
"s_add_u32 s86, s86, 2048\n"
"v_mfma_f32_16x16x32_bf16 v[180:183], a[108:111], a[172:175], v[180:183]\n"
"v_mfma_f32_16x16x32_bf16 v[72:75], a[80:83], a[144:147], v[72:75]\n"
"v_mfma_f32_16x16x32_bf16 v[72:75], a[112:115], a[172:175], v[72:75]\n"
"s_waitcnt vmcnt(6)\n"
"ds_read_b64_tr_b16 a[34:35], v247 offset:9216\n"
"v_mfma_f32_16x16x32_bf16 v[184:187], a[84:87], a[144:147], v[184:187]\n"
"ds_read_b64_tr_b16 a[38:39], v247 offset:9224\n"
"v_mfma_f32_16x16x32_bf16 v[184:187], a[116:119], a[172:175], v[184:187]\n"
"ds_read_b64_tr_b16 a[42:43], v247 offset:9472\n"
"v_mfma_f32_16x16x32_bf16 v[76:79], a[88:91], a[144:147], v[76:79]\n"
"ds_read_b64_tr_b16 a[46:47], v247 offset:9480\n"
"v_mfma_f32_16x16x32_bf16 v[76:79], a[120:123], a[172:175], v[76:79]\n"
"ds_read_b64_tr_b16 a[50:51], v247 offset:9728\n"
"v_mfma_f32_16x16x32_bf16 v[188:191], a[92:95], a[144:147], v[188:191]\n"
"ds_read_b64_tr_b16 a[54:55], v247 offset:9736\n"
"v_mfma_f32_16x16x32_bf16 v[188:191], a[124:127], a[172:175], v[188:191]\n"
"ds_read_b64_tr_b16 a[58:59], v247 offset:9984\n"
"v_mfma_f32_16x16x32_bf16 v[80:83], a[64:67], a[148:151], v[80:83]\n"
"ds_read_b128 a[144:147], v248 offset:2048\n"
"v_mfma_f32_16x16x32_bf16 v[80:83], a[96:99], a[176:179], v[80:83]\n"
"ds_read_b64_tr_b16 a[62:63], v247 offset:9992\n"
"v_mfma_f32_16x16x32_bf16 v[192:195], a[68:71], a[148:151], v[192:195]\n"
"ds_read_b128 a[172:175], v225 offset:2048\n"
"v_mfma_f32_16x16x32_bf16 v[192:195], a[100:103], a[176:179], v[192:195]\n"
"v_mfma_f32_16x16x32_bf16 v[84:87], a[72:75], a[148:151], v[84:87]\n"
"v_mfma_f32_16x16x32_bf16 v[84:87], a[104:107], a[176:179], v[84:87]\n"
"v_mfma_f32_16x16x32_bf16 v[196:199], a[76:79], a[148:151], v[196:199]\n"
"v_mfma_f32_16x16x32_bf16 v[196:199], a[108:111], a[176:179], v[196:199]\n"
"v_mfma_f32_16x16x32_bf16 v[88:91], a[80:83], a[148:151], v[88:91]\n"
"v_mfma_f32_16x16x32_bf16 v[88:91], a[112:115], a[176:179], v[88:91]\n"
"s_mov_b32 m0, s86\n"
"v_mfma_f32_16x16x32_bf16 v[200:203], a[84:87], a[148:151], v[200:203]\n"
"buffer_load_dwordx4 v245, s[80:83], s87 offen nt lds\n"
"buffer_load_dwordx4 v246, s[80:83], s87 offen offset:1024 lds\n"
"v_mfma_f32_16x16x32_bf16 v[200:203], a[116:119], a[176:179], v[200:203]\n"
"s_add_u32 s87, s87, 2048\n"
"v_mfma_f32_16x16x32_bf16 v[92:95], a[88:91], a[148:151], v[92:95]\n"
"s_and_b32 s87, s87, s98\n"
"v_mfma_f32_16x16x32_bf16 v[92:95], a[120:123], a[176:179], v[92:95]\n"
"s_add_u32 s86, s86, 2048\n"
"v_mfma_f32_16x16x32_bf16 v[204:207], a[92:95], a[148:151], v[204:207]\n"
"v_mfma_f32_16x16x32_bf16 v[204:207], a[124:127], a[176:179], v[204:207]\n"
"v_mfma_f32_16x16x32_bf16 v[96:99], a[64:67], a[152:155], v[96:99]\n"
"ds_read_b128 a[148:151], v226 offset:2048\n"
"v_mfma_f32_16x16x32_bf16 v[96:99], a[96:99], a[180:183], v[96:99]\n"
"v_mfma_f32_16x16x32_bf16 v[208:211], a[68:71], a[152:155], v[208:211]\n"
"ds_read_b128 a[176:179], v227 offset:2048\n"
"v_mfma_f32_16x16x32_bf16 v[208:211], a[100:103], a[180:183], v[208:211]\n"
"v_mfma_f32_16x16x32_bf16 v[100:103], a[72:75], a[152:155], v[100:103]\n"
"v_mfma_f32_16x16x32_bf16 v[100:103], a[104:107], a[180:183], v[100:103]\n"
"v_mfma_f32_16x16x32_bf16 v[212:215], a[76:79], a[152:155], v[212:215]\n"
"v_mfma_f32_16x16x32_bf16 v[212:215], a[108:111], a[180:183], v[212:215]\n"
"v_mfma_f32_16x16x32_bf16 v[104:107], a[80:83], a[152:155], v[104:107]\n"
"v_mfma_f32_16x16x32_bf16 v[104:107], a[112:115], a[180:183], v[104:107]\n"
"v_mfma_f32_16x16x32_bf16 v[216:219], a[84:87], a[152:155], v[216:219]\n"
"v_mfma_f32_16x16x32_bf16 v[216:219], a[116:119], a[180:183], v[216:219]\n"
"v_mfma_f32_16x16x32_bf16 v[108:111], a[88:91], a[152:155], v[108:111]\n"
"v_mfma_f32_16x16x32_bf16 v[108:111], a[120:123], a[180:183], v[108:111]\n"
"v_mfma_f32_16x16x32_bf16 v[220:223], a[92:95], a[152:155], v[220:223]\n"
"v_mfma_f32_16x16x32_bf16 v[220:223], a[124:127], a[180:183], v[220:223]\n"
"; qproj final section\n"
"s_waitcnt lgkmcnt(3)\n"
"v_mfma_f32_16x16x32_bf16 v[0:3], a[0:3], a[128:131], v[0:3]\n"
"ds_read_b128 a[152:155], v248 offset:3072\n"
"v_mfma_f32_16x16x32_bf16 v[0:3], a[32:35], a[156:159], v[0:3]\n"
"v_mfma_f32_16x16x32_bf16 v[112:115], a[4:7], a[128:131], v[112:115]\n"
"s_mov_b32 m0, s86\n"
"ds_read_b128 a[180:183], v225 offset:3072\n"
"v_mfma_f32_16x16x32_bf16 v[112:115], a[36:39], a[156:159], v[112:115]\n"
"buffer_load_dwordx4 v245, s[80:83], s87 offen nt lds\n"
"buffer_load_dwordx4 v246, s[80:83], s87 offen offset:1024 lds\n"
"v_mfma_f32_16x16x32_bf16 v[4:7], a[8:11], a[128:131], v[4:7]\n"
"s_add_u32 s87, s87, 2048\n"
"v_bfe_u32 v248, %[tA], 4, 3\n"
"v_subrev_u32 v244, s38, v248\n"
"v_mfma_f32_16x16x32_bf16 v[4:7], a[40:43], a[156:159], v[4:7]\n"
"s_and_b32 s87, s87, s98\n"
"v_bfe_u32 v244, v244, 31, 1\n"
"v_sub_u32 v248, v248, v244\n"
"v_mfma_f32_16x16x32_bf16 v[116:119], a[12:15], a[128:131], v[116:119]\n"
"s_add_u32 s86, s86, 2048\n"
"v_lshrrev_b32 v244, 7, %[tA]\n"
"v_mul_u32_u24 v244, 1152, v244\n"
"v_mfma_f32_16x16x32_bf16 v[116:119], a[44:47], a[156:159], v[116:119]\n"
"v_lshl_add_u32 v244, v248, 7, v244\n"
"v_add_u32 v244, 128, v244\n"
"v_mfma_f32_16x16x32_bf16 v[8:11], a[16:19], a[128:131], v[8:11]\n"
"v_xor_b32 v247, 32, v253\n"
"v_lshlrev_b32 v243, 3, %[tA]\n"
"v_mfma_f32_16x16x32_bf16 v[8:11], a[48:51], a[156:159], v[8:11]\n"
"s_waitcnt lgkmcnt(0)\n"
"s_barrier\n"
"s_mov_b32 s84, 6\n"
"v_mfma_f32_16x16x32_bf16 v[120:123], a[20:23], a[128:131], v[120:123]\n"
"v_mul_f32 v0, s65, v0\n"
"v_mul_f32 v1, s65, v1\n"
"v_mfma_f32_16x16x32_bf16 v[120:123], a[52:55], a[156:159], v[120:123]\n"
"v_mul_f32 v2, s65, v2\n"
"v_mul_f32 v3, s65, v3\n"
"v_mfma_f32_16x16x32_bf16 v[12:15], a[24:27], a[128:131], v[12:15]\n"
"v_mul_f32 v112, s65, v112\n"
"v_mul_f32 v113, s65, v113\n"
"v_mfma_f32_16x16x32_bf16 v[12:15], a[56:59], a[156:159], v[12:15]\n"
"v_mul_f32 v114, s65, v114\n"
"v_mul_f32 v115, s65, v115\n"
"v_mfma_f32_16x16x32_bf16 v[124:127], a[28:31], a[128:131], v[124:127]\n"
"v_cvt_pk_bf16_f32 v0, v0, v1\n"
"v_cvt_pk_bf16_f32 v1, v2, v3\n"
"v_mfma_f32_16x16x32_bf16 v[124:127], a[60:63], a[156:159], v[124:127]\n"
"v_cvt_pk_bf16_f32 v2, v112, v113\n"
"v_cvt_pk_bf16_f32 v3, v114, v115\n"
"v_mfma_f32_16x16x32_bf16 v[16:19], a[0:3], a[132:135], v[16:19]\n"
"v_mul_f32 v4, s65, v4\n"
"v_mul_f32 v5, s65, v5\n"
"v_mfma_f32_16x16x32_bf16 v[16:19], a[32:35], a[160:163], v[16:19]\n"
"v_mul_f32 v6, s65, v6\n"
"v_mul_f32 v7, s65, v7\n"
"v_mfma_f32_16x16x32_bf16 v[128:131], a[4:7], a[132:135], v[128:131]\n"
"v_mul_f32 v116, s65, v116\n"
"v_mul_f32 v117, s65, v117\n"
"v_mfma_f32_16x16x32_bf16 v[128:131], a[36:39], a[160:163], v[128:131]\n"
"v_mul_f32 v118, s65, v118\n"
"v_mul_f32 v119, s65, v119\n"
"v_mfma_f32_16x16x32_bf16 v[20:23], a[8:11], a[132:135], v[20:23]\n"
"v_cvt_pk_bf16_f32 v4, v4, v5\n"
"v_cvt_pk_bf16_f32 v5, v6, v7\n"
"v_mfma_f32_16x16x32_bf16 v[20:23], a[40:43], a[160:163], v[20:23]\n"
"v_cvt_pk_bf16_f32 v6, v116, v117\n"
"v_cvt_pk_bf16_f32 v7, v118, v119\n"
"v_mfma_f32_16x16x32_bf16 v[132:135], a[12:15], a[132:135], v[132:135]\n"
"v_mul_f32 v8, s65, v8\n"
"v_mul_f32 v9, s65, v9\n"
"v_mfma_f32_16x16x32_bf16 v[132:135], a[44:47], a[160:163], v[132:135]\n"
"v_mul_f32 v10, s65, v10\n"
"v_mul_f32 v11, s65, v11\n"
"v_mfma_f32_16x16x32_bf16 v[24:27], a[16:19], a[132:135], v[24:27]\n"
"v_mul_f32 v120, s65, v120\n"
"v_mul_f32 v121, s65, v121\n"
"v_mfma_f32_16x16x32_bf16 v[24:27], a[48:51], a[160:163], v[24:27]\n"
"s_mov_b32 m0, s86\n"
"v_mul_f32 v122, s65, v122\n"
"v_mul_f32 v123, s65, v123\n"
"v_mfma_f32_16x16x32_bf16 v[136:139], a[20:23], a[132:135], v[136:139]\n"
"buffer_load_dwordx4 v245, s[80:83], s87 offen nt lds\n"
"buffer_load_dwordx4 v246, s[80:83], s87 offen offset:1024 lds\n"
"v_mfma_f32_16x16x32_bf16 v[136:139], a[52:55], a[160:163], v[136:139]\n"
"s_add_u32 s87, s87, 2048\n"
"v_cvt_pk_bf16_f32 v8, v8, v9\n"
"v_cvt_pk_bf16_f32 v9, v10, v11\n"
"v_mfma_f32_16x16x32_bf16 v[28:31], a[24:27], a[132:135], v[28:31]\n"
"s_and_b32 s87, s87, s98\n"
"v_cvt_pk_bf16_f32 v10, v120, v121\n"
"v_cvt_pk_bf16_f32 v11, v122, v123\n"
"v_mfma_f32_16x16x32_bf16 v[28:31], a[56:59], a[160:163], v[28:31]\n"
"s_add_u32 s86, s86, 2048\n"
"v_mul_f32 v12, s65, v12\n"
"v_mul_f32 v13, s65, v13\n"
"v_mfma_f32_16x16x32_bf16 v[140:143], a[28:31], a[132:135], v[140:143]\n"
"v_mul_f32 v14, s65, v14\n"
"v_mul_f32 v15, s65, v15\n"
"v_mfma_f32_16x16x32_bf16 v[140:143], a[60:63], a[160:163], v[140:143]\n"
"v_mul_f32 v124, s65, v124\n"
"v_mul_f32 v125, s65, v125\n"
"v_mfma_f32_16x16x32_bf16 v[32:35], a[0:3], a[136:139], v[32:35]\n"
"v_mul_f32 v126, s65, v126\n"
"v_mul_f32 v127, s65, v127\n"
"v_mfma_f32_16x16x32_bf16 v[32:35], a[32:35], a[164:167], v[32:35]\n"
"v_cvt_pk_bf16_f32 v12, v12, v13\n"
"v_cvt_pk_bf16_f32 v13, v14, v15\n"
"v_mfma_f32_16x16x32_bf16 v[144:147], a[4:7], a[136:139], v[144:147]\n"
"v_cvt_pk_bf16_f32 v14, v124, v125\n"
"v_cvt_pk_bf16_f32 v15, v126, v127\n"
"v_mul_f32 v16, s65, v16\n"
"v_mfma_f32_16x16x32_bf16 v[144:147], a[36:39], a[164:167], v[144:147]\n"
"v_mul_f32 v17, s65, v17\n"
"v_mul_f32 v18, s65, v18\n"
"v_mul_f32 v19, s65, v19\n"
"v_mfma_f32_16x16x32_bf16 v[36:39], a[8:11], a[136:139], v[36:39]\n"
"v_mul_f32 v128, s65, v128\n"
"v_mul_f32 v129, s65, v129\n"
"v_mul_f32 v130, s65, v130\n"
"v_mfma_f32_16x16x32_bf16 v[36:39], a[40:43], a[164:167], v[36:39]\n"
"v_mul_f32 v131, s65, v131\n"
"v_cvt_pk_bf16_f32 v16, v16, v17\n"
"v_cvt_pk_bf16_f32 v17, v18, v19\n"
"v_mfma_f32_16x16x32_bf16 v[148:151], a[12:15], a[136:139], v[148:151]\n"
"v_cvt_pk_bf16_f32 v18, v128, v129\n"
"v_cvt_pk_bf16_f32 v19, v130, v131\n"
"v_mul_f32 v20, s65, v20\n"
"v_mfma_f32_16x16x32_bf16 v[148:151], a[44:47], a[164:167], v[148:151]\n"
"v_mul_f32 v21, s65, v21\n"
"v_mul_f32 v22, s65, v22\n"
"v_mul_f32 v23, s65, v23\n"
"v_mfma_f32_16x16x32_bf16 v[40:43], a[16:19], a[136:139], v[40:43]\n"
"v_mul_f32 v132, s65, v132\n"
"v_mul_f32 v133, s65, v133\n"
"v_mul_f32 v134, s65, v134\n"
"v_mfma_f32_16x16x32_bf16 v[40:43], a[48:51], a[164:167], v[40:43]\n"
"v_mul_f32 v135, s65, v135\n"
"v_cvt_pk_bf16_f32 v20, v20, v21\n"
"v_cvt_pk_bf16_f32 v21, v22, v23\n"
"v_mfma_f32_16x16x32_bf16 v[152:155], a[20:23], a[136:139], v[152:155]\n"
"v_cvt_pk_bf16_f32 v22, v132, v133\n"
"v_cvt_pk_bf16_f32 v23, v134, v135\n"
"v_mul_f32 v24, s65, v24\n"
"v_mfma_f32_16x16x32_bf16 v[152:155], a[52:55], a[164:167], v[152:155]\n"
"v_mul_f32 v25, s65, v25\n"
"v_mul_f32 v26, s65, v26\n"
"v_mul_f32 v27, s65, v27\n"
"v_mfma_f32_16x16x32_bf16 v[44:47], a[24:27], a[136:139], v[44:47]\n"
"v_mul_f32 v136, s65, v136\n"
"v_mul_f32 v137, s65, v137\n"
"v_mul_f32 v138, s65, v138\n"
"v_mfma_f32_16x16x32_bf16 v[44:47], a[56:59], a[164:167], v[44:47]\n"
"v_mul_f32 v139, s65, v139\n"
"v_cvt_pk_bf16_f32 v24, v24, v25\n"
"v_cvt_pk_bf16_f32 v25, v26, v27\n"
"v_mfma_f32_16x16x32_bf16 v[156:159], a[28:31], a[136:139], v[156:159]\n"
"v_cvt_pk_bf16_f32 v26, v136, v137\n"
"v_cvt_pk_bf16_f32 v27, v138, v139\n"
"v_mul_f32 v28, s65, v28\n"
"v_mfma_f32_16x16x32_bf16 v[156:159], a[60:63], a[164:167], v[156:159]\n"
"v_mul_f32 v29, s65, v29\n"
"v_mul_f32 v30, s65, v30\n"
"v_mul_f32 v31, s65, v31\n"
"v_mfma_f32_16x16x32_bf16 v[224:227], a[224:227], v[0:3], 0\n"
"v_mul_f32 v140, s65, v140\n"
"v_mul_f32 v141, s65, v141\n"
"v_mul_f32 v142, s65, v142\n"
"v_mfma_f32_16x16x32_bf16 v[224:227], a[228:231], v[4:7], v[224:227]\n"
"s_mov_b32 m0, s86\n"
"v_mul_f32 v143, s65, v143\n"
"v_cvt_pk_bf16_f32 v28, v28, v29\n"
"v_cvt_pk_bf16_f32 v29, v30, v31\n"
"v_mfma_f32_16x16x32_bf16 v[224:227], a[232:235], v[8:11], v[224:227]\n"
"buffer_load_dwordx4 v245, s[80:83], s87 offen nt lds\n"
"buffer_load_dwordx4 v246, s[80:83], s87 offen offset:1024 lds\n"
"v_mfma_f32_16x16x32_bf16 v[224:227], a[236:239], v[12:15], v[224:227]\n"
"s_add_u32 s87, s87, 2048\n"
"v_cvt_pk_bf16_f32 v30, v140, v141\n"
"v_cvt_pk_bf16_f32 v31, v142, v143\n"
"v_mul_f32 v32, s65, v32\n"
"v_mfma_f32_16x16x32_bf16 v[48:51], a[0:3], a[140:143], v[48:51]\n"
"s_and_b32 s87, s87, s98\n"
"v_mul_f32 v33, s65, v33\n"
"v_mul_f32 v34, s65, v34\n"
"v_mul_f32 v35, s65, v35\n"
"v_mfma_f32_16x16x32_bf16 v[48:51], a[32:35], a[168:171], v[48:51]\n"
"s_add_u32 s86, s86, 2048\n"
"v_mul_f32 v144, s65, v144\n"
"v_mul_f32 v145, s65, v145\n"
"v_mul_f32 v146, s65, v146\n"
"v_mfma_f32_16x16x32_bf16 v[160:163], a[4:7], a[140:143], v[160:163]\n"
"v_mul_f32 v147, s65, v147\n"
"v_cvt_pk_bf16_f32 v32, v32, v33\n"
"v_cvt_pk_bf16_f32 v33, v34, v35\n"
"v_mfma_f32_16x16x32_bf16 v[160:163], a[36:39], a[168:171], v[160:163]\n"
"v_cvt_pk_bf16_f32 v34, v144, v145\n"
"v_cvt_pk_bf16_f32 v35, v146, v147\n"
"v_mul_f32 v36, s65, v36\n"
"v_mfma_f32_16x16x32_bf16 v[52:55], a[8:11], a[140:143], v[52:55]\n"
"v_max3_f32 v240, v224, v225, v226\n"
"v_max_f32 v240, v240, v227\n"
"v_mul_f32 v37, s65, v37\n"
"v_mfma_f32_16x16x32_bf16 v[52:55], a[40:43], a[168:171], v[52:55]\n"
"v_mul_f32 v38, s65, v38\n"
"v_mul_f32 v39, s65, v39\n"
"v_mul_f32 v148, s65, v148\n"
"v_mfma_f32_16x16x32_bf16 v[164:167], a[12:15], a[140:143], v[164:167]\n"
"v_mul_f32 v149, s65, v149\n"
"v_mul_f32 v150, s65, v150\n"
"v_mul_f32 v151, s65, v151\n"
"v_mfma_f32_16x16x32_bf16 v[164:167], a[44:47], a[168:171], v[164:167]\n"
"v_cvt_pk_bf16_f32 v36, v36, v37\n"
"v_cvt_pk_bf16_f32 v37, v38, v39\n"
"v_cvt_pk_bf16_f32 v38, v148, v149\n"
"v_mfma_f32_16x16x32_bf16 v[56:59], a[16:19], a[140:143], v[56:59]\n"
"v_cvt_pk_bf16_f32 v39, v150, v151\n"
"v_mul_f32 v40, s65, v40\n"
"v_mul_f32 v41, s65, v41\n"
"v_mfma_f32_16x16x32_bf16 v[56:59], a[48:51], a[168:171], v[56:59]\n"
"v_mul_f32 v42, s65, v42\n"
"v_mul_f32 v43, s65, v43\n"
"v_mul_f32 v152, s65, v152\n"
"v_mfma_f32_16x16x32_bf16 v[168:171], a[20:23], a[140:143], v[168:171]\n"
"v_mul_f32 v153, s65, v153\n"
"v_mul_f32 v154, s65, v154\n"
"v_mul_f32 v155, s65, v155\n"
"v_mfma_f32_16x16x32_bf16 v[168:171], a[52:55], a[168:171], v[168:171]\n"
"v_cvt_pk_bf16_f32 v40, v40, v41\n"
"v_cvt_pk_bf16_f32 v41, v42, v43\n"
"v_cvt_pk_bf16_f32 v42, v152, v153\n"
"v_mfma_f32_16x16x32_bf16 v[60:63], a[24:27], a[140:143], v[60:63]\n"
"v_cvt_pk_bf16_f32 v43, v154, v155\n"
"v_mul_f32 v44, s65, v44\n"
"v_mul_f32 v45, s65, v45\n"
"v_mfma_f32_16x16x32_bf16 v[60:63], a[56:59], a[168:171], v[60:63]\n"
"v_mul_f32 v46, s65, v46\n"
"v_mul_f32 v47, s65, v47\n"
"v_mul_f32 v156, s65, v156\n"
"v_mfma_f32_16x16x32_bf16 v[172:175], a[28:31], a[140:143], v[172:175]\n"
"v_mul_f32 v157, s65, v157\n"
"v_mul_f32 v158, s65, v158\n"
"v_mul_f32 v159, s65, v159\n"
"v_mfma_f32_16x16x32_bf16 v[172:175], a[60:63], a[168:171], v[172:175]\n"
"v_cvt_pk_bf16_f32 v44, v44, v45\n"
"v_cvt_pk_bf16_f32 v45, v46, v47\n"
"v_cvt_pk_bf16_f32 v46, v156, v157\n"
"v_mfma_f32_16x16x32_bf16 v[232:235], a[224:227], v[16:19], 0\n"
"v_cvt_pk_bf16_f32 v47, v158, v159\n"
"v_mul_f32 v48, s65, v48\n"
"v_mfma_f32_16x16x32_bf16 v[232:235], a[228:231], v[20:23], v[232:235]\n"
"v_mul_f32 v49, s65, v49\n"
"v_mul_f32 v50, s65, v50\n"
"v_mfma_f32_16x16x32_bf16 v[232:235], a[232:235], v[24:27], v[232:235]\n"
"v_mul_f32 v51, s65, v51\n"
"v_mul_f32 v160, s65, v160\n"
"v_mfma_f32_16x16x32_bf16 v[232:235], a[236:239], v[28:31], v[232:235]\n"
"v_mul_f32 v161, s65, v161\n"
"v_mul_f32 v162, s65, v162\n"
"v_mfma_f32_16x16x32_bf16 v[64:67], a[0:3], a[144:147], v[64:67]\n"
"v_mul_f32 v163, s65, v163\n"
"v_cvt_pk_bf16_f32 v48, v48, v49\n"
"v_mfma_f32_16x16x32_bf16 v[64:67], a[32:35], a[172:175], v[64:67]\n"
"s_mov_b32 m0, s86\n"
"v_cvt_pk_bf16_f32 v49, v50, v51\n"
"v_cvt_pk_bf16_f32 v50, v160, v161\n"
"v_mfma_f32_16x16x32_bf16 v[176:179], a[4:7], a[144:147], v[176:179]\n"
"buffer_load_dwordx4 v245, s[80:83], s87 offen nt lds\n"
"buffer_load_dwordx4 v246, s[80:83], s87 offen offset:1024 lds\n"
"v_mfma_f32_16x16x32_bf16 v[176:179], a[36:39], a[172:175], v[176:179]\n"
"s_add_u32 s87, s87, 2048\n"
"v_cvt_pk_bf16_f32 v51, v162, v163\n"
"v_mul_f32 v52, s65, v52\n"
"v_mul_f32 v53, s65, v53\n"
"v_mfma_f32_16x16x32_bf16 v[68:71], a[8:11], a[144:147], v[68:71]\n"
"s_and_b32 s87, s87, s98\n"
"v_max3_f32 v240, v240, v232, v233\n"
"v_max3_f32 v240, v240, v234, v235\n"
"v_mul_f32 v54, s65, v54\n"
"v_mfma_f32_16x16x32_bf16 v[68:71], a[40:43], a[172:175], v[68:71]\n"
"s_add_u32 s86, s86, 2048\n"
"v_mul_f32 v55, s65, v55\n"
"v_mul_f32 v164, s65, v164\n"
"v_mul_f32 v165, s65, v165\n"
"v_mfma_f32_16x16x32_bf16 v[180:183], a[12:15], a[144:147], v[180:183]\n"
"v_mul_f32 v166, s65, v166\n"
"v_mul_f32 v167, s65, v167\n"
"v_cvt_pk_bf16_f32 v52, v52, v53\n"
"v_mfma_f32_16x16x32_bf16 v[180:183], a[44:47], a[172:175], v[180:183]\n"
"v_cvt_pk_bf16_f32 v53, v54, v55\n"
"v_cvt_pk_bf16_f32 v54, v164, v165\n"
"v_cvt_pk_bf16_f32 v55, v166, v167\n"
"v_mfma_f32_16x16x32_bf16 v[72:75], a[16:19], a[144:147], v[72:75]\n"
"v_mul_f32 v56, s65, v56\n"
"v_mul_f32 v57, s65, v57\n"
"v_mul_f32 v58, s65, v58\n"
"v_mfma_f32_16x16x32_bf16 v[72:75], a[48:51], a[172:175], v[72:75]\n"
"v_mul_f32 v59, s65, v59\n"
"v_mul_f32 v168, s65, v168\n"
"v_mul_f32 v169, s65, v169\n"
"v_mfma_f32_16x16x32_bf16 v[184:187], a[20:23], a[144:147], v[184:187]\n"
"v_mul_f32 v170, s65, v170\n"
"v_mul_f32 v171, s65, v171\n"
"v_cvt_pk_bf16_f32 v56, v56, v57\n"
"v_mfma_f32_16x16x32_bf16 v[184:187], a[52:55], a[172:175], v[184:187]\n"
"v_cvt_pk_bf16_f32 v57, v58, v59\n"
"v_cvt_pk_bf16_f32 v58, v168, v169\n"
"v_cvt_pk_bf16_f32 v59, v170, v171\n"
"v_mfma_f32_16x16x32_bf16 v[76:79], a[24:27], a[144:147], v[76:79]\n"
"v_mul_f32 v60, s65, v60\n"
"v_mul_f32 v61, s65, v61\n"
"v_mul_f32 v62, s65, v62\n"
"v_mfma_f32_16x16x32_bf16 v[76:79], a[56:59], a[172:175], v[76:79]\n"
"v_mul_f32 v63, s65, v63\n"
"v_mul_f32 v172, s65, v172\n"
"v_mul_f32 v173, s65, v173\n"
"v_mfma_f32_16x16x32_bf16 v[188:191], a[28:31], a[144:147], v[188:191]\n"
"v_mul_f32 v174, s65, v174\n"
"v_mul_f32 v175, s65, v175\n"
"v_cvt_pk_bf16_f32 v60, v60, v61\n"
"v_mfma_f32_16x16x32_bf16 v[188:191], a[60:63], a[172:175], v[188:191]\n"
"v_cvt_pk_bf16_f32 v61, v62, v63\n"
"v_cvt_pk_bf16_f32 v62, v172, v173\n"
"v_cvt_pk_bf16_f32 v63, v174, v175\n"
"v_mfma_f32_16x16x32_bf16 v[224:227], a[224:227], v[32:35], 0\n"
"v_mul_f32 v64, s65, v64\n"
"v_mul_f32 v65, s65, v65\n"
"v_mfma_f32_16x16x32_bf16 v[224:227], a[228:231], v[36:39], v[224:227]\n"
"v_mul_f32 v66, s65, v66\n"
"v_mul_f32 v67, s65, v67\n"
"v_mfma_f32_16x16x32_bf16 v[224:227], a[232:235], v[40:43], v[224:227]\n"
"v_mul_f32 v176, s65, v176\n"
"v_mul_f32 v177, s65, v177\n"
"v_mfma_f32_16x16x32_bf16 v[224:227], a[236:239], v[44:47], v[224:227]\n"
"v_mul_f32 v178, s65, v178\n"
"v_mul_f32 v179, s65, v179\n"
"v_mfma_f32_16x16x32_bf16 v[80:83], a[0:3], a[148:151], v[80:83]\n"
"v_cvt_pk_bf16_f32 v64, v64, v65\n"
"v_cvt_pk_bf16_f32 v65, v66, v67\n"
"v_mfma_f32_16x16x32_bf16 v[80:83], a[32:35], a[176:179], v[80:83]\n"
"v_cvt_pk_bf16_f32 v66, v176, v177\n"
"v_cvt_pk_bf16_f32 v67, v178, v179\n"
"v_mfma_f32_16x16x32_bf16 v[192:195], a[4:7], a[148:151], v[192:195]\n"
"v_mul_f32 v68, s65, v68\n"
"v_mul_f32 v69, s65, v69\n"
"v_mfma_f32_16x16x32_bf16 v[192:195], a[36:39], a[176:179], v[192:195]\n"
"v_mul_f32 v70, s65, v70\n"
"v_mul_f32 v71, s65, v71\n"
"v_mfma_f32_16x16x32_bf16 v[84:87], a[8:11], a[148:151], v[84:87]\n"
"v_max3_f32 v240, v240, v224, v225\n"
"v_max3_f32 v240, v240, v226, v227\n"
"Lh4:\n"
"s_cbranch_execz Lw1\n"
"v_mfma_f32_16x16x32_bf16 v[84:87], a[40:43], a[176:179], v[84:87]\n"
"s_mov_b32 m0, s86\n"
"v_mul_f32 v180, s65, v180\n"
"v_mul_f32 v181, s65, v181\n"
"v_mfma_f32_16x16x32_bf16 v[196:199], a[12:15], a[148:151], v[196:199]\n"
"buffer_load_dwordx4 v245, s[80:83], s87 offen nt lds\n"
"buffer_load_dwordx4 v246, s[80:83], s87 offen offset:1024 lds\n"
"v_mfma_f32_16x16x32_bf16 v[196:199], a[44:47], a[176:179], v[196:199]\n"
"s_add_u32 s87, s87, 2048\n"
"v_mul_f32 v182, s65, v182\n"
"v_mul_f32 v183, s65, v183\n"
"v_mfma_f32_16x16x32_bf16 v[88:91], a[16:19], a[148:151], v[88:91]\n"
"s_and_b32 s87, s87, s98\n"
"v_cvt_pk_bf16_f32 v68, v68, v69\n"
"v_cvt_pk_bf16_f32 v69, v70, v71\n"
"v_mfma_f32_16x16x32_bf16 v[88:91], a[48:51], a[176:179], v[88:91]\n"
"s_add_u32 s86, s86, 2048\n"
"v_cvt_pk_bf16_f32 v70, v180, v181\n"
"v_cvt_pk_bf16_f32 v71, v182, v183\n"
"v_mul_f32 v72, s65, v72\n"
"v_mfma_f32_16x16x32_bf16 v[200:203], a[20:23], a[148:151], v[200:203]\n"
"v_mul_f32 v73, s65, v73\n"
"v_mul_f32 v74, s65, v74\n"
"v_mul_f32 v75, s65, v75\n"
"v_mfma_f32_16x16x32_bf16 v[200:203], a[52:55], a[176:179], v[200:203]\n"
"v_mul_f32 v184, s65, v184\n"
"v_mul_f32 v185, s65, v185\n"
"v_mul_f32 v186, s65, v186\n"
"v_mfma_f32_16x16x32_bf16 v[92:95], a[24:27], a[148:151], v[92:95]\n"
"v_mul_f32 v187, s65, v187\n"
"v_cvt_pk_bf16_f32 v72, v72, v73\n"
"v_cvt_pk_bf16_f32 v73, v74, v75\n"
"v_mfma_f32_16x16x32_bf16 v[92:95], a[56:59], a[176:179], v[92:95]\n"
"v_cvt_pk_bf16_f32 v74, v184, v185\n"
"v_cvt_pk_bf16_f32 v75, v186, v187\n"
"v_mul_f32 v76, s65, v76\n"
"v_mfma_f32_16x16x32_bf16 v[204:207], a[28:31], a[148:151], v[204:207]\n"
"v_mul_f32 v77, s65, v77\n"
"v_mul_f32 v78, s65, v78\n"
"v_mul_f32 v79, s65, v79\n"
"v_mfma_f32_16x16x32_bf16 v[204:207], a[60:63], a[176:179], v[204:207]\n"
"v_mul_f32 v188, s65, v188\n"
"v_mul_f32 v189, s65, v189\n"
"v_mul_f32 v190, s65, v190\n"
"v_mfma_f32_16x16x32_bf16 v[232:235], a[224:227], v[48:51], 0\n"
"v_mul_f32 v191, s65, v191\n"
"v_cvt_pk_bf16_f32 v76, v76, v77\n"
"v_cvt_pk_bf16_f32 v77, v78, v79\n"
"v_mfma_f32_16x16x32_bf16 v[232:235], a[228:231], v[52:55], v[232:235]\n"
"v_cvt_pk_bf16_f32 v78, v188, v189\n"
"v_cvt_pk_bf16_f32 v79, v190, v191\n"
"v_mfma_f32_16x16x32_bf16 v[232:235], a[232:235], v[56:59], v[232:235]\n"
"v_mul_f32 v80, s65, v80\n"
"v_mul_f32 v81, s65, v81\n"
"v_mfma_f32_16x16x32_bf16 v[232:235], a[236:239], v[60:63], v[232:235]\n"
"v_mul_f32 v82, s65, v82\n"
"v_mul_f32 v83, s65, v83\n"
"v_mfma_f32_16x16x32_bf16 v[96:99], a[0:3], a[152:155], v[96:99]\n"
"v_mul_f32 v192, s65, v192\n"
"v_mul_f32 v193, s65, v193\n"
"v_mfma_f32_16x16x32_bf16 v[96:99], a[32:35], a[180:183], v[96:99]\n"
"v_mul_f32 v194, s65, v194\n"
"v_mul_f32 v195, s65, v195\n"
"v_mfma_f32_16x16x32_bf16 v[208:211], a[4:7], a[152:155], v[208:211]\n"
"v_cvt_pk_bf16_f32 v80, v80, v81\n"
"v_cvt_pk_bf16_f32 v81, v82, v83\n"
"v_mfma_f32_16x16x32_bf16 v[208:211], a[36:39], a[180:183], v[208:211]\n"
"v_cvt_pk_bf16_f32 v82, v192, v193\n"
"v_cvt_pk_bf16_f32 v83, v194, v195\n"
"v_mul_f32 v84, s65, v84\n"
"v_mfma_f32_16x16x32_bf16 v[100:103], a[8:11], a[152:155], v[100:103]\n"
"v_max3_f32 v240, v240, v232, v233\n"
"v_max3_f32 v240, v240, v234, v235\n"
"v_mul_f32 v85, s65, v85\n"
"v_mfma_f32_16x16x32_bf16 v[100:103], a[40:43], a[180:183], v[100:103]\n"
"v_mul_f32 v86, s65, v86\n"
"v_mul_f32 v87, s65, v87\n"
"v_mul_f32 v196, s65, v196\n"
"v_mfma_f32_16x16x32_bf16 v[212:215], a[12:15], a[152:155], v[212:215]\n"
"v_mul_f32 v197, s65, v197\n"
"v_mul_f32 v198, s65, v198\n"
"v_mul_f32 v199, s65, v199\n"
"v_mfma_f32_16x16x32_bf16 v[212:215], a[44:47], a[180:183], v[212:215]\n"
"v_cvt_pk_bf16_f32 v84, v84, v85\n"
"v_cvt_pk_bf16_f32 v85, v86, v87\n"
"v_cvt_pk_bf16_f32 v86, v196, v197\n"
"v_mfma_f32_16x16x32_bf16 v[104:107], a[16:19], a[152:155], v[104:107]\n"
"v_cvt_pk_bf16_f32 v87, v198, v199\n"
"v_mul_f32 v88, s65, v88\n"
"v_mul_f32 v89, s65, v89\n"
"v_mfma_f32_16x16x32_bf16 v[104:107], a[48:51], a[180:183], v[104:107]\n"
"s_mov_b32 m0, s86\n"
"v_mul_f32 v90, s65, v90\n"
"v_mul_f32 v91, s65, v91\n"
"v_mul_f32 v200, s65, v200\n"
"v_mfma_f32_16x16x32_bf16 v[216:219], a[20:23], a[152:155], v[216:219]\n"
"buffer_load_dwordx4 v245, s[80:83], s87 offen nt lds\n"
"buffer_load_dwordx4 v246, s[80:83], s87 offen offset:1024 lds\n"
"v_mfma_f32_16x16x32_bf16 v[216:219], a[52:55], a[180:183], v[216:219]\n"
"s_add_u32 s87, s87, 2048\n"
"v_mul_f32 v201, s65, v201\n"
"v_mul_f32 v202, s65, v202\n"
"v_mul_f32 v203, s65, v203\n"
"v_mfma_f32_16x16x32_bf16 v[108:111], a[24:27], a[152:155], v[108:111]\n"
"s_and_b32 s87, s87, s98\n"
"v_cvt_pk_bf16_f32 v88, v88, v89\n"
"v_cvt_pk_bf16_f32 v89, v90, v91\n"
"v_cvt_pk_bf16_f32 v90, v200, v201\n"
"v_mfma_f32_16x16x32_bf16 v[108:111], a[56:59], a[180:183], v[108:111]\n"
"s_add_u32 s86, s86, 2048\n"
"v_cvt_pk_bf16_f32 v91, v202, v203\n"
"v_mul_f32 v92, s65, v92\n"
"v_mul_f32 v93, s65, v93\n"
"v_mfma_f32_16x16x32_bf16 v[220:223], a[28:31], a[152:155], v[220:223]\n"
"v_mul_f32 v94, s65, v94\n"
"v_mul_f32 v95, s65, v95\n"
"v_mul_f32 v204, s65, v204\n"
"v_mfma_f32_16x16x32_bf16 v[220:223], a[60:63], a[180:183], v[220:223]\n"
"v_mul_f32 v205, s65, v205\n"
"v_mul_f32 v206, s65, v206\n"
"v_mul_f32 v207, s65, v207\n"
"v_mfma_f32_16x16x32_bf16 v[224:227], a[224:227], v[64:67], 0\n"
"v_cvt_pk_bf16_f32 v92, v92, v93\n"
"v_cvt_pk_bf16_f32 v93, v94, v95\n"
"v_cvt_pk_bf16_f32 v94, v204, v205\n"
"v_mfma_f32_16x16x32_bf16 v[224:227], a[228:231], v[68:71], v[224:227]\n"
"v_cvt_pk_bf16_f32 v95, v206, v207\n"
"v_mul_f32 v96, s65, v96\n"
"v_mul_f32 v97, s65, v97\n"
"v_mul_f32 v98, s65, v98\n"
"v_mul_f32 v99, s65, v99\n"
"v_mul_f32 v208, s65, v208\n"
"v_mul_f32 v209, s65, v209\n"
"v_mul_f32 v210, s65, v210\n"
"v_mul_f32 v211, s65, v211\n"
"v_cvt_pk_bf16_f32 v96, v96, v97\n"
"v_cvt_pk_bf16_f32 v97, v98, v99\n"
"v_mfma_f32_16x16x32_bf16 v[224:227], a[232:235], v[72:75], v[224:227]\n"
"v_cvt_pk_bf16_f32 v98, v208, v209\n"
"v_cvt_pk_bf16_f32 v99, v210, v211\n"
"v_mul_f32 v100, s65, v100\n"
"v_mul_f32 v101, s65, v101\n"
"v_mul_f32 v102, s65, v102\n"
"v_mul_f32 v103, s65, v103\n"
"v_mul_f32 v212, s65, v212\n"
"v_mul_f32 v213, s65, v213\n"
"v_mul_f32 v214, s65, v214\n"
"v_mul_f32 v215, s65, v215\n"
"v_cvt_pk_bf16_f32 v100, v100, v101\n"
"v_mfma_f32_16x16x32_bf16 v[224:227], a[236:239], v[76:79], v[224:227]\n"
"v_cvt_pk_bf16_f32 v101, v102, v103\n"
"v_cvt_pk_bf16_f32 v102, v212, v213\n"
"v_cvt_pk_bf16_f32 v103, v214, v215\n"
"v_mul_f32 v104, s65, v104\n"
"v_mul_f32 v105, s65, v105\n"
"v_mul_f32 v106, s65, v106\n"
"v_mul_f32 v107, s65, v107\n"
"v_mul_f32 v216, s65, v216\n"
"v_mul_f32 v217, s65, v217\n"
"v_mul_f32 v218, s65, v218\n"
"v_mul_f32 v219, s65, v219\n"
"v_mfma_f32_16x16x32_bf16 v[232:235], a[224:227], v[80:83], 0\n"
"v_cvt_pk_bf16_f32 v104, v104, v105\n"
"v_cvt_pk_bf16_f32 v105, v106, v107\n"
"v_cvt_pk_bf16_f32 v106, v216, v217\n"
"v_cvt_pk_bf16_f32 v107, v218, v219\n"
"v_mul_f32 v108, s65, v108\n"
"v_mul_f32 v109, s65, v109\n"
"v_mul_f32 v110, s65, v110\n"
"v_mul_f32 v111, s65, v111\n"
"v_mul_f32 v220, s65, v220\n"
"v_mul_f32 v221, s65, v221\n"
"v_mul_f32 v222, s65, v222\n"
"v_mfma_f32_16x16x32_bf16 v[232:235], a[228:231], v[84:87], v[232:235]\n"
"v_mul_f32 v223, s65, v223\n"
"v_cvt_pk_bf16_f32 v108, v108, v109\n"
"v_cvt_pk_bf16_f32 v109, v110, v111\n"
"v_cvt_pk_bf16_f32 v110, v220, v221\n"
"v_cvt_pk_bf16_f32 v111, v222, v223\n"
"v_mfma_f32_16x16x32_bf16 v[232:235], a[232:235], v[88:91], v[232:235]\n"
"v_max3_f32 v240, v240, v224, v225\n"
"v_max3_f32 v240, v240, v226, v227\n"
"v_mfma_f32_16x16x32_bf16 v[232:235], a[236:239], v[92:95], v[232:235]\n"
"v_mfma_f32_16x16x32_bf16 v[224:227], a[224:227], v[96:99], 0\n"
"v_mfma_f32_16x16x32_bf16 v[224:227], a[228:231], v[100:103], v[224:227]\n"
"v_mfma_f32_16x16x32_bf16 v[224:227], a[232:235], v[104:107], v[224:227]\n"
"s_nop 4 ; auto\n"
"v_max3_f32 v240, v240, v232, v233\n"
"v_max3_f32 v240, v240, v234, v235\n"
"v_mfma_f32_16x16x32_bf16 v[224:227], a[236:239], v[108:111], v[224:227]\n"
"s_nop 7 ; auto\n"
"v_max3_f32 v240, v240, v224, v225\n"
"v_max3_f32 v240, v240, v226, v227\n"
"v_mov_b32 v241, v240\n"
"s_nop 1 ; auto\n"
"v_permlane32_swap_b32 v240, v241\n"
"v_max_f32 v240, v240, v241\n"
"v_mov_b32 v241, v240\n"
"s_nop 1 ; auto\n"
"v_permlane16_swap_b32 v240, v241\n"
"v_max_f32 v240, v240, v241\n"
"v_sub_f32 v232, 0, v240\n"
"v_sub_f32 v233, 0, v240\n"
"v_sub_f32 v234, 0, v240\n"
"v_sub_f32 v235, 0, v240\n"
"v_mov_b32 v236, 0\n"
"v_mov_b32 v237, 0\n"
"v_mov_b32 v238, 0\n"
"v_mov_b32 v239, 0\n"
"v_mov_b32 v240, 0\n"
"v_mov_b32 v241, 0\n"
"v_mov_b32 v242, 0\n"
"; attn block -1\n"
"v_mfma_f32_16x16x32_bf16 v[208:211], a[224:227], v[0:3], v[232:235]\n"
"v_mfma_f32_16x16x32_bf16 v[208:211], a[228:231], v[4:7], v[208:211]\n"
"v_mfma_f32_16x16x32_bf16 v[208:211], a[232:235], v[8:11], v[208:211]\n"
"v_mfma_f32_16x16x32_bf16 v[208:211], a[236:239], v[12:15], v[208:211]\n"
"v_mfma_f32_16x16x32_bf16 v[212:215], a[252:255], v[12:15], v[232:235]\n"
"v_mfma_f32_16x16x32_bf16 v[212:215], a[248:251], v[8:11], v[212:215]\n"
"v_mfma_f32_16x16x32_bf16 v[212:215], a[244:247], v[4:7], v[212:215]\n"
"v_mfma_f32_16x16x32_bf16 v[212:215], a[240:243], v[0:3], v[212:215]\n"
"v_mfma_f32_16x16x32_bf16 v[216:219], a[224:227], v[16:19], v[232:235]\n"
"s_nop 2 ; auto\n"
"v_exp_f32 v208, v208\n"
"s_nop 0 ; auto\n"
"v_add_f32 v236, v236, v208\n"
"v_mfma_f32_16x16x32_bf16 v[216:219], a[228:231], v[20:23], v[216:219]\n"
"v_exp_f32 v209, v209\n"
"s_nop 0 ; auto\n"
"v_add_f32 v236, v236, v209\n"
"v_cvt_pk_bf16_f32 v224, v208, v209\n"
"v_mfma_f32_16x16x32_bf16 v[216:219], a[232:235], v[24:27], v[216:219]\n"
"v_exp_f32 v210, v210\n"
"s_nop 0 ; auto\n"
"v_add_f32 v236, v236, v210\n"
"v_mfma_f32_16x16x32_bf16 v[216:219], a[236:239], v[28:31], v[216:219]\n"
"v_exp_f32 v211, v211\n"
"s_nop 0 ; auto\n"
"v_add_f32 v236, v236, v211\n"
"v_cvt_pk_bf16_f32 v225, v210, v211\n"
"v_mfma_f32_16x16x32_bf16 v[220:223], a[252:255], v[28:31], v[232:235]\n"
"v_exp_f32 v212, v212\n"
"s_nop 0 ; auto\n"
"v_add_f32 v236, v236, v212\n"
"v_mfma_f32_16x16x32_bf16 v[220:223], a[248:251], v[24:27], v[220:223]\n"
"v_exp_f32 v213, v213\n"
"s_nop 0 ; auto\n"
"v_add_f32 v236, v236, v213\n"
"v_cvt_pk_bf16_f32 v226, v212, v213\n"
"v_mfma_f32_16x16x32_bf16 v[220:223], a[244:247], v[20:23], v[220:223]\n"
"s_mov_b32 m0, s86\n"
"v_exp_f32 v214, v214\n"
"buffer_load_dwordx4 v245, s[80:83], s87 offen nt lds\n"
"buffer_load_dwordx4 v246, s[80:83], s87 offen offset:1024 lds\n"
"v_mfma_f32_16x16x32_bf16 v[220:223], a[240:243], v[16:19], v[220:223]\n"
"v_exp_f32 v215, v215\n"
"v_add_f32 v236, v236, v214\n"
"v_add_f32 v236, v236, v215\n"
"v_cvt_pk_bf16_f32 v227, v214, v215\n"
"; attn block 1\n"
"v_mfma_f32_16x16x32_bf16 v[208:211], a[224:227], v[32:35], v[232:235]\n"
"s_barrier\n"
"v_exp_f32 v216, v216\n"
"v_mfma_f32_16x16x32_bf16 v[208:211], a[228:231], v[36:39], v[208:211]\n"
"ds_read_b128 a[160:163], v249 offset:0\n"
"v_add_f32 v237, v237, v216\n"
"v_mfma_f32_16x16x32_bf16 v[208:211], a[232:235], v[40:43], v[208:211]\n"
"s_add_u32 s87, s87, 2048\n"
"v_exp_f32 v217, v217\n"
"v_mfma_f32_16x16x32_bf16 v[208:211], a[236:239], v[44:47], v[208:211]\n"
"v_add_f32 v237, v237, v217\n"
"v_cvt_pk_bf16_f32 v228, v216, v217\n"
"v_mfma_f32_16x16x32_bf16 v[212:215], a[252:255], v[44:47], v[232:235]\n"
"s_and_b32 s87, s87, s98\n"
"v_exp_f32 v218, v218\n"
"v_mfma_f32_16x16x32_bf16 v[212:215], a[248:251], v[40:43], v[212:215]\n"
"ds_read_b128 a[164:167], v249 offset:256\n"
"v_add_f32 v237, v237, v218\n"
"v_mfma_f32_16x16x32_bf16 v[212:215], a[244:247], v[36:39], v[212:215]\n"
"s_add_u32 s86, s86, 2048\n"
"v_exp_f32 v219, v219\n"
"v_mfma_f32_16x16x32_bf16 v[212:215], a[240:243], v[32:35], v[212:215]\n"
"v_add_f32 v237, v237, v219\n"
"v_cvt_pk_bf16_f32 v229, v218, v219\n"
"v_mfma_f32_16x16x32_bf16 v[112:115], a[192:195], v[224:227], 0\n"
"s_cmp_eq_u32 s86, s89\n"
"v_exp_f32 v220, v220\n"
"v_mfma_f32_16x16x32_bf16 v[116:119], a[196:199], v[224:227], 0\n"
"ds_read_b128 a[168:171], v249 offset:512\n"
"v_add_f32 v237, v237, v220\n"
"v_mfma_f32_16x16x32_bf16 v[120:123], a[200:203], v[224:227], 0\n"
"s_cselect_b32 s86, s66, s86\n"
"v_exp_f32 v221, v221\n"
"v_mfma_f32_16x16x32_bf16 v[124:127], a[204:207], v[224:227], 0\n"
"v_add_f32 v237, v237, v221\n"
"v_cvt_pk_bf16_f32 v230, v220, v221\n"
"v_mfma_f32_16x16x32_bf16 v[128:131], a[208:211], v[224:227], 0\n"
"v_exp_f32 v222, v222\n"
"v_mfma_f32_16x16x32_bf16 v[132:135], a[212:215], v[224:227], 0\n"
"ds_read_b128 a[172:175], v249 offset:768\n"
"v_add_f32 v237, v237, v222\n"
"v_mfma_f32_16x16x32_bf16 v[136:139], a[216:219], v[224:227], 0\n"
"v_exp_f32 v223, v223\n"
"v_mfma_f32_16x16x32_bf16 v[140:143], a[220:223], v[224:227], 0\n"
"v_add_f32 v237, v237, v223\n"
"v_cvt_pk_bf16_f32 v231, v222, v223\n"
"v_mfma_f32_16x16x32_bf16 v[216:219], a[224:227], v[48:51], v[232:235]\n"
"v_exp_f32 v208, v208\n"
"v_mfma_f32_16x16x32_bf16 v[216:219], a[228:231], v[52:55], v[216:219]\n"
"ds_read_b128 a[176:179], v249 offset:4096\n"
"v_add_f32 v238, v238, v208\n"
"v_mfma_f32_16x16x32_bf16 v[216:219], a[232:235], v[56:59], v[216:219]\n"
"s_add_u32 s88, s88, s90\n"
"v_exp_f32 v209, v209\n"
"v_mfma_f32_16x16x32_bf16 v[216:219], a[236:239], v[60:63], v[216:219]\n"
"v_lshrrev_b32 v248, 2, %[tA]\n"
"v_and_b32 v248, 28, v248\n"
"v_add_f32 v238, v238, v209\n"
"v_cvt_pk_bf16_f32 v224, v208, v209\n"
"v_mfma_f32_16x16x32_bf16 v[220:223], a[252:255], v[60:63], v[232:235]\n"
"s_cmp_eq_u32 s88, 32768\n"
"v_exp_f32 v210, v210\n"
"v_mfma_f32_16x16x32_bf16 v[220:223], a[248:251], v[56:59], v[220:223]\n"
"ds_read_b128 a[180:183], v249 offset:4352\n"
"v_add_f32 v238, v238, v210\n"
"v_mfma_f32_16x16x32_bf16 v[220:223], a[244:247], v[52:55], v[220:223]\n"
"s_cselect_b32 s90, 0xffff8000, s96\n"
"v_exp_f32 v211, v211\n"
"v_mfma_f32_16x16x32_bf16 v[220:223], a[240:243], v[48:51], v[220:223]\n"
"buffer_store_dword v242, v248, s[32:35], s74 offen sc1\n"
"v_add_f32 v238, v238, v211\n"
"v_cvt_pk_bf16_f32 v225, v210, v211\n"
"v_mfma_f32_16x16x32_bf16 v[144:147], a[192:195], v[228:231], 0\n"
"v_exp_f32 v212, v212\n"
"v_mfma_f32_16x16x32_bf16 v[148:151], a[196:199], v[228:231], 0\n"
"ds_read_b128 a[184:187], v249 offset:4608\n"
"v_add_f32 v238, v238, v212\n"
"v_mfma_f32_16x16x32_bf16 v[152:155], a[200:203], v[228:231], 0\n"
"v_exp_f32 v213, v213\n"
"v_mfma_f32_16x16x32_bf16 v[156:159], a[204:207], v[228:231], 0\n"
"v_lshlrev_b32 v248, 12, %[tA]\n"
"v_add_f32 v238, v238, v213\n"
"v_cvt_pk_bf16_f32 v226, v212, v213\n"
"v_mfma_f32_16x16x32_bf16 v[160:163], a[208:211], v[228:231], 0\n"
"s_mov_b32 m0, s86\n"
"v_exp_f32 v214, v214\n"
"v_mfma_f32_16x16x32_bf16 v[164:167], a[212:215], v[228:231], 0\n"
"buffer_load_dwordx4 v245, s[80:83], s87 offen nt lds\n"
"buffer_load_dwordx4 v246, s[80:83], s87 offen offset:1024 lds\n"
"v_mfma_f32_16x16x32_bf16 v[168:171], a[216:219], v[228:231], 0\n"
"v_exp_f32 v215, v215\n"
"v_mfma_f32_16x16x32_bf16 v[172:175], a[220:223], v[228:231], 0\n"
"buffer_load_dword v248, v248, s[76:79], 0 offen\n"
"v_add_f32 v238, v238, v214\n"
"v_add_f32 v238, v238, v215\n"
"v_cvt_pk_bf16_f32 v227, v214, v215\n"
"v_mfma_f32_16x16x32_bf16 v[208:211], a[224:227], v[64:67], v[232:235]\n"
"v_exp_f32 v216, v216\n"
"v_mfma_f32_16x16x32_bf16 v[208:211], a[228:231], v[68:71], v[208:211]\n"
"ds_read_b128 a[188:191], v249 offset:4864\n"
"v_add_f32 v239, v239, v216\n"
"v_mfma_f32_16x16x32_bf16 v[208:211], a[232:235], v[72:75], v[208:211]\n"
"s_add_u32 s87, s87, 2048\n"
"v_exp_f32 v217, v217\n"
"v_mfma_f32_16x16x32_bf16 v[208:211], a[236:239], v[76:79], v[208:211]\n"
"v_add_f32 v239, v239, v217\n"
"v_cvt_pk_bf16_f32 v228, v216, v217\n"
"v_mfma_f32_16x16x32_bf16 v[212:215], a[252:255], v[76:79], v[232:235]\n"
"s_and_b32 s87, s87, s98\n"
"v_exp_f32 v218, v218\n"
"v_mfma_f32_16x16x32_bf16 v[212:215], a[248:251], v[72:75], v[212:215]\n"
"ds_read_b64_tr_b16 a[128:129], v250 offset:0\n"
"v_add_f32 v239, v239, v218\n"
"v_mfma_f32_16x16x32_bf16 v[212:215], a[244:247], v[68:71], v[212:215]\n"
"s_add_u32 s86, s86, 2048\n"
"v_exp_f32 v219, v219\n"
"v_mfma_f32_16x16x32_bf16 v[212:215], a[240:243], v[64:67], v[212:215]\n"
"v_add_f32 v239, v239, v219\n"
"v_cvt_pk_bf16_f32 v229, v218, v219\n"
"v_mfma_f32_16x16x32_bf16 v[176:179], a[192:195], v[224:227], 0\n"
"s_cmp_eq_u32 s86, s89\n"
"v_exp_f32 v220, v220\n"
"v_mfma_f32_16x16x32_bf16 v[180:183], a[196:199], v[224:227], 0\n"
"ds_read_b64_tr_b16 a[130:131], v250 offset:4096\n"
"v_add_f32 v239, v239, v220\n"
"v_mfma_f32_16x16x32_bf16 v[184:187], a[200:203], v[224:227], 0\n"
"s_cselect_b32 s86, s66, s86\n"
"v_exp_f32 v221, v221\n"
"v_mfma_f32_16x16x32_bf16 v[188:191], a[204:207], v[224:227], 0\n"
"v_add_f32 v239, v239, v221\n"
"v_cvt_pk_bf16_f32 v230, v220, v221\n"
"v_mfma_f32_16x16x32_bf16 v[192:195], a[208:211], v[224:227], 0\n"
"v_exp_f32 v222, v222\n"
"v_mfma_f32_16x16x32_bf16 v[196:199], a[212:215], v[224:227], 0\n"
"ds_read_b64_tr_b16 a[132:133], v251 offset:0\n"
"v_add_f32 v239, v239, v222\n"
"v_mfma_f32_16x16x32_bf16 v[200:203], a[216:219], v[224:227], 0\n"
"v_exp_f32 v223, v223\n"
"v_mfma_f32_16x16x32_bf16 v[204:207], a[220:223], v[224:227], 0\n"
"v_add_f32 v239, v239, v223\n"
"v_cvt_pk_bf16_f32 v231, v222, v223\n"
"v_mfma_f32_16x16x32_bf16 v[216:219], a[224:227], v[80:83], v[232:235]\n"
"v_exp_f32 v208, v208\n"
"v_mfma_f32_16x16x32_bf16 v[216:219], a[228:231], v[84:87], v[216:219]\n"
"ds_read_b64_tr_b16 a[134:135], v251 offset:4096\n"
"v_add_f32 v240, v240, v208\n"
"v_mfma_f32_16x16x32_bf16 v[216:219], a[232:235], v[88:91], v[216:219]\n"
"v_exp_f32 v209, v209\n"
"v_mfma_f32_16x16x32_bf16 v[216:219], a[236:239], v[92:95], v[216:219]\n"
"v_add_u32 v249, s90, v249\n"
"v_add_f32 v240, v240, v209\n"
"v_cvt_pk_bf16_f32 v224, v208, v209\n"
"v_mfma_f32_16x16x32_bf16 v[220:223], a[252:255], v[92:95], v[232:235]\n"
"v_exp_f32 v210, v210\n"
"v_mfma_f32_16x16x32_bf16 v[220:223], a[248:251], v[88:91], v[220:223]\n"
"ds_read_b64_tr_b16 a[136:137], v250 offset:256\n"
"v_add_f32 v240, v240, v210\n"
"v_mfma_f32_16x16x32_bf16 v[220:223], a[244:247], v[84:87], v[220:223]\n"
"v_exp_f32 v211, v211\n"
"v_mfma_f32_16x16x32_bf16 v[220:223], a[240:243], v[80:83], v[220:223]\n"
"v_add_f32 v240, v240, v211\n"
"v_cvt_pk_bf16_f32 v225, v210, v211\n"
"v_mfma_f32_16x16x32_bf16 a[0:3], a[192:195], v[228:231], 0\n"
"v_exp_f32 v212, v212\n"
"v_mfma_f32_16x16x32_bf16 a[4:7], a[196:199], v[228:231], 0\n"
"ds_read_b64_tr_b16 a[138:139], v250 offset:4352\n"
"v_add_f32 v240, v240, v212\n"
"v_mfma_f32_16x16x32_bf16 a[8:11], a[200:203], v[228:231], 0\n"
"v_exp_f32 v213, v213\n"
"v_mfma_f32_16x16x32_bf16 a[12:15], a[204:207], v[228:231], 0\n"
"v_add_f32 v240, v240, v213\n"
"v_cvt_pk_bf16_f32 v226, v212, v213\n"
"v_mfma_f32_16x16x32_bf16 a[16:19], a[208:211], v[228:231], 0\n"
"s_mov_b32 m0, s86\n"
"v_exp_f32 v214, v214\n"
"v_mfma_f32_16x16x32_bf16 a[20:23], a[212:215], v[228:231], 0\n"
"buffer_load_dwordx4 v245, s[80:83], s87 offen nt lds\n"
"buffer_load_dwordx4 v246, s[80:83], s87 offen offset:1024 lds\n"
"v_mfma_f32_16x16x32_bf16 a[24:27], a[216:219], v[228:231], 0\n"
"v_exp_f32 v215, v215\n"
"v_mfma_f32_16x16x32_bf16 a[28:31], a[220:223], v[228:231], 0\n"
"v_add_f32 v240, v240, v214\n"
"v_add_f32 v240, v240, v215\n"
"v_cvt_pk_bf16_f32 v227, v214, v215\n"
"v_mfma_f32_16x16x32_bf16 v[208:211], a[224:227], v[96:99], v[232:235]\n"
"v_exp_f32 v216, v216\n"
"v_mfma_f32_16x16x32_bf16 v[208:211], a[228:231], v[100:103], v[208:211]\n"
"ds_read_b64_tr_b16 a[140:141], v251 offset:256\n"
"v_add_f32 v241, v241, v216\n"
"v_mfma_f32_16x16x32_bf16 v[208:211], a[232:235], v[104:107], v[208:211]\n"
"s_add_u32 s87, s87, 2048\n"
"v_exp_f32 v217, v217\n"
"v_mfma_f32_16x16x32_bf16 v[208:211], a[236:239], v[108:111], v[208:211]\n"
"v_add_f32 v241, v241, v217\n"
"v_cvt_pk_bf16_f32 v228, v216, v217\n"
"v_mfma_f32_16x16x32_bf16 v[212:215], a[252:255], v[108:111], v[232:235]\n"
"s_and_b32 s87, s87, s98\n"
"v_exp_f32 v218, v218\n"
"v_mfma_f32_16x16x32_bf16 v[212:215], a[248:251], v[104:107], v[212:215]\n"
"ds_read_b64_tr_b16 a[142:143], v251 offset:4352\n"
"v_add_f32 v241, v241, v218\n"
"v_mfma_f32_16x16x32_bf16 v[212:215], a[244:247], v[100:103], v[212:215]\n"
"s_add_u32 s86, s86, 2048\n"
"v_exp_f32 v219, v219\n"
"v_mfma_f32_16x16x32_bf16 v[212:215], a[240:243], v[96:99], v[212:215]\n"
"v_add_f32 v241, v241, v219\n"
"v_cvt_pk_bf16_f32 v229, v218, v219\n"
"v_mfma_f32_16x16x32_bf16 a[32:35], a[192:195], v[224:227], 0\n"
"s_cmp_eq_u32 s86, s89\n"
"v_exp_f32 v220, v220\n"
"v_mfma_f32_16x16x32_bf16 a[36:39], a[196:199], v[224:227], 0\n"
"ds_read_b64_tr_b16 a[144:145], v250 offset:512\n"
"v_add_f32 v241, v241, v220\n"
"v_mfma_f32_16x16x32_bf16 a[40:43], a[200:203], v[224:227], 0\n"
"s_cselect_b32 s86, s66, s86\n"
"v_exp_f32 v221, v221\n"
"v_mfma_f32_16x16x32_bf16 a[44:47], a[204:207], v[224:227], 0\n"
"v_add_f32 v241, v241, v221\n"
"v_cvt_pk_bf16_f32 v230, v220, v221\n"
"v_mfma_f32_16x16x32_bf16 a[48:51], a[208:211], v[224:227], 0\n"
"v_exp_f32 v222, v222\n"
"v_mfma_f32_16x16x32_bf16 a[52:55], a[212:215], v[224:227], 0\n"
"ds_read_b64_tr_b16 a[146:147], v250 offset:4608\n"
"v_add_f32 v241, v241, v222\n"
"v_mfma_f32_16x16x32_bf16 a[56:59], a[216:219], v[224:227], 0\n"
"s_waitcnt lgkmcnt(10)\n"
"v_exp_f32 v223, v223\n"
"v_mfma_f32_16x16x32_bf16 a[60:63], a[220:223], v[224:227], 0\n"
"v_add_f32 v241, v241, v223\n"
"v_cvt_pk_bf16_f32 v231, v222, v223\n"
"v_mfma_f32_16x16x32_bf16 v[216:219], a[160:163], v[0:3], v[232:235]\n"
"v_exp_f32 v208, v208\n"
"v_mfma_f32_16x16x32_bf16 v[216:219], a[164:167], v[4:7], v[216:219]\n"
"ds_read_b64_tr_b16 a[148:149], v251 offset:512\n"
"v_add_f32 v242, v242, v208\n"
"v_mfma_f32_16x16x32_bf16 v[216:219], a[168:171], v[8:11], v[216:219]\n"
"v_exp_f32 v209, v209\n"
"v_mfma_f32_16x16x32_bf16 v[216:219], a[172:175], v[12:15], v[216:219]\n"
"v_add_f32 v242, v242, v209\n"
"v_cvt_pk_bf16_f32 v224, v208, v209\n"
"v_mfma_f32_16x16x32_bf16 v[220:223], a[188:191], v[12:15], v[232:235]\n"
"v_exp_f32 v210, v210\n"
"v_mfma_f32_16x16x32_bf16 v[220:223], a[184:187], v[8:11], v[220:223]\n"
"ds_read_b64_tr_b16 a[150:151], v251 offset:4608\n"
"v_add_f32 v242, v242, v210\n"
"v_mfma_f32_16x16x32_bf16 v[220:223], a[180:183], v[4:7], v[220:223]\n"
"v_exp_f32 v211, v211\n"
"v_mfma_f32_16x16x32_bf16 v[220:223], a[176:179], v[0:3], v[220:223]\n"
"v_add_f32 v242, v242, v211\n"
"v_cvt_pk_bf16_f32 v225, v210, v211\n"
"v_mfma_f32_16x16x32_bf16 a[64:67], a[192:195], v[228:231], 0\n"
"v_exp_f32 v212, v212\n"
"v_mfma_f32_16x16x32_bf16 a[68:71], a[196:199], v[228:231], 0\n"
"ds_read_b64_tr_b16 a[152:153], v250 offset:768\n"
"v_add_f32 v242, v242, v212\n"
"v_mfma_f32_16x16x32_bf16 a[72:75], a[200:203], v[228:231], 0\n"
"v_exp_f32 v213, v213\n"
"v_mfma_f32_16x16x32_bf16 a[76:79], a[204:207], v[228:231], 0\n"
"v_add_f32 v242, v242, v213\n"
"v_cvt_pk_bf16_f32 v226, v212, v213\n"
"v_mfma_f32_16x16x32_bf16 a[80:83], a[208:211], v[228:231], 0\n"
"s_mov_b32 m0, s86\n"
"v_exp_f32 v214, v214\n"
"v_mfma_f32_16x16x32_bf16 a[84:87], a[212:215], v[228:231], 0\n"
"buffer_load_dwordx4 v245, s[80:83], s87 offen nt lds\n"
"buffer_load_dwordx4 v246, s[80:83], s87 offen offset:1024 lds\n"
"v_mfma_f32_16x16x32_bf16 a[88:91], a[216:219], v[228:231], 0\n"
"v_exp_f32 v215, v215\n"
"v_mfma_f32_16x16x32_bf16 a[92:95], a[220:223], v[228:231], 0\n"
"v_add_f32 v242, v242, v214\n"
"v_add_f32 v242, v242, v215\n"
"v_cvt_pk_bf16_f32 v227, v214, v215\n"
"v_mfma_f32_16x16x32_bf16 v[208:211], a[160:163], v[16:19], v[232:235]\n"
"v_exp_f32 v216, v216\n"
"v_mfma_f32_16x16x32_bf16 v[208:211], a[164:167], v[20:23], v[208:211]\n"
"ds_read_b64_tr_b16 a[154:155], v250 offset:4864\n"
"v_add_f32 v236, v236, v216\n"
"v_mfma_f32_16x16x32_bf16 v[208:211], a[168:171], v[24:27], v[208:211]\n"
"s_add_u32 s87, s87, 2048\n"
"v_exp_f32 v217, v217\n"
"v_mfma_f32_16x16x32_bf16 v[208:211], a[172:175], v[28:31], v[208:211]\n"
"v_add_f32 v236, v236, v217\n"
"v_cvt_pk_bf16_f32 v228, v216, v217\n"
"v_mfma_f32_16x16x32_bf16 v[212:215], a[188:191], v[28:31], v[232:235]\n"
"s_and_b32 s87, s87, s98\n"
"v_exp_f32 v218, v218\n"
"v_mfma_f32_16x16x32_bf16 v[212:215], a[184:187], v[24:27], v[212:215]\n"
"ds_read_b64_tr_b16 a[156:157], v251 offset:768\n"
"v_add_f32 v236, v236, v218\n"
"v_mfma_f32_16x16x32_bf16 v[212:215], a[180:183], v[20:23], v[212:215]\n"
"s_add_u32 s86, s86, 2048\n"
"v_exp_f32 v219, v219\n"
"v_mfma_f32_16x16x32_bf16 v[212:215], a[176:179], v[16:19], v[212:215]\n"
"v_add_f32 v236, v236, v219\n"
"v_cvt_pk_bf16_f32 v229, v218, v219\n"
"v_mfma_f32_16x16x32_bf16 a[96:99], a[192:195], v[224:227], 0\n"
"s_cmp_eq_u32 s86, s89\n"
"v_exp_f32 v220, v220\n"
"v_mfma_f32_16x16x32_bf16 a[100:103], a[196:199], v[224:227], 0\n"
"ds_read_b64_tr_b16 a[158:159], v251 offset:4864\n"
"v_add_f32 v236, v236, v220\n"
"v_mfma_f32_16x16x32_bf16 a[104:107], a[200:203], v[224:227], 0\n"
"s_cselect_b32 s86, s66, s86\n"
"v_exp_f32 v221, v221\n"
"v_mfma_f32_16x16x32_bf16 a[108:111], a[204:207], v[224:227], 0\n"
"v_add_u32 v250, s90, v250\n"
"v_add_f32 v236, v236, v221\n"
"v_cvt_pk_bf16_f32 v230, v220, v221\n"
"v_mfma_f32_16x16x32_bf16 a[112:115], a[208:211], v[224:227], 0\n"
"v_exp_f32 v222, v222\n"
"v_mfma_f32_16x16x32_bf16 a[116:119], a[212:215], v[224:227], 0\n"
"v_add_u32 v251, s90, v251\n"
"v_add_f32 v236, v236, v222\n"
"v_mfma_f32_16x16x32_bf16 a[120:123], a[216:219], v[224:227], 0\n"
"v_exp_f32 v223, v223\n"
"v_mfma_f32_16x16x32_bf16 a[124:127], a[220:223], v[224:227], 0\n"
"La:\n"
"v_add_f32 v236, v236, v223\n"
"v_cvt_pk_bf16_f32 v231, v222, v223\n"
"; attn block 8\n"
"v_mfma_f32_16x16x32_bf16 v[216:219], a[160:163], v[32:35], v[232:235]\n"
"s_waitcnt vmcnt(8) lgkmcnt(0)\n"
"s_barrier\n"
"v_exp_f32 v208, v208\n"
"v_mfma_f32_16x16x32_bf16 v[216:219], a[164:167], v[36:39], v[216:219]\n"
"ds_read_b128 a[224:227], v249 offset:0\n"
"v_add_f32 v237, v237, v208\n"
"v_mfma_f32_16x16x32_bf16 v[216:219], a[168:171], v[40:43], v[216:219]\n"
"v_exp_f32 v209, v209\n"
"v_mfma_f32_16x16x32_bf16 v[216:219], a[172:175], v[44:47], v[216:219]\n"
"v_add_f32 v237, v237, v209\n"
"v_cvt_pk_bf16_f32 v224, v208, v209\n"
"v_mfma_f32_16x16x32_bf16 v[220:223], a[188:191], v[44:47], v[232:235]\n"
"v_exp_f32 v210, v210\n"
"v_mfma_f32_16x16x32_bf16 v[220:223], a[184:187], v[40:43], v[220:223]\n"
"ds_read_b128 a[228:231], v249 offset:256\n"
"v_add_f32 v237, v237, v210\n"
"v_mfma_f32_16x16x32_bf16 v[220:223], a[180:183], v[36:39], v[220:223]\n"
"v_exp_f32 v211, v211\n"
"v_mfma_f32_16x16x32_bf16 v[220:223], a[176:179], v[32:35], v[220:223]\n"
"v_add_f32 v237, v237, v211\n"
"v_cvt_pk_bf16_f32 v225, v210, v211\n"
"v_mfma_f32_16x16x32_bf16 v[112:115], a[128:131], v[228:231], v[112:115]\n"
"v_exp_f32 v212, v212\n"
"v_mfma_f32_16x16x32_bf16 v[116:119], a[132:135], v[228:231], v[116:119]\n"
"ds_read_b128 a[232:235], v249 offset:512\n"
"v_add_f32 v237, v237, v212\n"
"v_mfma_f32_16x16x32_bf16 v[120:123], a[136:139], v[228:231], v[120:123]\n"
"v_exp_f32 v213, v213\n"
"v_mfma_f32_16x16x32_bf16 v[124:127], a[140:143], v[228:231], v[124:127]\n"
"v_add_f32 v237, v237, v213\n"
"v_cvt_pk_bf16_f32 v226, v212, v213\n"
"v_mfma_f32_16x16x32_bf16 v[128:131], a[144:147], v[228:231], v[128:131]\n"
"s_mov_b32 m0, s86\n"
"v_exp_f32 v214, v214\n"
"v_mfma_f32_16x16x32_bf16 v[132:135], a[148:151], v[228:231], v[132:135]\n"
"buffer_load_dwordx4 v245, s[80:83], s87 offen nt lds\n"
"buffer_load_dwordx4 v246, s[80:83], s87 offen offset:1024 lds\n"
"v_mfma_f32_16x16x32_bf16 v[136:139], a[152:155], v[228:231], v[136:139]\n"
"v_exp_f32 v215, v215\n"
"v_mfma_f32_16x16x32_bf16 v[140:143], a[156:159], v[228:231], v[140:143]\n"
"v_add_f32 v237, v237, v214\n"
"v_add_f32 v237, v237, v215\n"
"v_cvt_pk_bf16_f32 v227, v214, v215\n"
"v_mfma_f32_16x16x32_bf16 v[208:211], a[160:163], v[48:51], v[232:235]\n"
"v_exp_f32 v216, v216\n"
"v_mfma_f32_16x16x32_bf16 v[208:211], a[164:167], v[52:55], v[208:211]\n"
"ds_read_b128 a[236:239], v249 offset:768\n"
"v_add_f32 v238, v238, v216\n"
"v_mfma_f32_16x16x32_bf16 v[208:211], a[168:171], v[56:59], v[208:211]\n"
"s_add_u32 s87, s87, 2048\n"
"v_exp_f32 v217, v217\n"
"v_mfma_f32_16x16x32_bf16 v[208:211], a[172:175], v[60:63], v[208:211]\n"
"v_add_f32 v238, v238, v217\n"
"v_cvt_pk_bf16_f32 v228, v216, v217\n"
"v_mfma_f32_16x16x32_bf16 v[212:215], a[188:191], v[60:63], v[232:235]\n"
"s_and_b32 s87, s87, s98\n"
"v_exp_f32 v218, v218\n"
"v_mfma_f32_16x16x32_bf16 v[212:215], a[184:187], v[56:59], v[212:215]\n"
"ds_read_b128 a[240:243], v249 offset:4096\n"
"v_add_f32 v238, v238, v218\n"
"v_mfma_f32_16x16x32_bf16 v[212:215], a[180:183], v[52:55], v[212:215]\n"
"s_add_u32 s86, s86, 2048\n"
"v_exp_f32 v219, v219\n"
"v_mfma_f32_16x16x32_bf16 v[212:215], a[176:179], v[48:51], v[212:215]\n"
"v_add_f32 v238, v238, v219\n"
"v_cvt_pk_bf16_f32 v229, v218, v219\n"
"v_mfma_f32_16x16x32_bf16 v[144:147], a[128:131], v[224:227], v[144:147]\n"
"s_cmp_eq_u32 s86, s89\n"
"v_exp_f32 v220, v220\n"
"v_mfma_f32_16x16x32_bf16 v[148:151], a[132:135], v[224:227], v[148:151]\n"
"ds_read_b128 a[244:247], v249 offset:4352\n"
"v_add_f32 v238, v238, v220\n"
"v_mfma_f32_16x16x32_bf16 v[152:155], a[136:139], v[224:227], v[152:155]\n"
"s_cselect_b32 s86, s66, s86\n"
"v_exp_f32 v221, v221\n"
"v_mfma_f32_16x16x32_bf16 v[156:159], a[140:143], v[224:227], v[156:159]\n"
"v_add_f32 v238, v238, v221\n"
"v_cvt_pk_bf16_f32 v230, v220, v221\n"
"v_mfma_f32_16x16x32_bf16 v[160:163], a[144:147], v[224:227], v[160:163]\n"
"v_exp_f32 v222, v222\n"
"v_mfma_f32_16x16x32_bf16 v[164:167], a[148:151], v[224:227], v[164:167]\n"
"ds_read_b128 a[248:251], v249 offset:4608\n"
"v_add_f32 v238, v238, v222\n"
"v_mfma_f32_16x16x32_bf16 v[168:171], a[152:155], v[224:227], v[168:171]\n"
"v_exp_f32 v223, v223\n"
"v_mfma_f32_16x16x32_bf16 v[172:175], a[156:159], v[224:227], v[172:175]\n"
"v_add_f32 v238, v238, v223\n"
"v_cvt_pk_bf16_f32 v231, v222, v223\n"
"Lh5:\n"
"s_cbranch_execz Lw1\n"
"v_mfma_f32_16x16x32_bf16 v[216:219], a[160:163], v[64:67], v[232:235]\n"
"v_exp_f32 v208, v208\n"
"v_mfma_f32_16x16x32_bf16 v[216:219], a[164:167], v[68:71], v[216:219]\n"
"ds_read_b128 a[252:255], v249 offset:4864\n"
"v_add_f32 v239, v239, v208\n"
"v_mfma_f32_16x16x32_bf16 v[216:219], a[168:171], v[72:75], v[216:219]\n"
"s_add_u32 s88, s88, s90\n"
"v_exp_f32 v209, v209\n"
"v_mfma_f32_16x16x32_bf16 v[216:219], a[172:175], v[76:79], v[216:219]\n"
"v_add_f32 v239, v239, v209\n"
"v_cvt_pk_bf16_f32 v224, v208, v209\n"
"v_mfma_f32_16x16x32_bf16 v[220:223], a[188:191], v[76:79], v[232:235]\n"
"s_cmp_eq_u32 s88, 32768\n"
"v_exp_f32 v210, v210\n"
"v_mfma_f32_16x16x32_bf16 v[220:223], a[184:187], v[72:75], v[220:223]\n"
"ds_read_b64_tr_b16 a[192:193], v250 offset:0\n"
"v_add_f32 v239, v239, v210\n"
"v_mfma_f32_16x16x32_bf16 v[220:223], a[180:183], v[68:71], v[220:223]\n"
"s_cselect_b32 s90, 0xffff8000, s96\n"
"v_exp_f32 v211, v211\n"
"v_mfma_f32_16x16x32_bf16 v[220:223], a[176:179], v[64:67], v[220:223]\n"
"v_add_f32 v239, v239, v211\n"
"v_cvt_pk_bf16_f32 v225, v210, v211\n"
"v_mfma_f32_16x16x32_bf16 v[176:179], a[128:131], v[228:231], v[176:179]\n"
"s_sub_u32 s84, s84, 1\n"
"v_exp_f32 v212, v212\n"
"v_mfma_f32_16x16x32_bf16 v[180:183], a[132:135], v[228:231], v[180:183]\n"
"ds_read_b64_tr_b16 a[194:195], v250 offset:4096\n"
"v_add_f32 v239, v239, v212\n"
"v_mfma_f32_16x16x32_bf16 v[184:187], a[136:139], v[228:231], v[184:187]\n"
"v_exp_f32 v213, v213\n"
"v_mfma_f32_16x16x32_bf16 v[188:191], a[140:143], v[228:231], v[188:191]\n"
"v_add_f32 v239, v239, v213\n"
"v_cvt_pk_bf16_f32 v226, v212, v213\n"
"v_mfma_f32_16x16x32_bf16 v[192:195], a[144:147], v[228:231], v[192:195]\n"
"s_mov_b32 m0, s86\n"
"v_exp_f32 v214, v214\n"
"v_mfma_f32_16x16x32_bf16 v[196:199], a[148:151], v[228:231], v[196:199]\n"
"buffer_load_dwordx4 v245, s[80:83], s87 offen nt lds\n"
"buffer_load_dwordx4 v246, s[80:83], s87 offen offset:1024 lds\n"
"v_mfma_f32_16x16x32_bf16 v[200:203], a[152:155], v[228:231], v[200:203]\n"
"v_exp_f32 v215, v215\n"
"v_mfma_f32_16x16x32_bf16 v[204:207], a[156:159], v[228:231], v[204:207]\n"
"v_add_f32 v239, v239, v214\n"
"v_add_f32 v239, v239, v215\n"
"v_cvt_pk_bf16_f32 v227, v214, v215\n"
"v_mfma_f32_16x16x32_bf16 v[208:211], a[160:163], v[80:83], v[232:235]\n"
"v_exp_f32 v216, v216\n"
"v_mfma_f32_16x16x32_bf16 v[208:211], a[164:167], v[84:87], v[208:211]\n"
"ds_read_b64_tr_b16 a[196:197], v251 offset:0\n"
"v_add_f32 v240, v240, v216\n"
"v_mfma_f32_16x16x32_bf16 v[208:211], a[168:171], v[88:91], v[208:211]\n"
"s_add_u32 s87, s87, 2048\n"
"v_exp_f32 v217, v217\n"
"v_mfma_f32_16x16x32_bf16 v[208:211], a[172:175], v[92:95], v[208:211]\n"
"v_add_u32 v249, s90, v249\n"
"v_add_f32 v240, v240, v217\n"
"v_cvt_pk_bf16_f32 v228, v216, v217\n"
"v_mfma_f32_16x16x32_bf16 v[212:215], a[188:191], v[92:95], v[232:235]\n"
"s_and_b32 s87, s87, s98\n"
"v_exp_f32 v218, v218\n"
"v_mfma_f32_16x16x32_bf16 v[212:215], a[184:187], v[88:91], v[212:215]\n"
"ds_read_b64_tr_b16 a[198:199], v251 offset:4096\n"
"v_add_f32 v240, v240, v218\n"
"v_mfma_f32_16x16x32_bf16 v[212:215], a[180:183], v[84:87], v[212:215]\n"
"s_add_u32 s86, s86, 2048\n"
"v_exp_f32 v219, v219\n"
"v_mfma_f32_16x16x32_bf16 v[212:215], a[176:179], v[80:83], v[212:215]\n"
"v_add_f32 v240, v240, v219\n"
"v_cvt_pk_bf16_f32 v229, v218, v219\n"
"v_mfma_f32_16x16x32_bf16 a[0:3], a[128:131], v[224:227], a[0:3]\n"
"s_cmp_eq_u32 s86, s89\n"
"v_exp_f32 v220, v220\n"
"v_mfma_f32_16x16x32_bf16 a[4:7], a[132:135], v[224:227], a[4:7]\n"
"ds_read_b64_tr_b16 a[200:201], v250 offset:256\n"
"v_add_f32 v240, v240, v220\n"
"v_mfma_f32_16x16x32_bf16 a[8:11], a[136:139], v[224:227], a[8:11]\n"
"s_cselect_b32 s86, s66, s86\n"
"v_exp_f32 v221, v221\n"
"v_mfma_f32_16x16x32_bf16 a[12:15], a[140:143], v[224:227], a[12:15]\n"
"v_add_f32 v240, v240, v221\n"
"v_cvt_pk_bf16_f32 v230, v220, v221\n"
"v_mfma_f32_16x16x32_bf16 a[16:19], a[144:147], v[224:227], a[16:19]\n"
"v_exp_f32 v222, v222\n"
"v_mfma_f32_16x16x32_bf16 a[20:23], a[148:151], v[224:227], a[20:23]\n"
"ds_read_b64_tr_b16 a[202:203], v250 offset:4352\n"
"v_add_f32 v240, v240, v222\n"
"v_mfma_f32_16x16x32_bf16 a[24:27], a[152:155], v[224:227], a[24:27]\n"
"v_exp_f32 v223, v223\n"
"v_mfma_f32_16x16x32_bf16 a[28:31], a[156:159], v[224:227], a[28:31]\n"
"v_add_f32 v240, v240, v223\n"
"v_cvt_pk_bf16_f32 v231, v222, v223\n"
"v_mfma_f32_16x16x32_bf16 v[216:219], a[160:163], v[96:99], v[232:235]\n"
"v_exp_f32 v208, v208\n"
"v_mfma_f32_16x16x32_bf16 v[216:219], a[164:167], v[100:103], v[216:219]\n"
"ds_read_b64_tr_b16 a[204:205], v251 offset:256\n"
"v_add_f32 v241, v241, v208\n"
"v_mfma_f32_16x16x32_bf16 v[216:219], a[168:171], v[104:107], v[216:219]\n"
"v_exp_f32 v209, v209\n"
"v_mfma_f32_16x16x32_bf16 v[216:219], a[172:175], v[108:111], v[216:219]\n"
"v_add_f32 v241, v241, v209\n"
"v_cvt_pk_bf16_f32 v224, v208, v209\n"
"v_mfma_f32_16x16x32_bf16 v[220:223], a[188:191], v[108:111], v[232:235]\n"
"v_exp_f32 v210, v210\n"
"v_mfma_f32_16x16x32_bf16 v[220:223], a[184:187], v[104:107], v[220:223]\n"
"ds_read_b64_tr_b16 a[206:207], v251 offset:4352\n"
"v_add_f32 v241, v241, v210\n"
"v_mfma_f32_16x16x32_bf16 v[220:223], a[180:183], v[100:103], v[220:223]\n"
"v_exp_f32 v211, v211\n"
"v_mfma_f32_16x16x32_bf16 v[220:223], a[176:179], v[96:99], v[220:223]\n"
"v_add_f32 v241, v241, v211\n"
"v_cvt_pk_bf16_f32 v225, v210, v211\n"
"v_mfma_f32_16x16x32_bf16 a[32:35], a[128:131], v[228:231], a[32:35]\n"
"v_exp_f32 v212, v212\n"
"v_mfma_f32_16x16x32_bf16 a[36:39], a[132:135], v[228:231], a[36:39]\n"
"ds_read_b64_tr_b16 a[208:209], v250 offset:512\n"
"v_add_f32 v241, v241, v212\n"
"v_mfma_f32_16x16x32_bf16 a[40:43], a[136:139], v[228:231], a[40:43]\n"
"v_exp_f32 v213, v213\n"
"v_mfma_f32_16x16x32_bf16 a[44:47], a[140:143], v[228:231], a[44:47]\n"
"v_add_f32 v241, v241, v213\n"
"v_cvt_pk_bf16_f32 v226, v212, v213\n"
"v_mfma_f32_16x16x32_bf16 a[48:51], a[144:147], v[228:231], a[48:51]\n"
"s_mov_b32 m0, s86\n"
"v_exp_f32 v214, v214\n"
"v_mfma_f32_16x16x32_bf16 a[52:55], a[148:151], v[228:231], a[52:55]\n"
"buffer_load_dwordx4 v245, s[80:83], s87 offen nt lds\n"
"buffer_load_dwordx4 v246, s[80:83], s87 offen offset:1024 lds\n"
"v_mfma_f32_16x16x32_bf16 a[56:59], a[152:155], v[228:231], a[56:59]\n"
"s_waitcnt lgkmcnt(9)\n"
"v_exp_f32 v215, v215\n"
"v_mfma_f32_16x16x32_bf16 a[60:63], a[156:159], v[228:231], a[60:63]\n"
"v_add_f32 v241, v241, v214\n"
"v_add_f32 v241, v241, v215\n"
"v_cvt_pk_bf16_f32 v227, v214, v215\n"
"v_mfma_f32_16x16x32_bf16 v[208:211], a[224:227], v[0:3], v[232:235]\n"
"v_exp_f32 v216, v216\n"
"v_mfma_f32_16x16x32_bf16 v[208:211], a[228:231], v[4:7], v[208:211]\n"
"ds_read_b64_tr_b16 a[210:211], v250 offset:4608\n"
"v_add_f32 v242, v242, v216\n"
"v_mfma_f32_16x16x32_bf16 v[208:211], a[232:235], v[8:11], v[208:211]\n"
"s_add_u32 s87, s87, 2048\n"
"v_exp_f32 v217, v217\n"
"v_mfma_f32_16x16x32_bf16 v[208:211], a[236:239], v[12:15], v[208:211]\n"
"v_add_f32 v242, v242, v217\n"
"v_cvt_pk_bf16_f32 v228, v216, v217\n"
"v_mfma_f32_16x16x32_bf16 v[212:215], a[252:255], v[12:15], v[232:235]\n"
"s_and_b32 s87, s87, s98\n"
"v_exp_f32 v218, v218\n"
"v_mfma_f32_16x16x32_bf16 v[212:215], a[248:251], v[8:11], v[212:215]\n"
"ds_read_b64_tr_b16 a[212:213], v251 offset:512\n"
"v_add_f32 v242, v242, v218\n"
"v_mfma_f32_16x16x32_bf16 v[212:215], a[244:247], v[4:7], v[212:215]\n"
"s_add_u32 s86, s86, 2048\n"
"v_exp_f32 v219, v219\n"
"v_mfma_f32_16x16x32_bf16 v[212:215], a[240:243], v[0:3], v[212:215]\n"
"v_add_f32 v242, v242, v219\n"
"v_cvt_pk_bf16_f32 v229, v218, v219\n"
"v_mfma_f32_16x16x32_bf16 a[64:67], a[128:131], v[224:227], a[64:67]\n"
"s_cmp_eq_u32 s86, s89\n"
"v_exp_f32 v220, v220\n"
"v_mfma_f32_16x16x32_bf16 a[68:71], a[132:135], v[224:227], a[68:71]\n"
"ds_read_b64_tr_b16 a[214:215], v251 offset:4608\n"
"v_add_f32 v242, v242, v220\n"
"v_mfma_f32_16x16x32_bf16 a[72:75], a[136:139], v[224:227], a[72:75]\n"
"s_cselect_b32 s86, s66, s86\n"
"v_exp_f32 v221, v221\n"
"v_mfma_f32_16x16x32_bf16 a[76:79], a[140:143], v[224:227], a[76:79]\n"
"v_add_f32 v242, v242, v221\n"
"v_cvt_pk_bf16_f32 v230, v220, v221\n"
"v_mfma_f32_16x16x32_bf16 a[80:83], a[144:147], v[224:227], a[80:83]\n"
"v_exp_f32 v222, v222\n"
"v_mfma_f32_16x16x32_bf16 a[84:87], a[148:151], v[224:227], a[84:87]\n"
"ds_read_b64_tr_b16 a[216:217], v250 offset:768\n"
"v_add_f32 v242, v242, v222\n"
"v_mfma_f32_16x16x32_bf16 a[88:91], a[152:155], v[224:227], a[88:91]\n"
"v_exp_f32 v223, v223\n"
"v_mfma_f32_16x16x32_bf16 a[92:95], a[156:159], v[224:227], a[92:95]\n"
"v_add_f32 v242, v242, v223\n"
"v_cvt_pk_bf16_f32 v231, v222, v223\n"
"v_mfma_f32_16x16x32_bf16 v[216:219], a[224:227], v[16:19], v[232:235]\n"
"v_exp_f32 v208, v208\n"
"v_mfma_f32_16x16x32_bf16 v[216:219], a[228:231], v[20:23], v[216:219]\n"
"ds_read_b64_tr_b16 a[218:219], v250 offset:4864\n"
"v_add_f32 v236, v236, v208\n"
"v_mfma_f32_16x16x32_bf16 v[216:219], a[232:235], v[24:27], v[216:219]\n"
"v_exp_f32 v209, v209\n"
"v_mfma_f32_16x16x32_bf16 v[216:219], a[236:239], v[28:31], v[216:219]\n"
"v_add_f32 v236, v236, v209\n"
"v_cvt_pk_bf16_f32 v224, v208, v209\n"
"v_mfma_f32_16x16x32_bf16 v[220:223], a[252:255], v[28:31], v[232:235]\n"
"v_exp_f32 v210, v210\n"
"v_mfma_f32_16x16x32_bf16 v[220:223], a[248:251], v[24:27], v[220:223]\n"
"ds_read_b64_tr_b16 a[220:221], v251 offset:768\n"
"v_add_f32 v236, v236, v210\n"
"v_mfma_f32_16x16x32_bf16 v[220:223], a[244:247], v[20:23], v[220:223]\n"
"v_exp_f32 v211, v211\n"
"v_mfma_f32_16x16x32_bf16 v[220:223], a[240:243], v[16:19], v[220:223]\n"
"v_add_f32 v236, v236, v211\n"
"v_cvt_pk_bf16_f32 v225, v210, v211\n"
"v_mfma_f32_16x16x32_bf16 a[96:99], a[128:131], v[228:231], a[96:99]\n"
"v_exp_f32 v212, v212\n"
"v_mfma_f32_16x16x32_bf16 a[100:103], a[132:135], v[228:231], a[100:103]\n"
"ds_read_b64_tr_b16 a[222:223], v251 offset:4864\n"
"v_add_f32 v236, v236, v212\n"
"v_mfma_f32_16x16x32_bf16 a[104:107], a[136:139], v[228:231], a[104:107]\n"
"s_mov_b32 m0, s86\n"
"v_exp_f32 v213, v213\n"
"v_mfma_f32_16x16x32_bf16 a[108:111], a[140:143], v[228:231], a[108:111]\n"
"v_add_u32 v250, s90, v250\n"
"v_add_f32 v236, v236, v213\n"
"v_cvt_pk_bf16_f32 v226, v212, v213\n"
"v_mfma_f32_16x16x32_bf16 a[112:115], a[144:147], v[228:231], a[112:115]\n"
"s_bitcmp0_b32 21, s84\n"
"v_exp_f32 v214, v214\n"
"v_mfma_f32_16x16x32_bf16 a[116:119], a[148:151], v[228:231], a[116:119]\n"
"s_cbranch_scc1 Lnop\n"
"buffer_load_dwordx4 v245, s[80:83], s87 offen nt lds\n"
"buffer_load_dwordx4 v246, s[80:83], s87 offen offset:1024 lds\n"
"s_add_u32 s87, s87, 2048\n"
"s_and_b32 s87, s87, s98\n"
"s_add_u32 s86, s86, 2048\n"
"s_cmp_eq_u32 s86, s89\n"
"s_cselect_b32 s86, s66, s86\n"
"Lnop:\n"
"v_mfma_f32_16x16x32_bf16 a[120:123], a[152:155], v[228:231], a[120:123]\n"
"v_exp_f32 v215, v215\n"
"v_mfma_f32_16x16x32_bf16 a[124:127], a[156:159], v[228:231], a[124:127]\n"
"v_add_u32 v251, s90, v251\n"
"v_add_f32 v236, v236, v214\n"
"v_add_f32 v236, v236, v215\n"
"v_cvt_pk_bf16_f32 v227, v214, v215\n"
"; attn block 15\n"
"v_mfma_f32_16x16x32_bf16 v[208:211], a[224:227], v[32:35], v[232:235]\n"
"s_waitcnt vmcnt(8) lgkmcnt(0)\n"
"s_barrier\n"
"v_exp_f32 v216, v216\n"
"v_mfma_f32_16x16x32_bf16 v[208:211], a[228:231], v[36:39], v[208:211]\n"
"ds_read_b128 a[160:163], v249 offset:0\n"
"v_add_f32 v237, v237, v216\n"
"v_mfma_f32_16x16x32_bf16 v[208:211], a[232:235], v[40:43], v[208:211]\n"
"v_exp_f32 v217, v217\n"
"v_mfma_f32_16x16x32_bf16 v[208:211], a[236:239], v[44:47], v[208:211]\n"
"v_add_f32 v237, v237, v217\n"
"v_cvt_pk_bf16_f32 v228, v216, v217\n"
"v_mfma_f32_16x16x32_bf16 v[212:215], a[252:255], v[44:47], v[232:235]\n"
"v_exp_f32 v218, v218\n"
"v_mfma_f32_16x16x32_bf16 v[212:215], a[248:251], v[40:43], v[212:215]\n"
"ds_read_b128 a[164:167], v249 offset:256\n"
"v_add_f32 v237, v237, v218\n"
"v_mfma_f32_16x16x32_bf16 v[212:215], a[244:247], v[36:39], v[212:215]\n"
"v_exp_f32 v219, v219\n"
"v_mfma_f32_16x16x32_bf16 v[212:215], a[240:243], v[32:35], v[212:215]\n"
"v_add_f32 v237, v237, v219\n"
"v_cvt_pk_bf16_f32 v229, v218, v219\n"
"v_mfma_f32_16x16x32_bf16 v[112:115], a[192:195], v[224:227], v[112:115]\n"
"v_exp_f32 v220, v220\n"
"v_mfma_f32_16x16x32_bf16 v[116:119], a[196:199], v[224:227], v[116:119]\n"
"ds_read_b128 a[168:171], v249 offset:512\n"
"v_add_f32 v237, v237, v220\n"
"v_mfma_f32_16x16x32_bf16 v[120:123], a[200:203], v[224:227], v[120:123]\n"
"v_exp_f32 v221, v221\n"
"v_mfma_f32_16x16x32_bf16 v[124:127], a[204:207], v[224:227], v[124:127]\n"
"v_add_f32 v237, v237, v221\n"
"v_cvt_pk_bf16_f32 v230, v220, v221\n"
"v_mfma_f32_16x16x32_bf16 v[128:131], a[208:211], v[224:227], v[128:131]\n"
"v_exp_f32 v222, v222\n"
"v_mfma_f32_16x16x32_bf16 v[132:135], a[212:215], v[224:227], v[132:135]\n"
"ds_read_b128 a[172:175], v249 offset:768\n"
"v_add_f32 v237, v237, v222\n"
"v_mfma_f32_16x16x32_bf16 v[136:139], a[216:219], v[224:227], v[136:139]\n"
"v_exp_f32 v223, v223\n"
"v_mfma_f32_16x16x32_bf16 v[140:143], a[220:223], v[224:227], v[140:143]\n"
"v_add_f32 v237, v237, v223\n"
"v_cvt_pk_bf16_f32 v231, v222, v223\n"
"v_mfma_f32_16x16x32_bf16 v[216:219], a[224:227], v[48:51], v[232:235]\n"
"v_exp_f32 v208, v208\n"
"v_mfma_f32_16x16x32_bf16 v[216:219], a[228:231], v[52:55], v[216:219]\n"
"ds_read_b128 a[176:179], v249 offset:4096\n"
"v_add_f32 v238, v238, v208\n"
"v_mfma_f32_16x16x32_bf16 v[216:219], a[232:235], v[56:59], v[216:219]\n"
"s_add_u32 s88, s88, s90\n"
"v_exp_f32 v209, v209\n"
"v_mfma_f32_16x16x32_bf16 v[216:219], a[236:239], v[60:63], v[216:219]\n"
"v_add_f32 v238, v238, v209\n"
"v_cvt_pk_bf16_f32 v224, v208, v209\n"
"v_mfma_f32_16x16x32_bf16 v[220:223], a[252:255], v[60:63], v[232:235]\n"
"s_cmp_eq_u32 s88, 32768\n"
"v_exp_f32 v210, v210\n"
"v_mfma_f32_16x16x32_bf16 v[220:223], a[248:251], v[56:59], v[220:223]\n"
"ds_read_b128 a[180:183], v249 offset:4352\n"
"v_add_f32 v238, v238, v210\n"
"v_mfma_f32_16x16x32_bf16 v[220:223], a[244:247], v[52:55], v[220:223]\n"
"s_cselect_b32 s90, 0xffff8000, s96\n"
"v_exp_f32 v211, v211\n"
"v_mfma_f32_16x16x32_bf16 v[220:223], a[240:243], v[48:51], v[220:223]\n"
"v_add_f32 v238, v238, v211\n"
"v_cvt_pk_bf16_f32 v225, v210, v211\n"
"v_mfma_f32_16x16x32_bf16 v[144:147], a[192:195], v[228:231], v[144:147]\n"
"v_exp_f32 v212, v212\n"
"v_mfma_f32_16x16x32_bf16 v[148:151], a[196:199], v[228:231], v[148:151]\n"
"ds_read_b128 a[184:187], v249 offset:4608\n"
"v_add_f32 v238, v238, v212\n"
"v_mfma_f32_16x16x32_bf16 v[152:155], a[200:203], v[228:231], v[152:155]\n"
"v_exp_f32 v213, v213\n"
"v_mfma_f32_16x16x32_bf16 v[156:159], a[204:207], v[228:231], v[156:159]\n"
"v_add_f32 v238, v238, v213\n"
"v_cvt_pk_bf16_f32 v226, v212, v213\n"
"v_mfma_f32_16x16x32_bf16 v[160:163], a[208:211], v[228:231], v[160:163]\n"
"s_mov_b32 m0, s86\n"
"v_exp_f32 v214, v214\n"
"v_mfma_f32_16x16x32_bf16 v[164:167], a[212:215], v[228:231], v[164:167]\n"
"buffer_load_dwordx4 v245, s[80:83], s87 offen nt lds\n"
"buffer_load_dwordx4 v246, s[80:83], s87 offen offset:1024 lds\n"
"v_mfma_f32_16x16x32_bf16 v[168:171], a[216:219], v[228:231], v[168:171]\n"
"v_exp_f32 v215, v215\n"
"v_mfma_f32_16x16x32_bf16 v[172:175], a[220:223], v[228:231], v[172:175]\n"
"v_add_f32 v238, v238, v214\n"
"v_add_f32 v238, v238, v215\n"
"v_cvt_pk_bf16_f32 v227, v214, v215\n"
"v_mfma_f32_16x16x32_bf16 v[208:211], a[224:227], v[64:67], v[232:235]\n"
"v_exp_f32 v216, v216\n"
"v_mfma_f32_16x16x32_bf16 v[208:211], a[228:231], v[68:71], v[208:211]\n"
"ds_read_b128 a[188:191], v249 offset:4864\n"
"v_add_f32 v239, v239, v216\n"
"v_mfma_f32_16x16x32_bf16 v[208:211], a[232:235], v[72:75], v[208:211]\n"
"s_add_u32 s87, s87, 2048\n"
"v_exp_f32 v217, v217\n"
"v_mfma_f32_16x16x32_bf16 v[208:211], a[236:239], v[76:79], v[208:211]\n"
"v_add_f32 v239, v239, v217\n"
"v_cvt_pk_bf16_f32 v228, v216, v217\n"
"v_mfma_f32_16x16x32_bf16 v[212:215], a[252:255], v[76:79], v[232:235]\n"
"s_and_b32 s87, s87, s98\n"
"v_exp_f32 v218, v218\n"
"v_mfma_f32_16x16x32_bf16 v[212:215], a[248:251], v[72:75], v[212:215]\n"
"ds_read_b64_tr_b16 a[128:129], v250 offset:0\n"
"v_add_f32 v239, v239, v218\n"
"v_mfma_f32_16x16x32_bf16 v[212:215], a[244:247], v[68:71], v[212:215]\n"
"s_add_u32 s86, s86, 2048\n"
"v_exp_f32 v219, v219\n"
"v_mfma_f32_16x16x32_bf16 v[212:215], a[240:243], v[64:67], v[212:215]\n"
"v_add_f32 v239, v239, v219\n"
"v_cvt_pk_bf16_f32 v229, v218, v219\n"
"v_mfma_f32_16x16x32_bf16 v[176:179], a[192:195], v[224:227], v[176:179]\n"
"s_cmp_eq_u32 s86, s89\n"
"v_exp_f32 v220, v220\n"
"v_mfma_f32_16x16x32_bf16 v[180:183], a[196:199], v[224:227], v[180:183]\n"
"ds_read_b64_tr_b16 a[130:131], v250 offset:4096\n"
"v_add_f32 v239, v239, v220\n"
"v_mfma_f32_16x16x32_bf16 v[184:187], a[200:203], v[224:227], v[184:187]\n"
"s_cselect_b32 s86, s66, s86\n"
"v_exp_f32 v221, v221\n"
"v_mfma_f32_16x16x32_bf16 v[188:191], a[204:207], v[224:227], v[188:191]\n"
"v_add_f32 v239, v239, v221\n"
"v_cvt_pk_bf16_f32 v230, v220, v221\n"
"v_mfma_f32_16x16x32_bf16 v[192:195], a[208:211], v[224:227], v[192:195]\n"
"v_exp_f32 v222, v222\n"
"v_mfma_f32_16x16x32_bf16 v[196:199], a[212:215], v[224:227], v[196:199]\n"
"ds_read_b64_tr_b16 a[132:133], v251 offset:0\n"
"v_add_f32 v239, v239, v222\n"
"v_mfma_f32_16x16x32_bf16 v[200:203], a[216:219], v[224:227], v[200:203]\n"
"v_exp_f32 v223, v223\n"
"v_mfma_f32_16x16x32_bf16 v[204:207], a[220:223], v[224:227], v[204:207]\n"
"v_add_f32 v239, v239, v223\n"
"v_cvt_pk_bf16_f32 v231, v222, v223\n"
"v_mfma_f32_16x16x32_bf16 v[216:219], a[224:227], v[80:83], v[232:235]\n"
"v_exp_f32 v208, v208\n"
"v_mfma_f32_16x16x32_bf16 v[216:219], a[228:231], v[84:87], v[216:219]\n"
"ds_read_b64_tr_b16 a[134:135], v251 offset:4096\n"
"v_add_f32 v240, v240, v208\n"
"v_mfma_f32_16x16x32_bf16 v[216:219], a[232:235], v[88:91], v[216:219]\n"
"v_exp_f32 v209, v209\n"
"v_mfma_f32_16x16x32_bf16 v[216:219], a[236:239], v[92:95], v[216:219]\n"
"v_add_u32 v249, s90, v249\n"
"v_add_f32 v240, v240, v209\n"
"v_cvt_pk_bf16_f32 v224, v208, v209\n"
"v_mfma_f32_16x16x32_bf16 v[220:223], a[252:255], v[92:95], v[232:235]\n"
"v_exp_f32 v210, v210\n"
"v_mfma_f32_16x16x32_bf16 v[220:223], a[248:251], v[88:91], v[220:223]\n"
"ds_read_b64_tr_b16 a[136:137], v250 offset:256\n"
"v_add_f32 v240, v240, v210\n"
"v_mfma_f32_16x16x32_bf16 v[220:223], a[244:247], v[84:87], v[220:223]\n"
"v_exp_f32 v211, v211\n"
"v_mfma_f32_16x16x32_bf16 v[220:223], a[240:243], v[80:83], v[220:223]\n"
"v_add_f32 v240, v240, v211\n"
"v_cvt_pk_bf16_f32 v225, v210, v211\n"
"v_mfma_f32_16x16x32_bf16 a[0:3], a[192:195], v[228:231], a[0:3]\n"
"v_exp_f32 v212, v212\n"
"v_mfma_f32_16x16x32_bf16 a[4:7], a[196:199], v[228:231], a[4:7]\n"
"ds_read_b64_tr_b16 a[138:139], v250 offset:4352\n"
"v_add_f32 v240, v240, v212\n"
"v_mfma_f32_16x16x32_bf16 a[8:11], a[200:203], v[228:231], a[8:11]\n"
"v_exp_f32 v213, v213\n"
"v_mfma_f32_16x16x32_bf16 a[12:15], a[204:207], v[228:231], a[12:15]\n"
"v_add_f32 v240, v240, v213\n"
"v_cvt_pk_bf16_f32 v226, v212, v213\n"
"v_mfma_f32_16x16x32_bf16 a[16:19], a[208:211], v[228:231], a[16:19]\n"
"s_mov_b32 m0, s86\n"
"v_exp_f32 v214, v214\n"
"v_mfma_f32_16x16x32_bf16 a[20:23], a[212:215], v[228:231], a[20:23]\n"
"buffer_load_dwordx4 v245, s[80:83], s87 offen nt lds\n"
"buffer_load_dwordx4 v246, s[80:83], s87 offen offset:1024 lds\n"
"v_mfma_f32_16x16x32_bf16 a[24:27], a[216:219], v[228:231], a[24:27]\n"
"v_exp_f32 v215, v215\n"
"v_mfma_f32_16x16x32_bf16 a[28:31], a[220:223], v[228:231], a[28:31]\n"
"v_add_f32 v240, v240, v214\n"
"v_add_f32 v240, v240, v215\n"
"v_cvt_pk_bf16_f32 v227, v214, v215\n"
"v_mfma_f32_16x16x32_bf16 v[208:211], a[224:227], v[96:99], v[232:235]\n"
"v_exp_f32 v216, v216\n"
"v_mfma_f32_16x16x32_bf16 v[208:211], a[228:231], v[100:103], v[208:211]\n"
"ds_read_b64_tr_b16 a[140:141], v251 offset:256\n"
"v_add_f32 v241, v241, v216\n"
"v_mfma_f32_16x16x32_bf16 v[208:211], a[232:235], v[104:107], v[208:211]\n"
"s_add_u32 s87, s87, 2048\n"
"v_exp_f32 v217, v217\n"
"v_mfma_f32_16x16x32_bf16 v[208:211], a[236:239], v[108:111], v[208:211]\n"
"v_add_f32 v241, v241, v217\n"
"v_cvt_pk_bf16_f32 v228, v216, v217\n"
"v_mfma_f32_16x16x32_bf16 v[212:215], a[252:255], v[108:111], v[232:235]\n"
"s_and_b32 s87, s87, s98\n"
"v_exp_f32 v218, v218\n"
"v_mfma_f32_16x16x32_bf16 v[212:215], a[248:251], v[104:107], v[212:215]\n"
"ds_read_b64_tr_b16 a[142:143], v251 offset:4352\n"
"v_add_f32 v241, v241, v218\n"
"v_mfma_f32_16x16x32_bf16 v[212:215], a[244:247], v[100:103], v[212:215]\n"
"s_add_u32 s86, s86, 2048\n"
"v_exp_f32 v219, v219\n"
"v_mfma_f32_16x16x32_bf16 v[212:215], a[240:243], v[96:99], v[212:215]\n"
"v_add_f32 v241, v241, v219\n"
"v_cvt_pk_bf16_f32 v229, v218, v219\n"
"v_mfma_f32_16x16x32_bf16 a[32:35], a[192:195], v[224:227], a[32:35]\n"
"s_cmp_eq_u32 s86, s89\n"
"v_exp_f32 v220, v220\n"
"v_mfma_f32_16x16x32_bf16 a[36:39], a[196:199], v[224:227], a[36:39]\n"
"ds_read_b64_tr_b16 a[144:145], v250 offset:512\n"
"v_add_f32 v241, v241, v220\n"
"v_mfma_f32_16x16x32_bf16 a[40:43], a[200:203], v[224:227], a[40:43]\n"
"s_cselect_b32 s86, s66, s86\n"
"v_exp_f32 v221, v221\n"
"v_mfma_f32_16x16x32_bf16 a[44:47], a[204:207], v[224:227], a[44:47]\n"
"v_add_f32 v241, v241, v221\n"
"v_cvt_pk_bf16_f32 v230, v220, v221\n"
"v_mfma_f32_16x16x32_bf16 a[48:51], a[208:211], v[224:227], a[48:51]\n"
"v_exp_f32 v222, v222\n"
"v_mfma_f32_16x16x32_bf16 a[52:55], a[212:215], v[224:227], a[52:55]\n"
"ds_read_b64_tr_b16 a[146:147], v250 offset:4608\n"
"v_add_f32 v241, v241, v222\n"
"v_mfma_f32_16x16x32_bf16 a[56:59], a[216:219], v[224:227], a[56:59]\n"
"s_waitcnt lgkmcnt(10)\n"
"v_exp_f32 v223, v223\n"
"v_mfma_f32_16x16x32_bf16 a[60:63], a[220:223], v[224:227], a[60:63]\n"
"v_add_f32 v241, v241, v223\n"
"v_cvt_pk_bf16_f32 v231, v222, v223\n"
"v_mfma_f32_16x16x32_bf16 v[216:219], a[160:163], v[0:3], v[232:235]\n"
"v_exp_f32 v208, v208\n"
"v_mfma_f32_16x16x32_bf16 v[216:219], a[164:167], v[4:7], v[216:219]\n"
"ds_read_b64_tr_b16 a[148:149], v251 offset:512\n"
"v_add_f32 v242, v242, v208\n"
"v_mfma_f32_16x16x32_bf16 v[216:219], a[168:171], v[8:11], v[216:219]\n"
"v_exp_f32 v209, v209\n"
"v_mfma_f32_16x16x32_bf16 v[216:219], a[172:175], v[12:15], v[216:219]\n"
"v_add_f32 v242, v242, v209\n"
"v_cvt_pk_bf16_f32 v224, v208, v209\n"
"v_mfma_f32_16x16x32_bf16 v[220:223], a[188:191], v[12:15], v[232:235]\n"
"v_exp_f32 v210, v210\n"
"v_mfma_f32_16x16x32_bf16 v[220:223], a[184:187], v[8:11], v[220:223]\n"
"ds_read_b64_tr_b16 a[150:151], v251 offset:4608\n"
"v_add_f32 v242, v242, v210\n"
"v_mfma_f32_16x16x32_bf16 v[220:223], a[180:183], v[4:7], v[220:223]\n"
"v_exp_f32 v211, v211\n"
"v_mfma_f32_16x16x32_bf16 v[220:223], a[176:179], v[0:3], v[220:223]\n"
"v_add_f32 v242, v242, v211\n"
"v_cvt_pk_bf16_f32 v225, v210, v211\n"
"v_mfma_f32_16x16x32_bf16 a[64:67], a[192:195], v[228:231], a[64:67]\n"
"v_exp_f32 v212, v212\n"
"v_mfma_f32_16x16x32_bf16 a[68:71], a[196:199], v[228:231], a[68:71]\n"
"ds_read_b64_tr_b16 a[152:153], v250 offset:768\n"
"v_add_f32 v242, v242, v212\n"
"v_mfma_f32_16x16x32_bf16 a[72:75], a[200:203], v[228:231], a[72:75]\n"
"v_exp_f32 v213, v213\n"
"v_mfma_f32_16x16x32_bf16 a[76:79], a[204:207], v[228:231], a[76:79]\n"
"v_add_f32 v242, v242, v213\n"
"v_cvt_pk_bf16_f32 v226, v212, v213\n"
"v_mfma_f32_16x16x32_bf16 a[80:83], a[208:211], v[228:231], a[80:83]\n"
"s_mov_b32 m0, s86\n"
"v_exp_f32 v214, v214\n"
"v_mfma_f32_16x16x32_bf16 a[84:87], a[212:215], v[228:231], a[84:87]\n"
"buffer_load_dwordx4 v245, s[80:83], s87 offen nt lds\n"
"buffer_load_dwordx4 v246, s[80:83], s87 offen offset:1024 lds\n"
"v_mfma_f32_16x16x32_bf16 a[88:91], a[216:219], v[228:231], a[88:91]\n"
"v_exp_f32 v215, v215\n"
"v_mfma_f32_16x16x32_bf16 a[92:95], a[220:223], v[228:231], a[92:95]\n"
"v_add_f32 v242, v242, v214\n"
"v_add_f32 v242, v242, v215\n"
"v_cvt_pk_bf16_f32 v227, v214, v215\n"
"v_mfma_f32_16x16x32_bf16 v[208:211], a[160:163], v[16:19], v[232:235]\n"
"v_exp_f32 v216, v216\n"
"v_mfma_f32_16x16x32_bf16 v[208:211], a[164:167], v[20:23], v[208:211]\n"
"ds_read_b64_tr_b16 a[154:155], v250 offset:4864\n"
"v_add_f32 v236, v236, v216\n"
"v_mfma_f32_16x16x32_bf16 v[208:211], a[168:171], v[24:27], v[208:211]\n"
"s_add_u32 s87, s87, 2048\n"
"v_exp_f32 v217, v217\n"
"v_mfma_f32_16x16x32_bf16 v[208:211], a[172:175], v[28:31], v[208:211]\n"
"v_add_f32 v236, v236, v217\n"
"v_cvt_pk_bf16_f32 v228, v216, v217\n"
"v_mfma_f32_16x16x32_bf16 v[212:215], a[188:191], v[28:31], v[232:235]\n"
"s_and_b32 s87, s87, s98\n"
"v_exp_f32 v218, v218\n"
"v_mfma_f32_16x16x32_bf16 v[212:215], a[184:187], v[24:27], v[212:215]\n"
"ds_read_b64_tr_b16 a[156:157], v251 offset:768\n"
"v_add_f32 v236, v236, v218\n"
"v_mfma_f32_16x16x32_bf16 v[212:215], a[180:183], v[20:23], v[212:215]\n"
"s_add_u32 s86, s86, 2048\n"
"v_exp_f32 v219, v219\n"
"v_mfma_f32_16x16x32_bf16 v[212:215], a[176:179], v[16:19], v[212:215]\n"
"v_add_f32 v236, v236, v219\n"
"v_cvt_pk_bf16_f32 v229, v218, v219\n"
"v_mfma_f32_16x16x32_bf16 a[96:99], a[192:195], v[224:227], a[96:99]\n"
"s_cmp_eq_u32 s86, s89\n"
"v_exp_f32 v220, v220\n"
"v_mfma_f32_16x16x32_bf16 a[100:103], a[196:199], v[224:227], a[100:103]\n"
"ds_read_b64_tr_b16 a[158:159], v251 offset:4864\n"
"v_add_f32 v236, v236, v220\n"
"v_mfma_f32_16x16x32_bf16 a[104:107], a[200:203], v[224:227], a[104:107]\n"
"s_cselect_b32 s86, s66, s86\n"
"v_exp_f32 v221, v221\n"
"v_mfma_f32_16x16x32_bf16 a[108:111], a[204:207], v[224:227], a[108:111]\n"
"v_add_u32 v250, s90, v250\n"
"v_add_f32 v236, v236, v221\n"
"v_cvt_pk_bf16_f32 v230, v220, v221\n"
"v_mfma_f32_16x16x32_bf16 a[112:115], a[208:211], v[224:227], a[112:115]\n"
"v_exp_f32 v222, v222\n"
"v_mfma_f32_16x16x32_bf16 a[116:119], a[212:215], v[224:227], a[116:119]\n"
"v_add_u32 v251, s90, v251\n"
"v_add_f32 v236, v236, v222\n"
"v_mfma_f32_16x16x32_bf16 a[120:123], a[216:219], v[224:227], a[120:123]\n"
"s_cmp_lg_u32 s84, 0\n"
"v_exp_f32 v223, v223\n"
"v_mfma_f32_16x16x32_bf16 a[124:127], a[220:223], v[224:227], a[124:127]\n"
"s_cbranch_scc1 La\n"
"v_add_f32 v236, v236, v223\n"
"v_cvt_pk_bf16_f32 v231, v222, v223\n"
"; attn block 92\n"
"v_mfma_f32_16x16x32_bf16 v[216:219], a[160:163], v[32:35], v[232:235]\n"
"s_waitcnt vmcnt(6) lgkmcnt(0)\n"
"s_barrier\n"
"v_exp_f32 v208, v208\n"
"v_mfma_f32_16x16x32_bf16 v[216:219], a[164:167], v[36:39], v[216:219]\n"
"ds_read_b128 a[224:227], v249 offset:0\n"
"v_add_f32 v237, v237, v208\n"
"v_mfma_f32_16x16x32_bf16 v[216:219], a[168:171], v[40:43], v[216:219]\n"
"v_exp_f32 v209, v209\n"
"v_mfma_f32_16x16x32_bf16 v[216:219], a[172:175], v[44:47], v[216:219]\n"
"v_add_f32 v237, v237, v209\n"
"v_cvt_pk_bf16_f32 v224, v208, v209\n"
"v_mfma_f32_16x16x32_bf16 v[220:223], a[188:191], v[44:47], v[232:235]\n"
"v_exp_f32 v210, v210\n"
"v_mfma_f32_16x16x32_bf16 v[220:223], a[184:187], v[40:43], v[220:223]\n"
"ds_read_b128 a[228:231], v249 offset:256\n"
"v_add_f32 v237, v237, v210\n"
"v_mfma_f32_16x16x32_bf16 v[220:223], a[180:183], v[36:39], v[220:223]\n"
"v_exp_f32 v211, v211\n"
"v_mfma_f32_16x16x32_bf16 v[220:223], a[176:179], v[32:35], v[220:223]\n"
"v_add_f32 v237, v237, v211\n"
"v_cvt_pk_bf16_f32 v225, v210, v211\n"
"v_mfma_f32_16x16x32_bf16 v[112:115], a[128:131], v[228:231], v[112:115]\n"
"v_exp_f32 v212, v212\n"
"v_mfma_f32_16x16x32_bf16 v[116:119], a[132:135], v[228:231], v[116:119]\n"
"ds_read_b128 a[232:235], v249 offset:512\n"
"v_add_f32 v237, v237, v212\n"
"v_mfma_f32_16x16x32_bf16 v[120:123], a[136:139], v[228:231], v[120:123]\n"
"v_exp_f32 v213, v213\n"
"v_mfma_f32_16x16x32_bf16 v[124:127], a[140:143], v[228:231], v[124:127]\n"
"v_add_f32 v237, v237, v213\n"
"v_cvt_pk_bf16_f32 v226, v212, v213\n"
"v_mfma_f32_16x16x32_bf16 v[128:131], a[144:147], v[228:231], v[128:131]\n"
"s_mov_b32 m0, s86\n"
"v_exp_f32 v214, v214\n"
"v_mfma_f32_16x16x32_bf16 v[132:135], a[148:151], v[228:231], v[132:135]\n"
"buffer_load_dwordx4 v245, s[80:83], s87 offen nt lds\n"
"buffer_load_dwordx4 v246, s[80:83], s87 offen offset:1024 lds\n"
"v_mfma_f32_16x16x32_bf16 v[136:139], a[152:155], v[228:231], v[136:139]\n"
"v_exp_f32 v215, v215\n"
"v_mfma_f32_16x16x32_bf16 v[140:143], a[156:159], v[228:231], v[140:143]\n"
"v_add_f32 v237, v237, v214\n"
"v_add_f32 v237, v237, v215\n"
"v_cvt_pk_bf16_f32 v227, v214, v215\n"
"v_mfma_f32_16x16x32_bf16 v[208:211], a[160:163], v[48:51], v[232:235]\n"
"v_exp_f32 v216, v216\n"
"v_mfma_f32_16x16x32_bf16 v[208:211], a[164:167], v[52:55], v[208:211]\n"
"ds_read_b128 a[236:239], v249 offset:768\n"
"v_add_f32 v238, v238, v216\n"
"v_mfma_f32_16x16x32_bf16 v[208:211], a[168:171], v[56:59], v[208:211]\n"
"s_add_u32 s87, s87, 2048\n"
"v_exp_f32 v217, v217\n"
"v_mfma_f32_16x16x32_bf16 v[208:211], a[172:175], v[60:63], v[208:211]\n"
"v_add_f32 v238, v238, v217\n"
"v_cvt_pk_bf16_f32 v228, v216, v217\n"
"v_mfma_f32_16x16x32_bf16 v[212:215], a[188:191], v[60:63], v[232:235]\n"
"s_and_b32 s87, s87, s98\n"
"v_exp_f32 v218, v218\n"
"v_mfma_f32_16x16x32_bf16 v[212:215], a[184:187], v[56:59], v[212:215]\n"
"ds_read_b128 a[240:243], v249 offset:4096\n"
"v_add_f32 v238, v238, v218\n"
"v_mfma_f32_16x16x32_bf16 v[212:215], a[180:183], v[52:55], v[212:215]\n"
"s_add_u32 s86, s86, 2048\n"
"v_exp_f32 v219, v219\n"
"v_mfma_f32_16x16x32_bf16 v[212:215], a[176:179], v[48:51], v[212:215]\n"
"v_add_f32 v238, v238, v219\n"
"v_cvt_pk_bf16_f32 v229, v218, v219\n"
"v_mfma_f32_16x16x32_bf16 v[144:147], a[128:131], v[224:227], v[144:147]\n"
"s_cmp_eq_u32 s86, s89\n"
"v_exp_f32 v220, v220\n"
"v_mfma_f32_16x16x32_bf16 v[148:151], a[132:135], v[224:227], v[148:151]\n"
"ds_read_b128 a[244:247], v249 offset:4352\n"
"v_add_f32 v238, v238, v220\n"
"v_mfma_f32_16x16x32_bf16 v[152:155], a[136:139], v[224:227], v[152:155]\n"
"s_cselect_b32 s86, s66, s86\n"
"v_exp_f32 v221, v221\n"
"v_mfma_f32_16x16x32_bf16 v[156:159], a[140:143], v[224:227], v[156:159]\n"
"v_add_f32 v238, v238, v221\n"
"v_cvt_pk_bf16_f32 v230, v220, v221\n"
"v_mfma_f32_16x16x32_bf16 v[160:163], a[144:147], v[224:227], v[160:163]\n"
"v_exp_f32 v222, v222\n"
"v_mfma_f32_16x16x32_bf16 v[164:167], a[148:151], v[224:227], v[164:167]\n"
"ds_read_b128 a[248:251], v249 offset:4608\n"
"v_add_f32 v238, v238, v222\n"
"v_mfma_f32_16x16x32_bf16 v[168:171], a[152:155], v[224:227], v[168:171]\n"
"v_exp_f32 v223, v223\n"
"v_mfma_f32_16x16x32_bf16 v[172:175], a[156:159], v[224:227], v[172:175]\n"
"v_add_f32 v238, v238, v223\n"
"v_cvt_pk_bf16_f32 v231, v222, v223\n"
"v_mfma_f32_16x16x32_bf16 v[216:219], a[160:163], v[64:67], v[232:235]\n"
"v_exp_f32 v208, v208\n"
"v_mfma_f32_16x16x32_bf16 v[216:219], a[164:167], v[68:71], v[216:219]\n"
"ds_read_b128 a[252:255], v249 offset:4864\n"
"v_add_f32 v239, v239, v208\n"
"v_mfma_f32_16x16x32_bf16 v[216:219], a[168:171], v[72:75], v[216:219]\n"
"s_add_u32 s88, s88, s90\n"
"v_exp_f32 v209, v209\n"
"v_mfma_f32_16x16x32_bf16 v[216:219], a[172:175], v[76:79], v[216:219]\n"
"v_add_f32 v239, v239, v209\n"
"v_cvt_pk_bf16_f32 v224, v208, v209\n"
"v_mfma_f32_16x16x32_bf16 v[220:223], a[188:191], v[76:79], v[232:235]\n"
"s_cmp_eq_u32 s88, 32768\n"
"v_exp_f32 v210, v210\n"
"v_mfma_f32_16x16x32_bf16 v[220:223], a[184:187], v[72:75], v[220:223]\n"
"ds_read_b64_tr_b16 a[192:193], v250 offset:0\n"
"v_add_f32 v239, v239, v210\n"
"v_mfma_f32_16x16x32_bf16 v[220:223], a[180:183], v[68:71], v[220:223]\n"
"s_cselect_b32 s90, 0xffff8000, s96\n"
"v_exp_f32 v211, v211\n"
"Lh6:\n"
"s_cbranch_execz Lw1\n"
"v_mfma_f32_16x16x32_bf16 v[220:223], a[176:179], v[64:67], v[220:223]\n"
"v_add_f32 v239, v239, v211\n"
"v_cvt_pk_bf16_f32 v225, v210, v211\n"
"v_mfma_f32_16x16x32_bf16 v[176:179], a[128:131], v[228:231], v[176:179]\n"
"s_add_u32 m0, s66, 8192\n"
"v_exp_f32 v212, v212\n"
"v_mfma_f32_16x16x32_bf16 v[180:183], a[132:135], v[228:231], v[180:183]\n"
"ds_read_b64_tr_b16 a[194:195], v250 offset:4096\n"
"v_add_f32 v239, v239, v212\n"
"v_mfma_f32_16x16x32_bf16 v[184:187], a[136:139], v[228:231], v[184:187]\n"
"s_add_u32 s62, s72, 0\n"
"v_exp_f32 v213, v213\n"
"v_mfma_f32_16x16x32_bf16 v[188:191], a[140:143], v[228:231], v[188:191]\n"
"v_add_f32 v239, v239, v213\n"
"v_cvt_pk_bf16_f32 v226, v212, v213\n"
"v_mfma_f32_16x16x32_bf16 v[192:195], a[144:147], v[228:231], v[192:195]\n"
"s_add_u32 s63, s72, 8192\n"
"v_exp_f32 v214, v214\n"
"v_mfma_f32_16x16x32_bf16 v[196:199], a[148:151], v[228:231], v[196:199]\n"
"buffer_load_dwordx4 v252, s[76:79], s62 offen lds\n"
"buffer_load_dwordx4 v252, s[76:79], s63 offen offset:1024 lds\n"
"v_mfma_f32_16x16x32_bf16 v[200:203], a[152:155], v[228:231], v[200:203]\n"
"v_exp_f32 v215, v215\n"
"v_mfma_f32_16x16x32_bf16 v[204:207], a[156:159], v[228:231], v[204:207]\n"
"v_add_f32 v239, v239, v214\n"
"v_add_f32 v239, v239, v215\n"
"v_cvt_pk_bf16_f32 v227, v214, v215\n"
"v_mfma_f32_16x16x32_bf16 v[208:211], a[160:163], v[80:83], v[232:235]\n"
"v_exp_f32 v216, v216\n"
"v_mfma_f32_16x16x32_bf16 v[208:211], a[164:167], v[84:87], v[208:211]\n"
"ds_read_b64_tr_b16 a[196:197], v251 offset:0\n"
"v_add_f32 v240, v240, v216\n"
"v_mfma_f32_16x16x32_bf16 v[208:211], a[168:171], v[88:91], v[208:211]\n"
"v_exp_f32 v217, v217\n"
"v_mfma_f32_16x16x32_bf16 v[208:211], a[172:175], v[92:95], v[208:211]\n"
"v_add_u32 v249, s90, v249\n"
"v_add_f32 v240, v240, v217\n"
"v_cvt_pk_bf16_f32 v228, v216, v217\n"
"v_mfma_f32_16x16x32_bf16 v[212:215], a[188:191], v[92:95], v[232:235]\n"
"v_exp_f32 v218, v218\n"
"v_mfma_f32_16x16x32_bf16 v[212:215], a[184:187], v[88:91], v[212:215]\n"
"ds_read_b64_tr_b16 a[198:199], v251 offset:4096\n"
"v_add_f32 v240, v240, v218\n"
"v_mfma_f32_16x16x32_bf16 v[212:215], a[180:183], v[84:87], v[212:215]\n"
"v_exp_f32 v219, v219\n"
"v_mfma_f32_16x16x32_bf16 v[212:215], a[176:179], v[80:83], v[212:215]\n"
"v_add_f32 v240, v240, v219\n"
"v_cvt_pk_bf16_f32 v229, v218, v219\n"
"v_mfma_f32_16x16x32_bf16 a[0:3], a[128:131], v[224:227], a[0:3]\n"
"v_exp_f32 v220, v220\n"
"v_mfma_f32_16x16x32_bf16 a[4:7], a[132:135], v[224:227], a[4:7]\n"
"ds_read_b64_tr_b16 a[200:201], v250 offset:256\n"
"v_add_f32 v240, v240, v220\n"
"v_mfma_f32_16x16x32_bf16 a[8:11], a[136:139], v[224:227], a[8:11]\n"
"v_exp_f32 v221, v221\n"
"v_mfma_f32_16x16x32_bf16 a[12:15], a[140:143], v[224:227], a[12:15]\n"
"v_add_f32 v240, v240, v221\n"
"v_cvt_pk_bf16_f32 v230, v220, v221\n"
"v_mfma_f32_16x16x32_bf16 a[16:19], a[144:147], v[224:227], a[16:19]\n"
"v_exp_f32 v222, v222\n"
"v_mfma_f32_16x16x32_bf16 a[20:23], a[148:151], v[224:227], a[20:23]\n"
"ds_read_b64_tr_b16 a[202:203], v250 offset:4352\n"
"v_add_f32 v240, v240, v222\n"
"v_mfma_f32_16x16x32_bf16 a[24:27], a[152:155], v[224:227], a[24:27]\n"
"v_exp_f32 v223, v223\n"
"v_mfma_f32_16x16x32_bf16 a[28:31], a[156:159], v[224:227], a[28:31]\n"
"v_add_f32 v240, v240, v223\n"
"v_cvt_pk_bf16_f32 v231, v222, v223\n"
"v_mfma_f32_16x16x32_bf16 v[216:219], a[160:163], v[96:99], v[232:235]\n"
"v_exp_f32 v208, v208\n"
"v_mfma_f32_16x16x32_bf16 v[216:219], a[164:167], v[100:103], v[216:219]\n"
"ds_read_b64_tr_b16 a[204:205], v251 offset:256\n"
"v_add_f32 v241, v241, v208\n"
"v_mfma_f32_16x16x32_bf16 v[216:219], a[168:171], v[104:107], v[216:219]\n"
"v_exp_f32 v209, v209\n"
"v_mfma_f32_16x16x32_bf16 v[216:219], a[172:175], v[108:111], v[216:219]\n"
"v_add_f32 v241, v241, v209\n"
"v_cvt_pk_bf16_f32 v224, v208, v209\n"
"v_mfma_f32_16x16x32_bf16 v[220:223], a[188:191], v[108:111], v[232:235]\n"
"v_exp_f32 v210, v210\n"
"v_mfma_f32_16x16x32_bf16 v[220:223], a[184:187], v[104:107], v[220:223]\n"
"ds_read_b64_tr_b16 a[206:207], v251 offset:4352\n"
"v_add_f32 v241, v241, v210\n"
"v_mfma_f32_16x16x32_bf16 v[220:223], a[180:183], v[100:103], v[220:223]\n"
"v_exp_f32 v211, v211\n"
"v_mfma_f32_16x16x32_bf16 v[220:223], a[176:179], v[96:99], v[220:223]\n"
"v_add_f32 v241, v241, v211\n"
"v_cvt_pk_bf16_f32 v225, v210, v211\n"
"v_mfma_f32_16x16x32_bf16 a[32:35], a[128:131], v[228:231], a[32:35]\n"
"s_add_u32 m0, s66, 10240\n"
"v_exp_f32 v212, v212\n"
"v_mfma_f32_16x16x32_bf16 a[36:39], a[132:135], v[228:231], a[36:39]\n"
"ds_read_b64_tr_b16 a[208:209], v250 offset:512\n"
"v_add_f32 v241, v241, v212\n"
"v_mfma_f32_16x16x32_bf16 a[40:43], a[136:139], v[228:231], a[40:43]\n"
"s_add_u32 s62, s72, 18432\n"
"v_exp_f32 v213, v213\n"
"v_mfma_f32_16x16x32_bf16 a[44:47], a[140:143], v[228:231], a[44:47]\n"
"v_add_f32 v241, v241, v213\n"
"v_cvt_pk_bf16_f32 v226, v212, v213\n"
"v_mfma_f32_16x16x32_bf16 a[48:51], a[144:147], v[228:231], a[48:51]\n"
"s_add_u32 s63, s72, 26624\n"
"v_exp_f32 v214, v214\n"
"v_mfma_f32_16x16x32_bf16 a[52:55], a[148:151], v[228:231], a[52:55]\n"
"buffer_load_dwordx4 v252, s[76:79], s62 offen lds\n"
"buffer_load_dwordx4 v252, s[76:79], s63 offen offset:1024 lds\n"
"v_mfma_f32_16x16x32_bf16 a[56:59], a[152:155], v[228:231], a[56:59]\n"
"s_waitcnt lgkmcnt(9)\n"
"v_exp_f32 v215, v215\n"
"v_mfma_f32_16x16x32_bf16 a[60:63], a[156:159], v[228:231], a[60:63]\n"
"v_add_f32 v241, v241, v214\n"
"v_add_f32 v241, v241, v215\n"
"v_cvt_pk_bf16_f32 v227, v214, v215\n"
"v_mfma_f32_16x16x32_bf16 v[208:211], a[224:227], v[0:3], v[232:235]\n"
"v_exp_f32 v216, v216\n"
"v_mfma_f32_16x16x32_bf16 v[208:211], a[228:231], v[4:7], v[208:211]\n"
"ds_read_b64_tr_b16 a[210:211], v250 offset:4608\n"
"v_add_f32 v242, v242, v216\n"
"v_mfma_f32_16x16x32_bf16 v[208:211], a[232:235], v[8:11], v[208:211]\n"
"v_exp_f32 v217, v217\n"
"v_mfma_f32_16x16x32_bf16 v[208:211], a[236:239], v[12:15], v[208:211]\n"
"v_add_f32 v242, v242, v217\n"
"v_cvt_pk_bf16_f32 v228, v216, v217\n"
"v_mfma_f32_16x16x32_bf16 v[212:215], a[252:255], v[12:15], v[232:235]\n"
"v_exp_f32 v218, v218\n"
"v_mfma_f32_16x16x32_bf16 v[212:215], a[248:251], v[8:11], v[212:215]\n"
"ds_read_b64_tr_b16 a[212:213], v251 offset:512\n"
"v_add_f32 v242, v242, v218\n"
"v_mfma_f32_16x16x32_bf16 v[212:215], a[244:247], v[4:7], v[212:215]\n"
"v_exp_f32 v219, v219\n"
"v_mfma_f32_16x16x32_bf16 v[212:215], a[240:243], v[0:3], v[212:215]\n"
"v_add_f32 v242, v242, v219\n"
"v_cvt_pk_bf16_f32 v229, v218, v219\n"
"v_mfma_f32_16x16x32_bf16 a[64:67], a[128:131], v[224:227], a[64:67]\n"
"v_exp_f32 v220, v220\n"
"v_mfma_f32_16x16x32_bf16 a[68:71], a[132:135], v[224:227], a[68:71]\n"
"ds_read_b64_tr_b16 a[214:215], v251 offset:4608\n"
"v_add_f32 v242, v242, v220\n"
"v_mfma_f32_16x16x32_bf16 a[72:75], a[136:139], v[224:227], a[72:75]\n"
"v_exp_f32 v221, v221\n"
"v_mfma_f32_16x16x32_bf16 a[76:79], a[140:143], v[224:227], a[76:79]\n"
"v_add_f32 v242, v242, v221\n"
"v_cvt_pk_bf16_f32 v230, v220, v221\n"
"v_mfma_f32_16x16x32_bf16 a[80:83], a[144:147], v[224:227], a[80:83]\n"
"v_exp_f32 v222, v222\n"
"v_mfma_f32_16x16x32_bf16 a[84:87], a[148:151], v[224:227], a[84:87]\n"
"ds_read_b64_tr_b16 a[216:217], v250 offset:768\n"
"v_add_f32 v242, v242, v222\n"
"v_mfma_f32_16x16x32_bf16 a[88:91], a[152:155], v[224:227], a[88:91]\n"
"s_lshl_b32 s62, s37, 2\n"
"s_add_u32 s62, s62, s36\n"
"s_mul_i32 s62, s62, 9216\n"
"v_exp_f32 v223, v223\n"
"v_mfma_f32_16x16x32_bf16 a[92:95], a[156:159], v[224:227], a[92:95]\n"
"buffer_load_dword v244, v244, s[76:79], s62 offen\n"
"v_add_f32 v242, v242, v223\n"
"v_cvt_pk_bf16_f32 v231, v222, v223\n"
"v_mfma_f32_16x16x32_bf16 v[216:219], a[224:227], v[16:19], v[232:235]\n"
"v_exp_f32 v208, v208\n"
"v_mfma_f32_16x16x32_bf16 v[216:219], a[228:231], v[20:23], v[216:219]\n"
"ds_read_b64_tr_b16 a[218:219], v250 offset:4864\n"
"v_add_f32 v236, v236, v208\n"
"v_mfma_f32_16x16x32_bf16 v[216:219], a[232:235], v[24:27], v[216:219]\n"
"v_exp_f32 v209, v209\n"
"v_mfma_f32_16x16x32_bf16 v[216:219], a[236:239], v[28:31], v[216:219]\n"
"v_add_f32 v236, v236, v209\n"
"v_cvt_pk_bf16_f32 v224, v208, v209\n"
"v_mfma_f32_16x16x32_bf16 v[220:223], a[252:255], v[28:31], v[232:235]\n"
"v_exp_f32 v210, v210\n"
"v_mfma_f32_16x16x32_bf16 v[220:223], a[248:251], v[24:27], v[220:223]\n"
"ds_read_b64_tr_b16 a[220:221], v251 offset:768\n"
"v_add_f32 v236, v236, v210\n"
"v_mfma_f32_16x16x32_bf16 v[220:223], a[244:247], v[20:23], v[220:223]\n"
"v_exp_f32 v211, v211\n"
"v_mfma_f32_16x16x32_bf16 v[220:223], a[240:243], v[16:19], v[220:223]\n"
"v_add_f32 v236, v236, v211\n"
"v_cvt_pk_bf16_f32 v225, v210, v211\n"
"v_mfma_f32_16x16x32_bf16 a[96:99], a[128:131], v[228:231], a[96:99]\n"
"s_add_u32 m0, s66, 12288\n"
"v_exp_f32 v212, v212\n"
"v_mfma_f32_16x16x32_bf16 a[100:103], a[132:135], v[228:231], a[100:103]\n"
"ds_read_b64_tr_b16 a[222:223], v251 offset:4864\n"
"v_add_f32 v236, v236, v212\n"
"v_mfma_f32_16x16x32_bf16 a[104:107], a[136:139], v[228:231], a[104:107]\n"
"s_add_u32 s62, s72, 36864\n"
"v_exp_f32 v213, v213\n"
"v_mfma_f32_16x16x32_bf16 a[108:111], a[140:143], v[228:231], a[108:111]\n"
"v_add_u32 v250, s90, v250\n"
"v_add_f32 v236, v236, v213\n"
"v_cvt_pk_bf16_f32 v226, v212, v213\n"
"v_mfma_f32_16x16x32_bf16 a[112:115], a[144:147], v[228:231], a[112:115]\n"
"s_add_u32 s63, s72, 45056\n"
"v_exp_f32 v214, v214\n"
"v_mfma_f32_16x16x32_bf16 a[116:119], a[148:151], v[228:231], a[116:119]\n"
"buffer_load_dwordx4 v252, s[76:79], s62 offen lds\n"
"buffer_load_dwordx4 v252, s[76:79], s63 offen offset:1024 lds\n"
"v_mfma_f32_16x16x32_bf16 a[120:123], a[152:155], v[228:231], a[120:123]\n"
"v_exp_f32 v215, v215\n"
"v_mfma_f32_16x16x32_bf16 a[124:127], a[156:159], v[228:231], a[124:127]\n"
"v_add_u32 v251, s90, v251\n"
"v_add_f32 v236, v236, v214\n"
"v_add_f32 v236, v236, v215\n"
"v_cvt_pk_bf16_f32 v227, v214, v215\n"
"; attn block 99\n"
"v_mfma_f32_16x16x32_bf16 v[208:211], a[224:227], v[32:35], v[232:235]\n"
"s_waitcnt vmcnt(7) lgkmcnt(0)\n"
"s_barrier\n"
"v_exp_f32 v216, v216\n"
"v_mfma_f32_16x16x32_bf16 v[208:211], a[228:231], v[36:39], v[208:211]\n"
"ds_read_b128 a[160:163], v249 offset:0\n"
"v_add_f32 v237, v237, v216\n"
"v_mfma_f32_16x16x32_bf16 v[208:211], a[232:235], v[40:43], v[208:211]\n"
"v_exp_f32 v217, v217\n"
"v_mfma_f32_16x16x32_bf16 v[208:211], a[236:239], v[44:47], v[208:211]\n"
"v_add_f32 v237, v237, v217\n"
"v_cvt_pk_bf16_f32 v228, v216, v217\n"
"v_mfma_f32_16x16x32_bf16 v[212:215], a[252:255], v[44:47], v[232:235]\n"
"v_exp_f32 v218, v218\n"
"v_mfma_f32_16x16x32_bf16 v[212:215], a[248:251], v[40:43], v[212:215]\n"
"ds_read_b128 a[164:167], v249 offset:256\n"
"v_add_f32 v237, v237, v218\n"
"v_mfma_f32_16x16x32_bf16 v[212:215], a[244:247], v[36:39], v[212:215]\n"
"v_exp_f32 v219, v219\n"
"v_mfma_f32_16x16x32_bf16 v[212:215], a[240:243], v[32:35], v[212:215]\n"
"v_add_f32 v237, v237, v219\n"
"v_cvt_pk_bf16_f32 v229, v218, v219\n"
"v_mfma_f32_16x16x32_bf16 v[112:115], a[192:195], v[224:227], v[112:115]\n"
"s_add_u32 m0, s66, 14336\n"
"v_exp_f32 v220, v220\n"
"v_mfma_f32_16x16x32_bf16 v[116:119], a[196:199], v[224:227], v[116:119]\n"
"ds_read_b128 a[168:171], v249 offset:512\n"
"v_add_f32 v237, v237, v220\n"
"v_mfma_f32_16x16x32_bf16 v[120:123], a[200:203], v[224:227], v[120:123]\n"
"s_add_u32 s62, s72, 55296\n"
"v_exp_f32 v221, v221\n"
"v_mfma_f32_16x16x32_bf16 v[124:127], a[204:207], v[224:227], v[124:127]\n"
"v_add_f32 v237, v237, v221\n"
"v_cvt_pk_bf16_f32 v230, v220, v221\n"
"v_mfma_f32_16x16x32_bf16 v[128:131], a[208:211], v[224:227], v[128:131]\n"
"s_add_u32 s63, s72, 63488\n"
"v_exp_f32 v222, v222\n"
"v_mfma_f32_16x16x32_bf16 v[132:135], a[212:215], v[224:227], v[132:135]\n"
"buffer_load_dwordx4 v252, s[76:79], s62 offen lds\n"
"buffer_load_dwordx4 v252, s[76:79], s63 offen offset:1024 lds\n"
"v_mfma_f32_16x16x32_bf16 v[136:139], a[216:219], v[224:227], v[136:139]\n"
"v_exp_f32 v223, v223\n"
"v_mfma_f32_16x16x32_bf16 v[140:143], a[220:223], v[224:227], v[140:143]\n"
"v_add_f32 v237, v237, v222\n"
"v_add_f32 v237, v237, v223\n"
"v_cvt_pk_bf16_f32 v231, v222, v223\n"
"v_mfma_f32_16x16x32_bf16 v[216:219], a[224:227], v[48:51], v[232:235]\n"
"v_exp_f32 v208, v208\n"
"v_mfma_f32_16x16x32_bf16 v[216:219], a[228:231], v[52:55], v[216:219]\n"
"ds_read_b128 a[172:175], v249 offset:768\n"
"v_add_f32 v238, v238, v208\n"
"v_mfma_f32_16x16x32_bf16 v[216:219], a[232:235], v[56:59], v[216:219]\n"
"v_exp_f32 v209, v209\n"
"v_mfma_f32_16x16x32_bf16 v[216:219], a[236:239], v[60:63], v[216:219]\n"
"v_add_f32 v238, v238, v209\n"
"v_cvt_pk_bf16_f32 v224, v208, v209\n"
"v_mfma_f32_16x16x32_bf16 v[220:223], a[252:255], v[60:63], v[232:235]\n"
"v_exp_f32 v210, v210\n"
"v_mfma_f32_16x16x32_bf16 v[220:223], a[248:251], v[56:59], v[220:223]\n"
"ds_read_b128 a[176:179], v249 offset:4096\n"
"v_add_f32 v238, v238, v210\n"
"v_mfma_f32_16x16x32_bf16 v[220:223], a[244:247], v[52:55], v[220:223]\n"
"v_exp_f32 v211, v211\n"
"v_mfma_f32_16x16x32_bf16 v[220:223], a[240:243], v[48:51], v[220:223]\n"
"v_add_f32 v238, v238, v211\n"
"v_cvt_pk_bf16_f32 v225, v210, v211\n"
"v_mfma_f32_16x16x32_bf16 v[144:147], a[192:195], v[228:231], v[144:147]\n"
"v_exp_f32 v212, v212\n"
"v_mfma_f32_16x16x32_bf16 v[148:151], a[196:199], v[228:231], v[148:151]\n"
"ds_read_b128 a[180:183], v249 offset:4352\n"
"v_add_f32 v238, v238, v212\n"
"v_mfma_f32_16x16x32_bf16 v[152:155], a[200:203], v[228:231], v[152:155]\n"
"v_exp_f32 v213, v213\n"
"v_mfma_f32_16x16x32_bf16 v[156:159], a[204:207], v[228:231], v[156:159]\n"
"v_add_f32 v238, v238, v213\n"
"v_cvt_pk_bf16_f32 v226, v212, v213\n"
"v_mfma_f32_16x16x32_bf16 v[160:163], a[208:211], v[228:231], v[160:163]\n"
"v_exp_f32 v214, v214\n"
"v_mfma_f32_16x16x32_bf16 v[164:167], a[212:215], v[228:231], v[164:167]\n"
"ds_read_b128 a[184:187], v249 offset:4608\n"
"v_add_f32 v238, v238, v214\n"
"v_mfma_f32_16x16x32_bf16 v[168:171], a[216:219], v[228:231], v[168:171]\n"
"v_exp_f32 v215, v215\n"
"v_mfma_f32_16x16x32_bf16 v[172:175], a[220:223], v[228:231], v[172:175]\n"
"v_add_f32 v238, v238, v215\n"
"v_cvt_pk_bf16_f32 v227, v214, v215\n"
"v_mfma_f32_16x16x32_bf16 v[208:211], a[224:227], v[64:67], v[232:235]\n"
"v_exp_f32 v216, v216\n"
"v_mfma_f32_16x16x32_bf16 v[208:211], a[228:231], v[68:71], v[208:211]\n"
"ds_read_b128 a[188:191], v249 offset:4864\n"
"v_add_f32 v239, v239, v216\n"
"v_mfma_f32_16x16x32_bf16 v[208:211], a[232:235], v[72:75], v[208:211]\n"
"v_exp_f32 v217, v217\n"
"v_mfma_f32_16x16x32_bf16 v[208:211], a[236:239], v[76:79], v[208:211]\n"
"v_add_f32 v239, v239, v217\n"
"v_cvt_pk_bf16_f32 v228, v216, v217\n"
"v_mfma_f32_16x16x32_bf16 v[212:215], a[252:255], v[76:79], v[232:235]\n"
"v_exp_f32 v218, v218\n"
"v_mfma_f32_16x16x32_bf16 v[212:215], a[248:251], v[72:75], v[212:215]\n"
"ds_read_b64_tr_b16 a[128:129], v250 offset:0\n"
"v_add_f32 v239, v239, v218\n"
"v_mfma_f32_16x16x32_bf16 v[212:215], a[244:247], v[68:71], v[212:215]\n"
"v_exp_f32 v219, v219\n"
"v_mfma_f32_16x16x32_bf16 v[212:215], a[240:243], v[64:67], v[212:215]\n"
"v_add_f32 v239, v239, v219\n"
"v_cvt_pk_bf16_f32 v229, v218, v219\n"
"v_mfma_f32_16x16x32_bf16 v[176:179], a[192:195], v[224:227], v[176:179]\n"
"s_add_u32 m0, s66, 28672\n"
"v_exp_f32 v220, v220\n"
"v_mfma_f32_16x16x32_bf16 v[180:183], a[196:199], v[224:227], v[180:183]\n"
"ds_read_b64_tr_b16 a[130:131], v250 offset:4096\n"
"v_add_f32 v239, v239, v220\n"
"v_mfma_f32_16x16x32_bf16 v[184:187], a[200:203], v[224:227], v[184:187]\n"
"s_add_u32 s62, s44, 0\n"
"v_exp_f32 v221, v221\n"
"v_mfma_f32_16x16x32_bf16 v[188:191], a[204:207], v[224:227], v[188:191]\n"
"v_add_f32 v239, v239, v221\n"
"v_cvt_pk_bf16_f32 v230, v220, v221\n"
"v_mfma_f32_16x16x32_bf16 v[192:195], a[208:211], v[224:227], v[192:195]\n"
"s_add_u32 s63, s44, 8192\n"
"v_exp_f32 v222, v222\n"
"v_mfma_f32_16x16x32_bf16 v[196:199], a[212:215], v[224:227], v[196:199]\n"
"buffer_load_dwordx4 v252, s[76:79], s62 offen lds\n"
"buffer_load_dwordx4 v252, s[76:79], s63 offen offset:1024 lds\n"
"v_mfma_f32_16x16x32_bf16 v[200:203], a[216:219], v[224:227], v[200:203]\n"
"v_exp_f32 v223, v223\n"
"v_mfma_f32_16x16x32_bf16 v[204:207], a[220:223], v[224:227], v[204:207]\n"
"v_add_f32 v239, v239, v222\n"
"v_add_f32 v239, v239, v223\n"
"v_cvt_pk_bf16_f32 v231, v222, v223\n"
"v_mfma_f32_16x16x32_bf16 v[216:219], a[224:227], v[80:83], v[232:235]\n"
"v_exp_f32 v208, v208\n"
"v_mfma_f32_16x16x32_bf16 v[216:219], a[228:231], v[84:87], v[216:219]\n"
"ds_read_b64_tr_b16 a[132:133], v251 offset:0\n"
"v_add_f32 v240, v240, v208\n"
"v_mfma_f32_16x16x32_bf16 v[216:219], a[232:235], v[88:91], v[216:219]\n"
"v_exp_f32 v209, v209\n"
"v_mfma_f32_16x16x32_bf16 v[216:219], a[236:239], v[92:95], v[216:219]\n"
"v_add_f32 v240, v240, v209\n"
"v_cvt_pk_bf16_f32 v224, v208, v209\n"
"v_mfma_f32_16x16x32_bf16 v[220:223], a[252:255], v[92:95], v[232:235]\n"
"v_exp_f32 v210, v210\n"
"v_mfma_f32_16x16x32_bf16 v[220:223], a[248:251], v[88:91], v[220:223]\n"
"ds_read_b64_tr_b16 a[134:135], v251 offset:4096\n"
"v_add_f32 v240, v240, v210\n"
"v_mfma_f32_16x16x32_bf16 v[220:223], a[244:247], v[84:87], v[220:223]\n"
"v_exp_f32 v211, v211\n"
"v_mfma_f32_16x16x32_bf16 v[220:223], a[240:243], v[80:83], v[220:223]\n"
"v_add_f32 v240, v240, v211\n"
"v_cvt_pk_bf16_f32 v225, v210, v211\n"
"v_mfma_f32_16x16x32_bf16 a[0:3], a[192:195], v[228:231], a[0:3]\n"
"v_exp_f32 v212, v212\n"
"v_mfma_f32_16x16x32_bf16 a[4:7], a[196:199], v[228:231], a[4:7]\n"
"ds_read_b64_tr_b16 a[136:137], v250 offset:256\n"
"v_add_f32 v240, v240, v212\n"
"v_mfma_f32_16x16x32_bf16 a[8:11], a[200:203], v[228:231], a[8:11]\n"
"v_exp_f32 v213, v213\n"
"v_mfma_f32_16x16x32_bf16 a[12:15], a[204:207], v[228:231], a[12:15]\n"
"v_add_f32 v240, v240, v213\n"
"v_cvt_pk_bf16_f32 v226, v212, v213\n"
"v_mfma_f32_16x16x32_bf16 a[16:19], a[208:211], v[228:231], a[16:19]\n"
"v_exp_f32 v214, v214\n"
"v_mfma_f32_16x16x32_bf16 a[20:23], a[212:215], v[228:231], a[20:23]\n"
"ds_read_b64_tr_b16 a[138:139], v250 offset:4352\n"
"v_add_f32 v240, v240, v214\n"
"v_mfma_f32_16x16x32_bf16 a[24:27], a[216:219], v[228:231], a[24:27]\n"
"v_exp_f32 v215, v215\n"
"v_mfma_f32_16x16x32_bf16 a[28:31], a[220:223], v[228:231], a[28:31]\n"
"v_add_f32 v240, v240, v215\n"
"v_cvt_pk_bf16_f32 v227, v214, v215\n"
"v_mfma_f32_16x16x32_bf16 v[208:211], a[224:227], v[96:99], v[232:235]\n"
"v_exp_f32 v216, v216\n"
"v_mfma_f32_16x16x32_bf16 v[208:211], a[228:231], v[100:103], v[208:211]\n"
"ds_read_b64_tr_b16 a[140:141], v251 offset:256\n"
"v_add_f32 v241, v241, v216\n"
"v_mfma_f32_16x16x32_bf16 v[208:211], a[232:235], v[104:107], v[208:211]\n"
"v_exp_f32 v217, v217\n"
"v_mfma_f32_16x16x32_bf16 v[208:211], a[236:239], v[108:111], v[208:211]\n"
"v_xor_b32 v245, s56, v253\n"
"v_add_f32 v241, v241, v217\n"
"v_cvt_pk_bf16_f32 v228, v216, v217\n"
"v_mfma_f32_16x16x32_bf16 v[212:215], a[252:255], v[108:111], v[232:235]\n"
"v_exp_f32 v218, v218\n"
"v_mfma_f32_16x16x32_bf16 v[212:215], a[248:251], v[104:107], v[212:215]\n"
"ds_read_b64_tr_b16 a[142:143], v251 offset:4352\n"
"v_add_f32 v241, v241, v218\n"
"v_mfma_f32_16x16x32_bf16 v[212:215], a[244:247], v[100:103], v[212:215]\n"
"v_exp_f32 v219, v219\n"
"v_mfma_f32_16x16x32_bf16 v[212:215], a[240:243], v[96:99], v[212:215]\n"
"v_add_u32 v245, s67, v245\n"
"v_add_f32 v241, v241, v219\n"
"v_cvt_pk_bf16_f32 v229, v218, v219\n"
"v_mfma_f32_16x16x32_bf16 a[32:35], a[192:195], v[224:227], a[32:35]\n"
"s_add_u32 m0, s66, 30720\n"
"v_exp_f32 v220, v220\n"
"v_mfma_f32_16x16x32_bf16 a[36:39], a[196:199], v[224:227], a[36:39]\n"
"ds_read_b64_tr_b16 a[144:145], v250 offset:512\n"
"v_add_f32 v241, v241, v220\n"
"v_mfma_f32_16x16x32_bf16 a[40:43], a[200:203], v[224:227], a[40:43]\n"
"s_add_u32 s62, s44, 18432\n"
"v_exp_f32 v221, v221\n"
"v_mfma_f32_16x16x32_bf16 a[44:47], a[204:207], v[224:227], a[44:47]\n"
"v_add_f32 v241, v241, v221\n"
"v_cvt_pk_bf16_f32 v230, v220, v221\n"
"v_mfma_f32_16x16x32_bf16 a[48:51], a[208:211], v[224:227], a[48:51]\n"
"s_add_u32 s63, s44, 26624\n"
"v_exp_f32 v222, v222\n"
"v_mfma_f32_16x16x32_bf16 a[52:55], a[212:215], v[224:227], a[52:55]\n"
"buffer_load_dwordx4 v252, s[76:79], s62 offen lds\n"
"buffer_load_dwordx4 v252, s[76:79], s63 offen offset:1024 lds\n"
"v_mfma_f32_16x16x32_bf16 a[56:59], a[216:219], v[224:227], a[56:59]\n"
"s_waitcnt lgkmcnt(9)\n"
"v_exp_f32 v223, v223\n"
"v_mfma_f32_16x16x32_bf16 a[60:63], a[220:223], v[224:227], a[60:63]\n"
"v_add_f32 v241, v241, v222\n"
"v_add_f32 v241, v241, v223\n"
"v_cvt_pk_bf16_f32 v231, v222, v223\n"
"v_mfma_f32_16x16x32_bf16 v[216:219], a[160:163], v[0:3], v[232:235]\n"
"v_exp_f32 v208, v208\n"
"v_mfma_f32_16x16x32_bf16 v[216:219], a[164:167], v[4:7], v[216:219]\n"
"ds_read_b64_tr_b16 a[146:147], v250 offset:4608\n"
"v_add_f32 v242, v242, v208\n"
"v_mfma_f32_16x16x32_bf16 v[216:219], a[168:171], v[8:11], v[216:219]\n"
"v_exp_f32 v209, v209\n"
"v_mfma_f32_16x16x32_bf16 v[216:219], a[172:175], v[12:15], v[216:219]\n"
"v_add_u32 v246, s49, v245\n"
"v_add_f32 v242, v242, v209\n"
"v_cvt_pk_bf16_f32 v224, v208, v209\n"
"v_mfma_f32_16x16x32_bf16 v[220:223], a[188:191], v[12:15], v[232:235]\n"
"v_exp_f32 v210, v210\n"
"v_mfma_f32_16x16x32_bf16 v[220:223], a[184:187], v[8:11], v[220:223]\n"
"ds_read_b64_tr_b16 a[148:149], v251 offset:512\n"
"v_add_f32 v242, v242, v210\n"
"v_mfma_f32_16x16x32_bf16 v[220:223], a[180:183], v[4:7], v[220:223]\n"
"v_exp_f32 v211, v211\n"
"v_mfma_f32_16x16x32_bf16 v[220:223], a[176:179], v[0:3], v[220:223]\n"
"v_add_f32 v242, v242, v211\n"
"v_cvt_pk_bf16_f32 v225, v210, v211\n"
"v_mfma_f32_16x16x32_bf16 a[64:67], a[192:195], v[228:231], a[64:67]\n"
"v_exp_f32 v212, v212\n"
"v_mfma_f32_16x16x32_bf16 a[68:71], a[196:199], v[228:231], a[68:71]\n"
"ds_read_b64_tr_b16 a[150:151], v251 offset:4608\n"
"v_add_f32 v242, v242, v212\n"
"v_mfma_f32_16x16x32_bf16 a[72:75], a[200:203], v[228:231], a[72:75]\n"
"v_exp_f32 v213, v213\n"
"v_mfma_f32_16x16x32_bf16 a[76:79], a[204:207], v[228:231], a[76:79]\n"
"v_add_f32 v242, v242, v213\n"
"v_cvt_pk_bf16_f32 v226, v212, v213\n"
"v_mfma_f32_16x16x32_bf16 a[80:83], a[208:211], v[228:231], a[80:83]\n"
"v_exp_f32 v214, v214\n"
"v_mfma_f32_16x16x32_bf16 a[84:87], a[212:215], v[228:231], a[84:87]\n"
"ds_read_b64_tr_b16 a[152:153], v250 offset:768\n"
"v_add_f32 v242, v242, v214\n"
"v_mfma_f32_16x16x32_bf16 a[88:91], a[216:219], v[228:231], a[88:91]\n"
"v_exp_f32 v215, v215\n"
"v_mfma_f32_16x16x32_bf16 a[92:95], a[220:223], v[228:231], a[92:95]\n"
"v_add_f32 v242, v242, v215\n"
"v_cvt_pk_bf16_f32 v227, v214, v215\n"
"v_mfma_f32_16x16x32_bf16 v[208:211], a[160:163], v[16:19], v[232:235]\n"
"v_exp_f32 v216, v216\n"
"v_mfma_f32_16x16x32_bf16 v[208:211], a[164:167], v[20:23], v[208:211]\n"
"ds_read_b64_tr_b16 a[154:155], v250 offset:4864\n"
"v_add_f32 v236, v236, v216\n"
"v_mfma_f32_16x16x32_bf16 v[208:211], a[168:171], v[24:27], v[208:211]\n"
"v_exp_f32 v217, v217\n"
"v_mfma_f32_16x16x32_bf16 v[208:211], a[172:175], v[28:31], v[208:211]\n"
"v_add_f32 v236, v236, v217\n"
"v_cvt_pk_bf16_f32 v228, v216, v217\n"
"v_mfma_f32_16x16x32_bf16 v[212:215], a[188:191], v[28:31], v[232:235]\n"
"v_exp_f32 v218, v218\n"
"v_mfma_f32_16x16x32_bf16 v[212:215], a[184:187], v[24:27], v[212:215]\n"
"ds_read_b64_tr_b16 a[156:157], v251 offset:768\n"
"v_add_f32 v236, v236, v218\n"
"v_mfma_f32_16x16x32_bf16 v[212:215], a[180:183], v[20:23], v[212:215]\n"
"v_exp_f32 v219, v219\n"
"v_mfma_f32_16x16x32_bf16 v[212:215], a[176:179], v[16:19], v[212:215]\n"
"v_add_f32 v236, v236, v219\n"
"v_cvt_pk_bf16_f32 v229, v218, v219\n"
"v_mfma_f32_16x16x32_bf16 a[96:99], a[192:195], v[224:227], a[96:99]\n"
"s_add_u32 m0, s66, 20480\n"
"v_exp_f32 v220, v220\n"
"v_mfma_f32_16x16x32_bf16 a[100:103], a[196:199], v[224:227], a[100:103]\n"
"ds_read_b64_tr_b16 a[158:159], v251 offset:4864\n"
"v_add_f32 v236, v236, v220\n"
"v_mfma_f32_16x16x32_bf16 a[104:107], a[200:203], v[224:227], a[104:107]\n"
"s_add_u32 s62, s72, 73728\n"
"v_exp_f32 v221, v221\n"
"v_mfma_f32_16x16x32_bf16 a[108:111], a[204:207], v[224:227], a[108:111]\n"
"v_add_u32 v250, s50, v245\n"
"v_add_u32 v251, s51, v245\n"
"v_add_f32 v236, v236, v221\n"
"v_cvt_pk_bf16_f32 v230, v220, v221\n"
"v_mfma_f32_16x16x32_bf16 a[112:115], a[208:211], v[224:227], a[112:115]\n"
"s_add_u32 s63, s72, 81920\n"
"v_exp_f32 v222, v222\n"
"v_mfma_f32_16x16x32_bf16 a[116:119], a[212:215], v[224:227], a[116:119]\n"
"buffer_load_dwordx4 v252, s[76:79], s62 offen lds\n"
"buffer_load_dwordx4 v252, s[76:79], s63 offen offset:1024 lds\n"
"v_mfma_f32_16x16x32_bf16 a[120:123], a[216:219], v[224:227], a[120:123]\n"
"v_exp_f32 v223, v223\n"
"v_mfma_f32_16x16x32_bf16 a[124:127], a[220:223], v[224:227], a[124:127]\n"
"v_add_u32 v245, s48, v245\n"
"v_add_f32 v236, v236, v222\n"
"v_add_f32 v236, v236, v223\n"
"v_cvt_pk_bf16_f32 v231, v222, v223\n"
"; attn block 106\n"
"v_mfma_f32_16x16x32_bf16 v[216:219], a[160:163], v[32:35], v[232:235]\n"
"s_waitcnt vmcnt(6) lgkmcnt(0)\n"
"s_barrier\n"
"v_exp_f32 v208, v208\n"
"v_mfma_f32_16x16x32_bf16 v[216:219], a[164:167], v[36:39], v[216:219]\n"
"ds_read_b64_tr_b16 a[192:193], v245 offset:8192\n"
"v_add_f32 v237, v237, v208\n"
"v_mfma_f32_16x16x32_bf16 v[216:219], a[168:171], v[40:43], v[216:219]\n"
"v_exp_f32 v209, v209\n"
"v_mfma_f32_16x16x32_bf16 v[216:219], a[172:175], v[44:47], v[216:219]\n"
"v_add_f32 v237, v237, v209\n"
"v_cvt_pk_bf16_f32 v224, v208, v209\n"
"v_mfma_f32_16x16x32_bf16 v[220:223], a[188:191], v[44:47], v[232:235]\n"
"v_exp_f32 v210, v210\n"
"v_mfma_f32_16x16x32_bf16 v[220:223], a[184:187], v[40:43], v[220:223]\n"
"ds_read_b64_tr_b16 a[194:195], v245 offset:10240\n"
"v_add_f32 v237, v237, v210\n"
"v_mfma_f32_16x16x32_bf16 v[220:223], a[180:183], v[36:39], v[220:223]\n"
"v_exp_f32 v211, v211\n"
"v_mfma_f32_16x16x32_bf16 v[220:223], a[176:179], v[32:35], v[220:223]\n"
"v_add_f32 v237, v237, v211\n"
"v_cvt_pk_bf16_f32 v225, v210, v211\n"
"v_mfma_f32_16x16x32_bf16 v[112:115], a[128:131], v[228:231], v[112:115]\n"
"s_add_u32 m0, s66, 22528\n"
"v_exp_f32 v212, v212\n"
"v_mfma_f32_16x16x32_bf16 v[116:119], a[132:135], v[228:231], v[116:119]\n"
"ds_read_b64_tr_b16 a[196:197], v246 offset:8192\n"
"v_add_f32 v237, v237, v212\n"
"v_mfma_f32_16x16x32_bf16 v[120:123], a[136:139], v[228:231], v[120:123]\n"
"s_add_u32 s62, s72, 92160\n"
"v_exp_f32 v213, v213\n"
"v_mfma_f32_16x16x32_bf16 v[124:127], a[140:143], v[228:231], v[124:127]\n"
"v_add_f32 v237, v237, v213\n"
"v_cvt_pk_bf16_f32 v226, v212, v213\n"
"v_mfma_f32_16x16x32_bf16 v[128:131], a[144:147], v[228:231], v[128:131]\n"
"s_add_u32 s63, s72, 100352\n"
"v_exp_f32 v214, v214\n"
"v_mfma_f32_16x16x32_bf16 v[132:135], a[148:151], v[228:231], v[132:135]\n"
"buffer_load_dwordx4 v252, s[76:79], s62 offen lds\n"
"buffer_load_dwordx4 v252, s[76:79], s63 offen offset:1024 lds\n"
"v_mfma_f32_16x16x32_bf16 v[136:139], a[152:155], v[228:231], v[136:139]\n"
"v_exp_f32 v215, v215\n"
"v_mfma_f32_16x16x32_bf16 v[140:143], a[156:159], v[228:231], v[140:143]\n"
"v_add_f32 v237, v237, v214\n"
"v_add_f32 v237, v237, v215\n"
"v_cvt_pk_bf16_f32 v227, v214, v215\n"
"v_mfma_f32_16x16x32_bf16 v[208:211], a[160:163], v[48:51], v[232:235]\n"
"v_exp_f32 v216, v216\n"
"v_mfma_f32_16x16x32_bf16 v[208:211], a[164:167], v[52:55], v[208:211]\n"
"ds_read_b64_tr_b16 a[198:199], v246 offset:10240\n"
"v_add_f32 v238, v238, v216\n"
"v_mfma_f32_16x16x32_bf16 v[208:211], a[168:171], v[56:59], v[208:211]\n"
"v_exp_f32 v217, v217\n"
"v_mfma_f32_16x16x32_bf16 v[208:211], a[172:175], v[60:63], v[208:211]\n"
"v_add_f32 v238, v238, v217\n"
"v_cvt_pk_bf16_f32 v228, v216, v217\n"
"v_mfma_f32_16x16x32_bf16 v[212:215], a[188:191], v[60:63], v[232:235]\n"
"v_exp_f32 v218, v218\n"
"v_mfma_f32_16x16x32_bf16 v[212:215], a[184:187], v[56:59], v[212:215]\n"
"ds_read_b64_tr_b16 a[200:201], v250 offset:8192\n"
"v_add_f32 v238, v238, v218\n"
"v_mfma_f32_16x16x32_bf16 v[212:215], a[180:183], v[52:55], v[212:215]\n"
"v_exp_f32 v219, v219\n"
"v_mfma_f32_16x16x32_bf16 v[212:215], a[176:179], v[48:51], v[212:215]\n"
"v_add_f32 v238, v238, v219\n"
"v_cvt_pk_bf16_f32 v229, v218, v219\n"
"v_mfma_f32_16x16x32_bf16 v[144:147], a[128:131], v[224:227], v[144:147]\n"
"s_add_u32 m0, s66, 24576\n"
"v_exp_f32 v220, v220\n"
"v_mfma_f32_16x16x32_bf16 v[148:151], a[132:135], v[224:227], v[148:151]\n"
"ds_read_b64_tr_b16 a[202:203], v250 offset:10240\n"
"v_add_f32 v238, v238, v220\n"
"v_mfma_f32_16x16x32_bf16 v[152:155], a[136:139], v[224:227], v[152:155]\n"
"s_add_u32 s62, s72, 110592\n"
"v_exp_f32 v221, v221\n"
"v_mfma_f32_16x16x32_bf16 v[156:159], a[140:143], v[224:227], v[156:159]\n"
"v_add_f32 v238, v238, v221\n"
"v_cvt_pk_bf16_f32 v230, v220, v221\n"
"v_mfma_f32_16x16x32_bf16 v[160:163], a[144:147], v[224:227], v[160:163]\n"
"s_add_u32 s63, s72, 118784\n"
"v_exp_f32 v222, v222\n"
"v_mfma_f32_16x16x32_bf16 v[164:167], a[148:151], v[224:227], v[164:167]\n"
"buffer_load_dwordx4 v252, s[76:79], s62 offen lds\n"
"buffer_load_dwordx4 v252, s[76:79], s63 offen offset:1024 lds\n"
"v_mfma_f32_16x16x32_bf16 v[168:171], a[152:155], v[224:227], v[168:171]\n"
"v_exp_f32 v223, v223\n"
"v_mfma_f32_16x16x32_bf16 v[172:175], a[156:159], v[224:227], v[172:175]\n"
"v_add_f32 v238, v238, v222\n"
"v_add_f32 v238, v238, v223\n"
"v_cvt_pk_bf16_f32 v231, v222, v223\n"
"v_mfma_f32_16x16x32_bf16 v[216:219], a[160:163], v[64:67], v[232:235]\n"
"v_exp_f32 v208, v208\n"
"v_mfma_f32_16x16x32_bf16 v[216:219], a[164:167], v[68:71], v[216:219]\n"
"ds_read_b64_tr_b16 a[204:205], v251 offset:8192\n"
"v_add_f32 v239, v239, v208\n"
"v_mfma_f32_16x16x32_bf16 v[216:219], a[168:171], v[72:75], v[216:219]\n"
"v_exp_f32 v209, v209\n"
"v_mfma_f32_16x16x32_bf16 v[216:219], a[172:175], v[76:79], v[216:219]\n"
"v_add_f32 v239, v239, v209\n"
"v_cvt_pk_bf16_f32 v224, v208, v209\n"
"v_mfma_f32_16x16x32_bf16 v[220:223], a[188:191], v[76:79], v[232:235]\n"
"v_exp_f32 v210, v210\n"
"v_mfma_f32_16x16x32_bf16 v[220:223], a[184:187], v[72:75], v[220:223]\n"
"ds_read_b64_tr_b16 a[206:207], v251 offset:10240\n"
"v_add_f32 v239, v239, v210\n"
"v_mfma_f32_16x16x32_bf16 v[220:223], a[180:183], v[68:71], v[220:223]\n"
"v_exp_f32 v211, v211\n"
"v_mfma_f32_16x16x32_bf16 v[220:223], a[176:179], v[64:67], v[220:223]\n"
"v_add_f32 v239, v239, v211\n"
"v_cvt_pk_bf16_f32 v225, v210, v211\n"
"v_mfma_f32_16x16x32_bf16 v[176:179], a[128:131], v[228:231], v[176:179]\n"
"s_add_u32 m0, s66, 26624\n"
"v_exp_f32 v212, v212\n"
"v_mfma_f32_16x16x32_bf16 v[180:183], a[132:135], v[228:231], v[180:183]\n"
"ds_read_b64_tr_b16 a[208:209], v245 offset:12288\n"
"v_add_f32 v239, v239, v212\n"
"v_mfma_f32_16x16x32_bf16 v[184:187], a[136:139], v[228:231], v[184:187]\n"
"s_add_u32 s62, s72, 129024\n"
"v_exp_f32 v213, v213\n"
"v_mfma_f32_16x16x32_bf16 v[188:191], a[140:143], v[228:231], v[188:191]\n"
"v_add_f32 v239, v239, v213\n"
"v_cvt_pk_bf16_f32 v226, v212, v213\n"
"v_mfma_f32_16x16x32_bf16 v[192:195], a[144:147], v[228:231], v[192:195]\n"
"s_add_u32 s63, s72, 137216\n"
"v_exp_f32 v214, v214\n"
"v_mfma_f32_16x16x32_bf16 v[196:199], a[148:151], v[228:231], v[196:199]\n"
"buffer_load_dwordx4 v252, s[76:79], s62 offen lds\n"
"buffer_load_dwordx4 v252, s[76:79], s63 offen offset:1024 lds\n"
"Lh7:\n"
"s_cbranch_execz Lw1\n"
"v_mfma_f32_16x16x32_bf16 v[200:203], a[152:155], v[228:231], v[200:203]\n"
"v_exp_f32 v215, v215\n"
"v_mfma_f32_16x16x32_bf16 v[204:207], a[156:159], v[228:231], v[204:207]\n"
"v_add_f32 v239, v239, v214\n"
"v_add_f32 v239, v239, v215\n"
"v_cvt_pk_bf16_f32 v227, v214, v215\n"
"v_mfma_f32_16x16x32_bf16 v[208:211], a[160:163], v[80:83], v[232:235]\n"
"v_exp_f32 v216, v216\n"
"v_mfma_f32_16x16x32_bf16 v[208:211], a[164:167], v[84:87], v[208:211]\n"
"ds_read_b64_tr_b16 a[210:211], v245 offset:14336\n"
"v_add_f32 v240, v240, v216\n"
"v_mfma_f32_16x16x32_bf16 v[208:211], a[168:171], v[88:91], v[208:211]\n"
"v_exp_f32 v217, v217\n"
"v_mfma_f32_16x16x32_bf16 v[208:211], a[172:175], v[92:95], v[208:211]\n"
"v_add_f32 v240, v240, v217\n"
"v_cvt_pk_bf16_f32 v228, v216, v217\n"
"v_mfma_f32_16x16x32_bf16 v[212:215], a[188:191], v[92:95], v[232:235]\n"
"v_exp_f32 v218, v218\n"
"v_mfma_f32_16x16x32_bf16 v[212:215], a[184:187], v[88:91], v[212:215]\n"
"ds_read_b64_tr_b16 a[212:213], v246 offset:12288\n"
"v_add_f32 v240, v240, v218\n"
"v_mfma_f32_16x16x32_bf16 v[212:215], a[180:183], v[84:87], v[212:215]\n"
"v_exp_f32 v219, v219\n"
"v_mfma_f32_16x16x32_bf16 v[212:215], a[176:179], v[80:83], v[212:215]\n"
"v_add_f32 v240, v240, v219\n"
"v_cvt_pk_bf16_f32 v229, v218, v219\n"
"v_mfma_f32_16x16x32_bf16 a[0:3], a[128:131], v[224:227], a[0:3]\n"
"s_add_u32 m0, s66, 32768\n"
"v_exp_f32 v220, v220\n"
"v_mfma_f32_16x16x32_bf16 a[4:7], a[132:135], v[224:227], a[4:7]\n"
"ds_read_b64_tr_b16 a[214:215], v246 offset:14336\n"
"v_add_f32 v240, v240, v220\n"
"v_mfma_f32_16x16x32_bf16 a[8:11], a[136:139], v[224:227], a[8:11]\n"
"s_add_u32 s62, s45, 0\n"
"v_exp_f32 v221, v221\n"
"v_mfma_f32_16x16x32_bf16 a[12:15], a[140:143], v[224:227], a[12:15]\n"
"v_add_f32 v240, v240, v221\n"
"v_cvt_pk_bf16_f32 v230, v220, v221\n"
"v_mfma_f32_16x16x32_bf16 a[16:19], a[144:147], v[224:227], a[16:19]\n"
"s_add_u32 s63, s45, 8192\n"
"v_exp_f32 v222, v222\n"
"v_mfma_f32_16x16x32_bf16 a[20:23], a[148:151], v[224:227], a[20:23]\n"
"buffer_load_dwordx4 v252, s[76:79], s62 offen lds\n"
"buffer_load_dwordx4 v252, s[76:79], s63 offen offset:1024 lds\n"
"v_mfma_f32_16x16x32_bf16 a[24:27], a[152:155], v[224:227], a[24:27]\n"
"v_exp_f32 v223, v223\n"
"v_mfma_f32_16x16x32_bf16 a[28:31], a[156:159], v[224:227], a[28:31]\n"
"v_add_f32 v240, v240, v222\n"
"v_add_f32 v240, v240, v223\n"
"v_cvt_pk_bf16_f32 v231, v222, v223\n"
"v_mfma_f32_16x16x32_bf16 v[216:219], a[160:163], v[96:99], v[232:235]\n"
"v_exp_f32 v208, v208\n"
"v_mfma_f32_16x16x32_bf16 v[216:219], a[164:167], v[100:103], v[216:219]\n"
"ds_read_b64_tr_b16 a[216:217], v250 offset:12288\n"
"v_add_f32 v241, v241, v208\n"
"v_mfma_f32_16x16x32_bf16 v[216:219], a[168:171], v[104:107], v[216:219]\n"
"v_exp_f32 v209, v209\n"
"v_mfma_f32_16x16x32_bf16 v[216:219], a[172:175], v[108:111], v[216:219]\n"
"v_add_f32 v241, v241, v209\n"
"v_cvt_pk_bf16_f32 v224, v208, v209\n"
"v_mfma_f32_16x16x32_bf16 v[220:223], a[188:191], v[108:111], v[232:235]\n"
"v_exp_f32 v210, v210\n"
"v_mfma_f32_16x16x32_bf16 v[220:223], a[184:187], v[104:107], v[220:223]\n"
"ds_read_b64_tr_b16 a[218:219], v250 offset:14336\n"
"v_add_f32 v241, v241, v210\n"
"v_mfma_f32_16x16x32_bf16 v[220:223], a[180:183], v[100:103], v[220:223]\n"
"v_exp_f32 v211, v211\n"
"v_mfma_f32_16x16x32_bf16 v[220:223], a[176:179], v[96:99], v[220:223]\n"
"v_add_f32 v241, v241, v211\n"
"v_cvt_pk_bf16_f32 v225, v210, v211\n"
"v_mfma_f32_16x16x32_bf16 a[32:35], a[128:131], v[228:231], a[32:35]\n"
"s_add_u32 m0, s66, 34816\n"
"v_exp_f32 v212, v212\n"
"v_mfma_f32_16x16x32_bf16 a[36:39], a[132:135], v[228:231], a[36:39]\n"
"ds_read_b64_tr_b16 a[220:221], v251 offset:12288\n"
"v_add_f32 v241, v241, v212\n"
"v_mfma_f32_16x16x32_bf16 a[40:43], a[136:139], v[228:231], a[40:43]\n"
"s_add_u32 s62, s45, 18432\n"
"v_exp_f32 v213, v213\n"
"v_mfma_f32_16x16x32_bf16 a[44:47], a[140:143], v[228:231], a[44:47]\n"
"v_add_f32 v241, v241, v213\n"
"v_cvt_pk_bf16_f32 v226, v212, v213\n"
"v_mfma_f32_16x16x32_bf16 a[48:51], a[144:147], v[228:231], a[48:51]\n"
"s_add_u32 s63, s45, 26624\n"
"v_exp_f32 v214, v214\n"
"v_mfma_f32_16x16x32_bf16 a[52:55], a[148:151], v[228:231], a[52:55]\n"
"buffer_load_dwordx4 v252, s[76:79], s62 offen lds\n"
"buffer_load_dwordx4 v252, s[76:79], s63 offen offset:1024 lds\n"
"v_mfma_f32_16x16x32_bf16 a[56:59], a[152:155], v[228:231], a[56:59]\n"
"v_exp_f32 v215, v215\n"
"v_mfma_f32_16x16x32_bf16 a[60:63], a[156:159], v[228:231], a[60:63]\n"
"v_add_f32 v241, v241, v214\n"
"v_add_f32 v241, v241, v215\n"
"v_cvt_pk_bf16_f32 v227, v214, v215\n"
"s_nop 1 ; auto\n"
"v_mfma_f32_16x16x32_bf16 a[64:67], a[128:131], v[224:227], a[64:67]\n"
"v_exp_f32 v216, v216\n"
"ds_read_b64_tr_b16 a[222:223], v251 offset:14336\n"
"v_add_f32 v242, v242, v216\n"
"v_mfma_f32_16x16x32_bf16 a[68:71], a[132:135], v[224:227], a[68:71]\n"
"v_exp_f32 v217, v217\n"
"s_nop 0 ; auto\n"
"v_add_f32 v242, v242, v217\n"
"v_cvt_pk_bf16_f32 v228, v216, v217\n"
"v_mfma_f32_16x16x32_bf16 a[72:75], a[136:139], v[224:227], a[72:75]\n"
"s_waitcnt vmcnt(12)\n"
"v_exp_f32 v218, v218\n"
"ds_read_b64_tr_b16 a[224:225], v253 offset:28672\n"
"v_add_f32 v242, v242, v218\n"
"v_mfma_f32_16x16x32_bf16 a[76:79], a[140:143], v[224:227], a[76:79]\n"
"v_exp_f32 v219, v219\n"
"s_nop 0 ; auto\n"
"v_add_f32 v242, v242, v219\n"
"v_cvt_pk_bf16_f32 v229, v218, v219\n"
"v_mfma_f32_16x16x32_bf16 a[80:83], a[144:147], v[224:227], a[80:83]\n"
"s_add_u32 m0, s66, 36864\n"
"v_exp_f32 v220, v220\n"
"ds_read_b64_tr_b16 a[228:229], v247 offset:28672\n"
"v_add_f32 v242, v242, v220\n"
"v_mfma_f32_16x16x32_bf16 a[84:87], a[148:151], v[224:227], a[84:87]\n"
"s_add_u32 s62, s46, 0\n"
"v_exp_f32 v221, v221\n"
"s_nop 0 ; auto\n"
"v_add_f32 v242, v242, v221\n"
"v_cvt_pk_bf16_f32 v230, v220, v221\n"
"v_mfma_f32_16x16x32_bf16 a[88:91], a[152:155], v[224:227], a[88:91]\n"
"s_add_u32 s63, s46, 8192\n"
"v_exp_f32 v222, v222\n"
"buffer_load_dwordx4 v252, s[76:79], s62 offen lds\n"
"buffer_load_dwordx4 v252, s[76:79], s63 offen offset:1024 lds\n"
"v_mfma_f32_16x16x32_bf16 a[92:95], a[156:159], v[224:227], a[92:95]\n"
"v_exp_f32 v223, v223\n"
"v_add_f32 v242, v242, v222\n"
"v_add_f32 v242, v242, v223\n"
"v_cvt_pk_bf16_f32 v231, v222, v223\n"
"s_nop 1 ; auto\n"
"v_mfma_f32_16x16x32_bf16 a[96:99], a[128:131], v[228:231], a[96:99]\n"
"v_mov_b32 v208, v236\n"
"v_mov_b32 v209, v237\n"
"v_mov_b32 v210, v238\n"
"ds_read_b64_tr_b16 a[232:233], v253 offset:28928\n"
"v_mov_b32 v211, v239\n"
"v_mov_b32 v212, v240\n"
"v_mov_b32 v213, v241\n"
"v_mfma_f32_16x16x32_bf16 a[100:103], a[132:135], v[228:231], a[100:103]\n"
"v_mov_b32 v214, v242\n"
"v_permlane32_swap_b32 v236, v208\n"
"v_permlane32_swap_b32 v237, v209\n"
"ds_read_b64_tr_b16 a[236:237], v247 offset:28928\n"
"v_permlane32_swap_b32 v238, v210\n"
"v_permlane32_swap_b32 v239, v211\n"
"v_permlane32_swap_b32 v240, v212\n"
"v_mfma_f32_16x16x32_bf16 a[104:107], a[136:139], v[228:231], a[104:107]\n"
"v_permlane32_swap_b32 v241, v213\n"
"v_permlane32_swap_b32 v242, v214\n"
"v_add_f32 v236, v236, v208\n"
"ds_read_b64_tr_b16 a[226:227], v253 offset:30720\n"
"v_add_f32 v237, v237, v209\n"
"v_add_f32 v238, v238, v210\n"
"v_add_f32 v239, v239, v211\n"
"v_mfma_f32_16x16x32_bf16 a[108:111], a[140:143], v[228:231], a[108:111]\n"
"v_add_f32 v240, v240, v212\n"
"v_add_f32 v241, v241, v213\n"
"v_add_f32 v242, v242, v214\n"
"ds_read_b64_tr_b16 a[230:231], v247 offset:30720\n"
"v_mov_b32 v208, v236\n"
"v_mov_b32 v209, v237\n"
"v_mov_b32 v210, v238\n"
"v_mfma_f32_16x16x32_bf16 a[112:115], a[144:147], v[228:231], a[112:115]\n"
"s_add_u32 m0, s66, 38912\n"
"v_mov_b32 v211, v239\n"
"v_mov_b32 v212, v240\n"
"v_mov_b32 v213, v241\n"
"ds_read_b64_tr_b16 a[234:235], v253 offset:30976\n"
"v_mov_b32 v214, v242\n"
"v_permlane16_swap_b32 v236, v208\n"
"v_permlane16_swap_b32 v237, v209\n"
"v_mfma_f32_16x16x32_bf16 a[116:119], a[148:151], v[228:231], a[116:119]\n"
"s_add_u32 s62, s46, 18432\n"
"v_permlane16_swap_b32 v238, v210\n"
"v_permlane16_swap_b32 v239, v211\n"
"v_permlane16_swap_b32 v240, v212\n"
"ds_read_b64_tr_b16 a[238:239], v247 offset:30976\n"
"v_permlane16_swap_b32 v241, v213\n"
"v_permlane16_swap_b32 v242, v214\n"
"v_add_f32 v236, v236, v208\n"
"v_mfma_f32_16x16x32_bf16 a[120:123], a[152:155], v[228:231], a[120:123]\n"
"s_add_u32 s63, s46, 26624\n"
"v_add_f32 v237, v237, v209\n"
"v_add_f32 v238, v238, v210\n"
"v_add_f32 v239, v239, v211\n"
"buffer_load_dwordx4 v252, s[76:79], s62 offen lds\n"
"buffer_load_dwordx4 v252, s[76:79], s63 offen offset:1024 lds\n"
"v_add_f32 v240, v240, v212\n"
"v_add_f32 v241, v241, v213\n"
"v_add_f32 v242, v242, v214\n"
"v_mfma_f32_16x16x32_bf16 a[124:127], a[156:159], v[228:231], a[124:127]\n"
"; phase A\n"
"v_min3_f32 v208, v236, v237, v238\n"
"v_add_f32 v209, v236, v237\n"
"v_add_f32 v210, v238, v239\n"
"v_add_f32 v211, v240, v241\n"
"v_min3_f32 v208, v208, v239, v240\n"
"v_add_f32 v209, v209, v210\n"
"v_add_f32 v211, v211, v242\n"
"v_min3_f32 v208, v208, v241, v242\n"
"v_add_f32 v209, v209, v211\n"
"s_mov_b32 s62, 0x21800000\n"
"s_mov_b32 s63, 0x5d800000\n"
"v_cmp_nle_f32 s[68:69], s62, v208\n"
"v_cmp_nge_f32 vcc, s63, v209\n"
"s_waitcnt lgkmcnt(0)\n"
"s_nop 0\n"
"s_or_b64 vcc, vcc, s[68:69]\n"
"s_cbranch_vccnz Lslow\n"
"Lslowret:\n"
"v_rcp_f32 v236, v236\n"
"v_rcp_f32 v237, v237\n"
"v_rcp_f32 v238, v238\n"
"v_rcp_f32 v239, v239\n"
"v_rcp_f32 v240, v240\n"
"v_rcp_f32 v241, v241\n"
"v_rcp_f32 v242, v242\n"
"v_add_u32 v224, s48, %[tA]\n"
"v_add_u32 v225, s49, %[tA]\n"
"v_add_u32 v226, s50, %[tA]\n"
"v_add_u32 v227, s51, %[tA]\n"
"s_add_u32 m0, s66, 28672\n"
"s_add_u32 s62, s47, 0\n"
"s_add_u32 s63, s47, 8192\n"
"buffer_load_dwordx4 v252, s[76:79], s62 offen lds\n"
"buffer_load_dwordx4 v252, s[76:79], s63 offen offset:1024 lds\n"
"v_mul_f32 v112, v236, v112\n"
"v_mul_f32 v113, v236, v113\n"
"v_mul_f32 v114, v236, v114\n"
"v_mul_f32 v115, v236, v115\n"
"v_mul_f32 v116, v236, v116\n"
"v_mul_f32 v117, v236, v117\n"
"v_mul_f32 v118, v236, v118\n"
"v_mul_f32 v119, v236, v119\n"
"v_cvt_pk_bf16_f32 v112, v112, v113\n"
"v_cvt_pk_bf16_f32 v113, v114, v115\n"
"v_cvt_pk_bf16_f32 v114, v116, v117\n"
"v_cvt_pk_bf16_f32 v115, v118, v119\n"
"s_add_u32 m0, s66, 30720\n"
"s_add_u32 s62, s47, 18432\n"
"s_add_u32 s63, s47, 26624\n"
"buffer_load_dwordx4 v252, s[76:79], s62 offen lds\n"
"buffer_load_dwordx4 v252, s[76:79], s63 offen offset:1024 lds\n"
"v_mfma_f32_16x16x32_bf16 v[0:3], a[224:227], v[112:115], 0\n"
"ds_write_b128 v224, v[112:115] offset:0\n"
"v_mul_f32 v144, v237, v144\n"
"v_mul_f32 v145, v237, v145\n"
"v_mul_f32 v146, v237, v146\n"
"v_mul_f32 v147, v237, v147\n"
"v_mul_f32 v148, v237, v148\n"
"v_mul_f32 v149, v237, v149\n"
"v_mfma_f32_16x16x32_bf16 v[4:7], a[228:231], v[112:115], 0\n"
"v_mul_f32 v150, v237, v150\n"
"v_mul_f32 v151, v237, v151\n"
"v_cvt_pk_bf16_f32 v144, v144, v145\n"
"v_cvt_pk_bf16_f32 v145, v146, v147\n"
"v_cvt_pk_bf16_f32 v146, v148, v149\n"
"v_cvt_pk_bf16_f32 v147, v150, v151\n"
"v_mfma_f32_16x16x32_bf16 v[8:11], a[232:235], v[112:115], 0\n"
"ds_write_b128 v224, v[144:147] offset:1024\n"
"v_mul_f32 v176, v238, v176\n"
"v_mul_f32 v177, v238, v177\n"
"v_mul_f32 v178, v238, v178\n"
"v_mul_f32 v179, v238, v179\n"
"v_mul_f32 v180, v238, v180\n"
"v_mul_f32 v181, v238, v181\n"
"v_mfma_f32_16x16x32_bf16 v[12:15], a[236:239], v[112:115], 0\n"
"v_mul_f32 v182, v238, v182\n"
"v_mul_f32 v183, v238, v183\n"
"v_cvt_pk_bf16_f32 v176, v176, v177\n"
"v_cvt_pk_bf16_f32 v177, v178, v179\n"
"v_cvt_pk_bf16_f32 v178, v180, v181\n"
"v_cvt_pk_bf16_f32 v179, v182, v183\n"
"v_mfma_f32_16x16x32_bf16 v[16:19], a[224:227], v[144:147], 0\n"
"ds_write_b128 v224, v[176:179] offset:2048\n"
"v_accvgpr_read_b32 v208, a0\n"
"v_accvgpr_read_b32 v209, a1\n"
"v_accvgpr_read_b32 v210, a2\n"
"v_accvgpr_read_b32 v211, a3\n"
"v_accvgpr_read_b32 v232, a4\n"
"v_accvgpr_read_b32 v233, a5\n"
"v_accvgpr_read_b32 v234, a6\n"
"v_accvgpr_read_b32 v235, a7\n"
"v_mul_f32 v208, v239, v208\n"
"v_mul_f32 v209, v239, v209\n"
"v_mfma_f32_16x16x32_bf16 v[20:23], a[228:231], v[144:147], 0\n"
"s_waitcnt vmcnt(8)\n"
"ds_read_b64_tr_b16 a[240:241], v253 offset:32768\n"
"ds_read_b64_tr_b16 a[244:245], v247 offset:32768\n"
"v_mul_f32 v210, v239, v210\n"
"v_mul_f32 v211, v239, v211\n"
"v_mul_f32 v232, v239, v232\n"
"v_mul_f32 v233, v239, v233\n"
"v_mul_f32 v234, v239, v234\n"
"v_mul_f32 v235, v239, v235\n"
"v_cvt_pk_bf16_f32 v208, v208, v209\n"
"v_cvt_pk_bf16_f32 v209, v210, v211\n"
"v_cvt_pk_bf16_f32 v210, v232, v233\n"
"v_cvt_pk_bf16_f32 v211, v234, v235\n"
"v_mfma_f32_16x16x32_bf16 v[24:27], a[232:235], v[144:147], 0\n"
"ds_write_b128 v224, v[208:211] offset:3072\n"
"v_accvgpr_read_b32 v212, a32\n"
"v_accvgpr_read_b32 v213, a33\n"
"v_accvgpr_read_b32 v214, a34\n"
"v_accvgpr_read_b32 v215, a35\n"
"v_accvgpr_read_b32 v228, a36\n"
"v_accvgpr_read_b32 v229, a37\n"
"v_accvgpr_read_b32 v230, a38\n"
"v_accvgpr_read_b32 v231, a39\n"
"v_mul_f32 v212, v240, v212\n"
"v_mul_f32 v213, v240, v213\n"
"v_mfma_f32_16x16x32_bf16 v[28:31], a[236:239], v[144:147], 0\n"
"ds_read_b64_tr_b16 a[248:249], v253 offset:33024\n"
"ds_read_b64_tr_b16 a[252:253], v247 offset:33024\n"
"v_mul_f32 v214, v240, v214\n"
"v_mul_f32 v215, v240, v215\n"
"v_mul_f32 v228, v240, v228\n"
"v_mul_f32 v229, v240, v229\n"
"v_mul_f32 v230, v240, v230\n"
"v_mul_f32 v231, v240, v231\n"
"v_cvt_pk_bf16_f32 v212, v212, v213\n"
"v_cvt_pk_bf16_f32 v213, v214, v215\n"
"v_cvt_pk_bf16_f32 v214, v228, v229\n"
"v_cvt_pk_bf16_f32 v215, v230, v231\n"
"v_mfma_f32_16x16x32_bf16 v[32:35], a[224:227], v[176:179], 0\n"
"ds_write_b128 v224, v[212:215] offset:4096\n"
"v_accvgpr_read_b32 v216, a64\n"
"v_accvgpr_read_b32 v217, a65\n"
"v_accvgpr_read_b32 v218, a66\n"
"v_accvgpr_read_b32 v219, a67\n"
"v_accvgpr_read_b32 v232, a68\n"
"v_accvgpr_read_b32 v233, a69\n"
"v_accvgpr_read_b32 v234, a70\n"
"v_accvgpr_read_b32 v235, a71\n"
"v_mul_f32 v216, v241, v216\n"
"v_mul_f32 v217, v241, v217\n"
"v_mfma_f32_16x16x32_bf16 v[36:39], a[228:231], v[176:179], 0\n"
"ds_read_b64_tr_b16 a[242:243], v253 offset:34816\n"
"ds_read_b64_tr_b16 a[246:247], v247 offset:34816\n"
"v_mul_f32 v218, v241, v218\n"
"v_mul_f32 v219, v241, v219\n"
"v_mul_f32 v232, v241, v232\n"
"v_mul_f32 v233, v241, v233\n"
"v_mul_f32 v234, v241, v234\n"
"v_mul_f32 v235, v241, v235\n"
"v_cvt_pk_bf16_f32 v216, v216, v217\n"
"v_cvt_pk_bf16_f32 v217, v218, v219\n"
"v_cvt_pk_bf16_f32 v218, v232, v233\n"
"v_cvt_pk_bf16_f32 v219, v234, v235\n"
"v_mfma_f32_16x16x32_bf16 v[40:43], a[232:235], v[176:179], 0\n"
"ds_write_b128 v224, v[216:219] offset:5120\n"
"v_accvgpr_read_b32 v220, a96\n"
"v_accvgpr_read_b32 v221, a97\n"
"v_accvgpr_read_b32 v222, a98\n"
"v_accvgpr_read_b32 v223, a99\n"
"v_accvgpr_read_b32 v228, a100\n"
"v_accvgpr_read_b32 v229, a101\n"
"v_accvgpr_read_b32 v230, a102\n"
"v_accvgpr_read_b32 v231, a103\n"
"v_mul_f32 v220, v242, v220\n"
"v_mul_f32 v221, v242, v221\n"
"v_mfma_f32_16x16x32_bf16 v[44:47], a[236:239], v[176:179], 0\n"
"ds_read_b64_tr_b16 a[250:251], v253 offset:35072\n"
"ds_read_b64_tr_b16 a[254:255], v247 offset:35072\n"
"v_mul_f32 v222, v242, v222\n"
"v_mul_f32 v223, v242, v223\n"
"v_mul_f32 v228, v242, v228\n"
"v_mul_f32 v229, v242, v229\n"
"v_mul_f32 v230, v242, v230\n"
"v_mul_f32 v231, v242, v231\n"
"v_cvt_pk_bf16_f32 v220, v220, v221\n"
"v_cvt_pk_bf16_f32 v221, v222, v223\n"
"v_cvt_pk_bf16_f32 v222, v228, v229\n"
"v_cvt_pk_bf16_f32 v223, v230, v231\n"
"v_mfma_f32_16x16x32_bf16 v[48:51], a[224:227], v[208:211], 0\n"
"ds_write_b128 v224, v[220:223] offset:6144\n"
"v_mfma_f32_16x16x32_bf16 v[52:55], a[228:231], v[208:211], 0\n"
"v_mfma_f32_16x16x32_bf16 v[56:59], a[232:235], v[208:211], 0\n"
"v_mfma_f32_16x16x32_bf16 v[60:63], a[236:239], v[208:211], 0\n"
"s_waitcnt lgkmcnt(0)\n"
"s_barrier\n"
"v_mfma_f32_16x16x32_bf16 v[64:67], a[224:227], v[212:215], 0\n"
"ds_read_b128 a[156:159], v225 offset:0\n"
"v_mfma_f32_16x16x32_bf16 v[68:71], a[228:231], v[212:215], 0\n"
"ds_read_b128 a[160:163], v225 offset:1024\n"
"v_mfma_f32_16x16x32_bf16 v[72:75], a[232:235], v[212:215], 0\n"
"ds_read_b128 a[164:167], v225 offset:2048\n"
"v_mfma_f32_16x16x32_bf16 v[76:79], a[236:239], v[212:215], 0\n"
"ds_read_b128 a[168:171], v225 offset:3072\n"
"v_mfma_f32_16x16x32_bf16 v[80:83], a[224:227], v[216:219], 0\n"
"ds_read_b128 a[172:175], v225 offset:4096\n"
"v_mfma_f32_16x16x32_bf16 v[84:87], a[228:231], v[216:219], 0\n"
"ds_read_b128 a[176:179], v225 offset:5120\n"
"v_mfma_f32_16x16x32_bf16 v[88:91], a[232:235], v[216:219], 0\n"
"ds_read_b128 a[180:183], v225 offset:6144\n"
"v_mfma_f32_16x16x32_bf16 v[92:95], a[236:239], v[216:219], 0\n"
"s_add_u32 m0, s66, 32768\n"
"s_add_u32 s62, s44, 36864\n"
"buffer_load_dwordx4 v252, s[76:79], s62 offen lds\n"
"v_mfma_f32_16x16x32_bf16 v[96:99], a[224:227], v[220:223], 0\n"
"v_mfma_f32_16x16x32_bf16 v[100:103], a[228:231], v[220:223], 0\n"
"s_add_u32 m0, s66, 33792\n"
"s_add_u32 s62, s44, 46080\n"
"buffer_load_dwordx4 v252, s[76:79], s62 offen lds\n"
"v_mfma_f32_16x16x32_bf16 v[104:107], a[232:235], v[220:223], 0\n"
"v_mfma_f32_16x16x32_bf16 v[108:111], a[236:239], v[220:223], 0\n"
"s_add_u32 m0, s66, 34816\n"
"s_add_u32 s62, s44, 55296\n"
"buffer_load_dwordx4 v252, s[76:79], s62 offen lds\n"
"; oproj step 1\n"
"s_waitcnt lgkmcnt(4)\n"
"v_mfma_f32_16x16x32_bf16 v[0:3], a[240:243], a[156:159], v[0:3]\n"
"s_waitcnt vmcnt(9)\n"
"ds_read_b64_tr_b16 a[224:225], v253 offset:36864\n"
"v_mul_f32 v120, v236, v120\n"
"v_mfma_f32_16x16x32_bf16 v[4:7], a[244:247], a[156:159], v[4:7]\n"
"ds_read_b64_tr_b16 a[228:229], v247 offset:36864\n"
"v_mul_f32 v121, v236, v121\n"
"v_mul_f32 v122, v236, v122\n"
"v_mfma_f32_16x16x32_bf16 v[8:11], a[248:251], a[156:159], v[8:11]\n"
"ds_read_b64_tr_b16 a[232:233], v253 offset:37120\n"
"v_mul_f32 v123, v236, v123\n"
"v_mul_f32 v124, v236, v124\n"
"v_mfma_f32_16x16x32_bf16 v[12:15], a[252:255], a[156:159], v[12:15]\n"
"ds_read_b64_tr_b16 a[236:237], v247 offset:37120\n"
"v_mul_f32 v125, v236, v125\n"
"v_mul_f32 v126, v236, v126\n"
"v_mfma_f32_16x16x32_bf16 v[28:31], a[252:255], a[160:163], v[28:31]\n"
"s_add_u32 m0, s66, 35840\n"
"s_add_u32 s62, s44, 64512\n"
"ds_read_b128 a[128:131], v226 offset:0\n"
"v_mfma_f32_16x16x32_bf16 v[24:27], a[248:251], a[160:163], v[24:27]\n"
"buffer_load_dwordx4 v252, s[76:79], s62 offen lds\n"
"v_mfma_f32_16x16x32_bf16 v[20:23], a[244:247], a[160:163], v[20:23]\n"
"s_waitcnt lgkmcnt(5)\n"
"ds_read_b128 a[132:135], v226 offset:1024\n"
"v_mul_f32 v127, v236, v127\n"
"v_mfma_f32_16x16x32_bf16 v[16:19], a[240:243], a[160:163], v[16:19]\n"
"ds_read_b128 a[136:139], v226 offset:2048\n"
"v_cvt_pk_bf16_f32 v120, v120, v121\n"
"v_cvt_pk_bf16_f32 v121, v122, v123\n"
"v_mfma_f32_16x16x32_bf16 v[32:35], a[240:243], a[164:167], v[32:35]\n"
"ds_read_b128 a[140:143], v226 offset:3072\n"
"v_cvt_pk_bf16_f32 v122, v124, v125\n"
"v_cvt_pk_bf16_f32 v123, v126, v127\n"
"v_mfma_f32_16x16x32_bf16 v[36:39], a[244:247], a[164:167], v[36:39]\n"
"ds_read_b128 a[144:147], v226 offset:4096\n"
"v_mul_f32 v152, v237, v152\n"
"v_mul_f32 v153, v237, v153\n"
"v_mfma_f32_16x16x32_bf16 v[40:43], a[248:251], a[164:167], v[40:43]\n"
"ds_write_b128 v224, v[120:123] offset:7168\n"
"v_mfma_f32_16x16x32_bf16 v[44:47], a[252:255], a[164:167], v[44:47]\n"
"s_add_u32 m0, s66, 36864\n"
"s_add_u32 s62, s45, 36864\n"
"v_mul_f32 v154, v237, v154\n"
"v_mul_f32 v155, v237, v155\n"
"v_mfma_f32_16x16x32_bf16 v[60:63], a[252:255], a[168:171], v[60:63]\n"
"s_waitcnt lgkmcnt(6)\n"
"buffer_load_dwordx4 v252, s[76:79], s62 offen lds\n"
"v_mfma_f32_16x16x32_bf16 v[56:59], a[248:251], a[168:171], v[56:59]\n"
"v_mul_f32 v156, v237, v156\n"
"v_mul_f32 v157, v237, v157\n"
"v_mul_f32 v158, v237, v158\n"
"v_mfma_f32_16x16x32_bf16 v[52:55], a[244:247], a[168:171], v[52:55]\n"
"s_waitcnt vmcnt(9)\n"
"ds_read_b64_tr_b16 a[226:227], v253 offset:38912\n"
"v_mul_f32 v159, v237, v159\n"
"v_mfma_f32_16x16x32_bf16 v[48:51], a[240:243], a[168:171], v[48:51]\n"
"ds_read_b64_tr_b16 a[230:231], v247 offset:38912\n"
"v_cvt_pk_bf16_f32 v152, v152, v153\n"
"v_cvt_pk_bf16_f32 v153, v154, v155\n"
"v_mfma_f32_16x16x32_bf16 v[64:67], a[240:243], a[172:175], v[64:67]\n"
"ds_read_b64_tr_b16 a[234:235], v253 offset:39168\n"
"v_cvt_pk_bf16_f32 v154, v156, v157\n"
"v_cvt_pk_bf16_f32 v155, v158, v159\n"
"v_mfma_f32_16x16x32_bf16 v[68:71], a[244:247], a[172:175], v[68:71]\n"
"ds_read_b64_tr_b16 a[238:239], v247 offset:39168\n"
"v_mul_f32 v184, v238, v184\n"
"v_mul_f32 v185, v238, v185\n"
"v_mfma_f32_16x16x32_bf16 v[72:75], a[248:251], a[172:175], v[72:75]\n"
"s_add_u32 m0, s66, 37888\n"
"s_add_u32 s62, s45, 46080\n"
"ds_read_b128 a[148:151], v226 offset:5120\n"
"v_mfma_f32_16x16x32_bf16 v[76:79], a[252:255], a[172:175], v[76:79]\n"
"buffer_load_dwordx4 v252, s[76:79], s62 offen lds\n"
"v_mfma_f32_16x16x32_bf16 v[92:95], a[252:255], a[176:179], v[92:95]\n"
"ds_read_b128 a[152:155], v226 offset:6144\n"
"v_mul_f32 v186, v238, v186\n"
"v_mul_f32 v187, v238, v187\n"
"v_mfma_f32_16x16x32_bf16 v[88:91], a[248:251], a[176:179], v[88:91]\n"
"ds_read_b64_tr_b16 a[96:97], v250 offset:24576\n"
"v_mul_f32 v188, v238, v188\n"
"v_mul_f32 v189, v238, v189\n"
"v_mfma_f32_16x16x32_bf16 v[84:87], a[244:247], a[176:179], v[84:87]\n"
"ds_read_b64_tr_b16 a[98:99], v250 offset:26624\n"
"v_mul_f32 v190, v238, v190\n"
"v_mul_f32 v191, v238, v191\n"
"v_mfma_f32_16x16x32_bf16 v[80:83], a[240:243], a[176:179], v[80:83]\n"
"ds_write_b128 v224, v[152:155] offset:8192\n"
"v_mfma_f32_16x16x32_bf16 v[96:99], a[240:243], a[180:183], v[96:99]\n"
"v_cvt_pk_bf16_f32 v184, v184, v185\n"
"v_cvt_pk_bf16_f32 v185, v186, v187\n"
"v_cvt_pk_bf16_f32 v186, v188, v189\n"
"v_mfma_f32_16x16x32_bf16 v[100:103], a[244:247], a[180:183], v[100:103]\n"
"s_add_u32 m0, s66, 38912\n"
"s_add_u32 s62, s45, 55296\n"
"v_cvt_pk_bf16_f32 v187, v190, v191\n"
"v_accvgpr_read_b32 v208, a8\n"
"v_mfma_f32_16x16x32_bf16 v[104:107], a[248:251], a[180:183], v[104:107]\n"
"s_waitcnt lgkmcnt(5)\n"
"buffer_load_dwordx4 v252, s[76:79], s62 offen lds\n"
"v_mfma_f32_16x16x32_bf16 v[108:111], a[252:255], a[180:183], v[108:111]\n"
"; oproj step 2\n"
"s_waitcnt lgkmcnt(3)\n"
"ds_write_b128 v224, v[184:187] offset:9216\n"
"v_mfma_f32_16x16x32_bf16 v[0:3], a[224:227], a[128:131], v[0:3]\n"
"s_waitcnt vmcnt(9)\n"
"ds_read_b64_tr_b16 a[240:241], v253 offset:28672\n"
"v_accvgpr_read_b32 v209, a9\n"
"v_mfma_f32_16x16x32_bf16 v[4:7], a[228:231], a[128:131], v[4:7]\n"
"ds_read_b64_tr_b16 a[244:245], v247 offset:28672\n"
"v_accvgpr_read_b32 v210, a10\n"
"v_accvgpr_read_b32 v211, a11\n"
"v_mfma_f32_16x16x32_bf16 v[8:11], a[232:235], a[128:131], v[8:11]\n"
"ds_read_b64_tr_b16 a[248:249], v253 offset:28928\n"
"v_accvgpr_read_b32 v232, a12\n"
"v_accvgpr_read_b32 v233, a13\n"
"v_mfma_f32_16x16x32_bf16 v[12:15], a[236:239], a[128:131], v[12:15]\n"
"ds_read_b64_tr_b16 a[252:253], v247 offset:28928\n"
"v_accvgpr_read_b32 v234, a14\n"
"v_accvgpr_read_b32 v235, a15\n"
"v_mfma_f32_16x16x32_bf16 v[28:31], a[236:239], a[132:135], v[28:31]\n"
"s_add_u32 m0, s66, 39936\n"
"s_add_u32 s62, s45, 64512\n"
"ds_read_b128 a[156:159], v227 offset:0\n"
"v_mfma_f32_16x16x32_bf16 v[24:27], a[232:235], a[132:135], v[24:27]\n"
"buffer_load_dwordx4 v252, s[76:79], s62 offen lds\n"
"v_mfma_f32_16x16x32_bf16 v[20:23], a[228:231], a[132:135], v[20:23]\n"
"ds_read_b128 a[160:163], v227 offset:1024\n"
"v_mul_f32 v208, v239, v208\n"
"v_mul_f32 v209, v239, v209\n"
"v_mfma_f32_16x16x32_bf16 v[16:19], a[224:227], a[132:135], v[16:19]\n"
"ds_read_b128 a[164:167], v227 offset:2048\n"
"v_mul_f32 v210, v239, v210\n"
"v_mul_f32 v211, v239, v211\n"
"v_mfma_f32_16x16x32_bf16 v[32:35], a[224:227], a[136:139], v[32:35]\n"
"ds_read_b128 a[168:171], v227 offset:3072\n"
"v_mul_f32 v232, v239, v232\n"
"v_mul_f32 v233, v239, v233\n"
"v_mfma_f32_16x16x32_bf16 v[36:39], a[228:231], a[136:139], v[36:39]\n"
"ds_read_b128 a[172:175], v227 offset:4096\n"
"v_mul_f32 v234, v239, v234\n"
"v_mul_f32 v235, v239, v235\n"
"v_mfma_f32_16x16x32_bf16 v[40:43], a[232:235], a[136:139], v[40:43]\n"
"v_cvt_pk_bf16_f32 v208, v208, v209\n"
"v_cvt_pk_bf16_f32 v209, v210, v211\n"
"v_cvt_pk_bf16_f32 v210, v232, v233\n"
"v_mfma_f32_16x16x32_bf16 v[44:47], a[236:239], a[136:139], v[44:47]\n"
"s_add_u32 m0, s66, 28672\n"
"s_add_u32 s62, s46, 36864\n"
"v_cvt_pk_bf16_f32 v211, v234, v235\n"
"v_accvgpr_read_b32 v212, a40\n"
"v_mfma_f32_16x16x32_bf16 v[60:63], a[236:239], a[140:143], v[60:63]\n"
"s_waitcnt lgkmcnt(5)\n"
"buffer_load_dwordx4 v252, s[76:79], s62 offen lds\n"
"v_mfma_f32_16x16x32_bf16 v[56:59], a[232:235], a[140:143], v[56:59]\n"
"ds_write_b128 v224, v[208:211] offset:10240\n"
"v_mfma_f32_16x16x32_bf16 v[52:55], a[228:231], a[140:143], v[52:55]\n"
"s_waitcnt vmcnt(9)\n"
"ds_read_b64_tr_b16 a[242:243], v253 offset:30720\n"
"v_accvgpr_read_b32 v213, a41\n"
"v_mfma_f32_16x16x32_bf16 v[48:51], a[224:227], a[140:143], v[48:51]\n"
"ds_read_b64_tr_b16 a[246:247], v247 offset:30720\n"
"v_accvgpr_read_b32 v214, a42\n"
"v_accvgpr_read_b32 v215, a43\n"
"v_mfma_f32_16x16x32_bf16 v[64:67], a[224:227], a[144:147], v[64:67]\n"
"ds_read_b64_tr_b16 a[250:251], v253 offset:30976\n"
"v_accvgpr_read_b32 v228, a44\n"
"v_accvgpr_read_b32 v229, a45\n"
"v_mfma_f32_16x16x32_bf16 v[68:71], a[228:231], a[144:147], v[68:71]\n"
"ds_read_b64_tr_b16 a[254:255], v247 offset:30976\n"
"v_accvgpr_read_b32 v230, a46\n"
"v_accvgpr_read_b32 v231, a47\n"
"v_mfma_f32_16x16x32_bf16 v[72:75], a[232:235], a[144:147], v[72:75]\n"
"s_add_u32 m0, s66, 29696\n"
"s_add_u32 s62, s46, 46080\n"
"ds_read_b128 a[176:179], v227 offset:5120\n"
"v_mfma_f32_16x16x32_bf16 v[76:79], a[236:239], a[144:147], v[76:79]\n"
"buffer_load_dwordx4 v252, s[76:79], s62 offen lds\n"
"v_mfma_f32_16x16x32_bf16 v[92:95], a[236:239], a[148:151], v[92:95]\n"
"ds_read_b128 a[180:183], v227 offset:6144\n"
"v_mul_f32 v212, v240, v212\n"
"v_mul_f32 v213, v240, v213\n"
"v_mfma_f32_16x16x32_bf16 v[88:91], a[232:235], a[148:151], v[88:91]\n"
"ds_read_b64_tr_b16 a[100:101], v251 offset:24576\n"
"v_mul_f32 v214, v240, v214\n"
"v_mul_f32 v215, v240, v215\n"
"v_mfma_f32_16x16x32_bf16 v[84:87], a[228:231], a[148:151], v[84:87]\n"
"ds_read_b64_tr_b16 a[102:103], v251 offset:26624\n"
"v_mul_f32 v228, v240, v228\n"
"v_mul_f32 v229, v240, v229\n"
"v_mfma_f32_16x16x32_bf16 v[80:83], a[224:227], a[148:151], v[80:83]\n"
"v_mul_f32 v230, v240, v230\n"
"v_mul_f32 v231, v240, v231\n"
"v_cvt_pk_bf16_f32 v212, v212, v213\n"
"v_mfma_f32_16x16x32_bf16 v[96:99], a[224:227], a[152:155], v[96:99]\n"
"v_cvt_pk_bf16_f32 v213, v214, v215\n"
"v_cvt_pk_bf16_f32 v214, v228, v229\n"
"v_cvt_pk_bf16_f32 v215, v230, v231\n"
"v_mfma_f32_16x16x32_bf16 v[100:103], a[228:231], a[152:155], v[100:103]\n"
"s_add_u32 m0, s66, 30720\n"
"s_add_u32 s62, s46, 55296\n"
"ds_write_b128 v224, v[212:215] offset:11264\n"
"v_mfma_f32_16x16x32_bf16 v[104:107], a[232:235], a[152:155], v[104:107]\n"
"s_waitcnt lgkmcnt(5)\n"
"buffer_load_dwordx4 v252, s[76:79], s62 offen lds\n"
"v_mfma_f32_16x16x32_bf16 v[108:111], a[236:239], a[152:155], v[108:111]\n"
"; oproj step 3\n"
"s_waitcnt lgkmcnt(3)\n"
"v_accvgpr_read_b32 v216, a72\n"
"v_accvgpr_read_b32 v217, a73\n"
"v_accvgpr_read_b32 v218, a74\n"
"v_mfma_f32_16x16x32_bf16 v[0:3], a[240:243], a[156:159], v[0:3]\n"
"s_waitcnt vmcnt(9)\n"
"ds_read_b64_tr_b16 a[224:225], v253 offset:32768\n"
"v_accvgpr_read_b32 v219, a75\n"
"v_mfma_f32_16x16x32_bf16 v[4:7], a[244:247], a[156:159], v[4:7]\n"
"ds_read_b64_tr_b16 a[228:229], v247 offset:32768\n"
"s_nop 0\n"
"Lh8:\n"
"s_cbranch_execz Lw2\n"
"v_accvgpr_read_b32 v232, a76\n"
"v_accvgpr_read_b32 v233, a77\n"
"v_mfma_f32_16x16x32_bf16 v[8:11], a[248:251], a[156:159], v[8:11]\n"
"ds_read_b64_tr_b16 a[232:233], v253 offset:33024\n"
"v_accvgpr_read_b32 v234, a78\n"
"v_accvgpr_read_b32 v235, a79\n"
"v_mfma_f32_16x16x32_bf16 v[12:15], a[252:255], a[156:159], v[12:15]\n"
"ds_read_b64_tr_b16 a[236:237], v247 offset:33024\n"
"v_mul_f32 v216, v241, v216\n"
"v_mul_f32 v217, v241, v217\n"
"v_mfma_f32_16x16x32_bf16 v[28:31], a[252:255], a[160:163], v[28:31]\n"
"s_add_u32 m0, s66, 31744\n"
"s_add_u32 s62, s46, 64512\n"
"ds_read_b64_tr_b16 a[0:1], v245 offset:20480\n"
"v_mfma_f32_16x16x32_bf16 v[24:27], a[248:251], a[160:163], v[24:27]\n"
"buffer_load_dwordx4 v252, s[76:79], s62 offen lds\n"
"v_mfma_f32_16x16x32_bf16 v[20:23], a[244:247], a[160:163], v[20:23]\n"
"ds_read_b64_tr_b16 a[2:3], v245 offset:22528\n"
"v_mul_f32 v218, v241, v218\n"
"v_mul_f32 v219, v241, v219\n"
"v_mfma_f32_16x16x32_bf16 v[16:19], a[240:243], a[160:163], v[16:19]\n"
"ds_read_b64_tr_b16 a[4:5], v246 offset:20480\n"
"v_mul_f32 v232, v241, v232\n"
"v_mul_f32 v233, v241, v233\n"
"v_mfma_f32_16x16x32_bf16 v[32:35], a[240:243], a[164:167], v[32:35]\n"
"ds_read_b64_tr_b16 a[6:7], v246 offset:22528\n"
"v_mul_f32 v234, v241, v234\n"
"v_mul_f32 v235, v241, v235\n"
"v_mfma_f32_16x16x32_bf16 v[36:39], a[244:247], a[164:167], v[36:39]\n"
"ds_read_b64_tr_b16 a[32:33], v250 offset:20480\n"
"v_cvt_pk_bf16_f32 v216, v216, v217\n"
"v_cvt_pk_bf16_f32 v217, v218, v219\n"
"v_mfma_f32_16x16x32_bf16 v[40:43], a[248:251], a[164:167], v[40:43]\n"
"ds_read_b64_tr_b16 a[34:35], v250 offset:22528\n"
"v_cvt_pk_bf16_f32 v218, v232, v233\n"
"v_cvt_pk_bf16_f32 v219, v234, v235\n"
"v_mfma_f32_16x16x32_bf16 v[44:47], a[252:255], a[164:167], v[44:47]\n"
"s_add_u32 m0, s66, 32768\n"
"s_add_u32 s62, s47, 36864\n"
"ds_read_b64_tr_b16 a[36:37], v251 offset:20480\n"
"v_mfma_f32_16x16x32_bf16 v[60:63], a[252:255], a[168:171], v[60:63]\n"
"s_waitcnt lgkmcnt(7)\n"
"buffer_load_dwordx4 v252, s[76:79], s62 offen lds\n"
"v_mfma_f32_16x16x32_bf16 v[56:59], a[248:251], a[168:171], v[56:59]\n"
"ds_read_b64_tr_b16 a[38:39], v251 offset:22528\n"
"v_accvgpr_read_b32 v220, a104\n"
"v_accvgpr_read_b32 v221, a105\n"
"v_mfma_f32_16x16x32_bf16 v[52:55], a[244:247], a[168:171], v[52:55]\n"
"s_waitcnt vmcnt(9)\n"
"ds_read_b64_tr_b16 a[226:227], v253 offset:34816\n"
"v_accvgpr_read_b32 v222, a106\n"
"v_mfma_f32_16x16x32_bf16 v[48:51], a[240:243], a[168:171], v[48:51]\n"
"ds_read_b64_tr_b16 a[230:231], v247 offset:34816\n"
"v_accvgpr_read_b32 v223, a107\n"
"v_accvgpr_read_b32 v228, a108\n"
"v_mfma_f32_16x16x32_bf16 v[64:67], a[240:243], a[172:175], v[64:67]\n"
"ds_read_b64_tr_b16 a[234:235], v253 offset:35072\n"
"v_accvgpr_read_b32 v229, a109\n"
"v_accvgpr_read_b32 v230, a110\n"
"v_mfma_f32_16x16x32_bf16 v[68:71], a[244:247], a[172:175], v[68:71]\n"
"ds_read_b64_tr_b16 a[238:239], v247 offset:35072\n"
"v_accvgpr_read_b32 v231, a111\n"
"v_mul_f32 v220, v242, v220\n"
"v_mfma_f32_16x16x32_bf16 v[72:75], a[248:251], a[172:175], v[72:75]\n"
"s_add_u32 m0, s66, 33792\n"
"s_add_u32 s62, s47, 46080\n"
"ds_read_b64_tr_b16 a[64:65], v245 offset:24576\n"
"v_mfma_f32_16x16x32_bf16 v[76:79], a[252:255], a[172:175], v[76:79]\n"
"buffer_load_dwordx4 v252, s[76:79], s62 offen lds\n"
"v_mfma_f32_16x16x32_bf16 v[92:95], a[252:255], a[176:179], v[92:95]\n"
"ds_read_b64_tr_b16 a[66:67], v245 offset:26624\n"
"v_mul_f32 v221, v242, v221\n"
"v_mul_f32 v222, v242, v222\n"
"v_mfma_f32_16x16x32_bf16 v[88:91], a[248:251], a[176:179], v[88:91]\n"
"ds_read_b64_tr_b16 a[68:69], v246 offset:24576\n"
"v_mul_f32 v223, v242, v223\n"
"v_mul_f32 v228, v242, v228\n"
"v_mfma_f32_16x16x32_bf16 v[84:87], a[244:247], a[176:179], v[84:87]\n"
"ds_read_b64_tr_b16 a[70:71], v246 offset:26624\n"
"v_mul_f32 v229, v242, v229\n"
"v_mul_f32 v230, v242, v230\n"
"v_mfma_f32_16x16x32_bf16 v[80:83], a[240:243], a[176:179], v[80:83]\n"
"ds_write_b128 v224, v[216:219] offset:12288\n"
"v_mfma_f32_16x16x32_bf16 v[96:99], a[240:243], a[180:183], v[96:99]\n"
"v_mul_f32 v231, v242, v231\n"
"v_cvt_pk_bf16_f32 v220, v220, v221\n"
"v_cvt_pk_bf16_f32 v221, v222, v223\n"
"v_mfma_f32_16x16x32_bf16 v[100:103], a[244:247], a[180:183], v[100:103]\n"
"s_add_u32 m0, s66, 34816\n"
"s_add_u32 s62, s47, 55296\n"
"v_cvt_pk_bf16_f32 v222, v228, v229\n"
"v_cvt_pk_bf16_f32 v223, v230, v231\n"
"v_mfma_f32_16x16x32_bf16 v[104:107], a[248:251], a[180:183], v[104:107]\n"
"s_waitcnt lgkmcnt(5)\n"
"buffer_load_dwordx4 v252, s[76:79], s62 offen lds\n"
"v_mfma_f32_16x16x32_bf16 v[108:111], a[252:255], a[180:183], v[108:111]\n"
"; oproj step 4\n"
"ds_write_b128 v224, v[220:223] offset:13312\n"
"v_mfma_f32_16x16x32_bf16 v[0:3], a[224:227], v[120:123], v[0:3]\n"
"s_waitcnt vmcnt(9)\n"
"ds_read_b64_tr_b16 a[240:241], v253 offset:36864\n"
"v_mul_f32 v128, v236, v128\n"
"v_mfma_f32_16x16x32_bf16 v[4:7], a[228:231], v[120:123], v[4:7]\n"
"ds_read_b64_tr_b16 a[244:245], v247 offset:36864\n"
"v_mul_f32 v129, v236, v129\n"
"v_mul_f32 v130, v236, v130\n"
"v_mfma_f32_16x16x32_bf16 v[8:11], a[232:235], v[120:123], v[8:11]\n"
"ds_read_b64_tr_b16 a[248:249], v253 offset:37120\n"
"v_mul_f32 v131, v236, v131\n"
"v_mul_f32 v132, v236, v132\n"
"v_mfma_f32_16x16x32_bf16 v[12:15], a[236:239], v[120:123], v[12:15]\n"
"ds_read_b64_tr_b16 a[252:253], v247 offset:37120\n"
"v_mul_f32 v133, v236, v133\n"
"v_mul_f32 v134, v236, v134\n"
"v_mfma_f32_16x16x32_bf16 v[28:31], a[236:239], v[152:155], v[28:31]\n"
"s_add_u32 m0, s66, 35840\n"
"s_add_u32 s62, s47, 64512\n"
"v_mul_f32 v135, v236, v135\n"
"v_cvt_pk_bf16_f32 v128, v128, v129\n"
"v_mfma_f32_16x16x32_bf16 v[24:27], a[232:235], v[152:155], v[24:27]\n"
"buffer_load_dwordx4 v252, s[76:79], s62 offen lds\n"
"v_mfma_f32_16x16x32_bf16 v[20:23], a[228:231], v[152:155], v[20:23]\n"
"v_cvt_pk_bf16_f32 v129, v130, v131\n"
"v_cvt_pk_bf16_f32 v130, v132, v133\n"
"v_cvt_pk_bf16_f32 v131, v134, v135\n"
"v_mfma_f32_16x16x32_bf16 v[16:19], a[224:227], v[152:155], v[16:19]\n"
"v_mul_f32 v160, v237, v160\n"
"v_mul_f32 v161, v237, v161\n"
"v_mul_f32 v162, v237, v162\n"
"v_mfma_f32_16x16x32_bf16 v[32:35], a[224:227], v[184:187], v[32:35]\n"
"v_mul_f32 v163, v237, v163\n"
"v_mul_f32 v164, v237, v164\n"
"v_mul_f32 v165, v237, v165\n"
"v_mfma_f32_16x16x32_bf16 v[36:39], a[228:231], v[184:187], v[36:39]\n"
"s_waitcnt lgkmcnt(4)\n"
"s_barrier\n"
"v_mul_f32 v166, v237, v166\n"
"v_mul_f32 v167, v237, v167\n"
"v_mfma_f32_16x16x32_bf16 v[40:43], a[232:235], v[184:187], v[40:43]\n"
"ds_read_b128 a[156:159], v225 offset:7168\n"
"v_cvt_pk_bf16_f32 v160, v160, v161\n"
"v_cvt_pk_bf16_f32 v161, v162, v163\n"
"v_mfma_f32_16x16x32_bf16 v[44:47], a[236:239], v[184:187], v[44:47]\n"
"s_add_u32 m0, s66, 36864\n"
"s_add_u32 s62, s44, 73728\n"
"ds_read_b128 a[160:163], v225 offset:8192\n"
"v_mfma_f32_16x16x32_bf16 v[60:63], a[236:239], v[208:211], v[60:63]\n"
"s_waitcnt lgkmcnt(2)\n"
"buffer_load_dwordx4 v252, s[76:79], s62 offen lds\n"
"v_mfma_f32_16x16x32_bf16 v[56:59], a[232:235], v[208:211], v[56:59]\n"
"ds_read_b128 a[164:167], v225 offset:9216\n"
"v_cvt_pk_bf16_f32 v162, v164, v165\n"
"v_cvt_pk_bf16_f32 v163, v166, v167\n"
"v_mfma_f32_16x16x32_bf16 v[52:55], a[228:231], v[208:211], v[52:55]\n"
"s_waitcnt vmcnt(9)\n"
"ds_read_b64_tr_b16 a[242:243], v253 offset:38912\n"
"v_mfma_f32_16x16x32_bf16 v[48:51], a[224:227], v[208:211], v[48:51]\n"
"ds_read_b64_tr_b16 a[246:247], v247 offset:38912\n"
"v_mul_f32 v192, v238, v192\n"
"v_mfma_f32_16x16x32_bf16 v[64:67], a[224:227], v[212:215], v[64:67]\n"
"ds_read_b64_tr_b16 a[250:251], v253 offset:39168\n"
"v_mul_f32 v193, v238, v193\n"
"v_mfma_f32_16x16x32_bf16 v[68:71], a[228:231], v[212:215], v[68:71]\n"
"ds_read_b64_tr_b16 a[254:255], v247 offset:39168\n"
"v_mul_f32 v194, v238, v194\n"
"v_mfma_f32_16x16x32_bf16 v[72:75], a[232:235], v[212:215], v[72:75]\n"
"s_add_u32 m0, s66, 37888\n"
"s_add_u32 s62, s44, 82944\n"
"ds_read_b128 a[168:171], v225 offset:10240\n"
"v_mfma_f32_16x16x32_bf16 v[76:79], a[236:239], v[212:215], v[76:79]\n"
"buffer_load_dwordx4 v252, s[76:79], s62 offen lds\n"
"v_mfma_f32_16x16x32_bf16 v[92:95], a[236:239], v[216:219], v[92:95]\n"
"ds_read_b128 a[172:175], v225 offset:11264\n"
"v_mul_f32 v195, v238, v195\n"
"v_mfma_f32_16x16x32_bf16 v[88:91], a[232:235], v[216:219], v[88:91]\n"
"ds_read_b128 a[176:179], v225 offset:12288\n"
"v_mul_f32 v196, v238, v196\n"
"v_mfma_f32_16x16x32_bf16 v[84:87], a[228:231], v[216:219], v[84:87]\n"
"ds_read_b128 a[180:183], v225 offset:13312\n"
"v_mul_f32 v197, v238, v197\n"
"v_mfma_f32_16x16x32_bf16 v[80:83], a[224:227], v[216:219], v[80:83]\n"
"ds_write_b128 v224, v[128:131] offset:14336\n"
"v_mfma_f32_16x16x32_bf16 v[96:99], a[224:227], v[220:223], v[96:99]\n"
"ds_write_b128 v224, v[160:163] offset:15360\n"
"v_mfma_f32_16x16x32_bf16 v[100:103], a[228:231], v[220:223], v[100:103]\n"
"s_add_u32 m0, s66, 38912\n"
"s_add_u32 s62, s44, 92160\n"
"v_mul_f32 v198, v238, v198\n"
"v_mfma_f32_16x16x32_bf16 v[104:107], a[232:235], v[220:223], v[104:107]\n"
"s_waitcnt lgkmcnt(6)\n"
"buffer_load_dwordx4 v252, s[76:79], s62 offen lds\n"
"v_mfma_f32_16x16x32_bf16 v[108:111], a[236:239], v[220:223], v[108:111]\n"
"; oproj step 5\n"
"v_mul_f32 v199, v238, v199\n"
"v_cvt_pk_bf16_f32 v192, v192, v193\n"
"v_mfma_f32_16x16x32_bf16 v[0:3], a[240:243], a[156:159], v[0:3]\n"
"s_waitcnt vmcnt(9)\n"
"ds_read_b64_tr_b16 a[224:225], v253 offset:28672\n"
"v_mfma_f32_16x16x32_bf16 v[4:7], a[244:247], a[156:159], v[4:7]\n"
"ds_read_b64_tr_b16 a[228:229], v247 offset:28672\n"
"v_cvt_pk_bf16_f32 v193, v194, v195\n"
"v_mfma_f32_16x16x32_bf16 v[8:11], a[248:251], a[156:159], v[8:11]\n"
"ds_read_b64_tr_b16 a[232:233], v253 offset:28928\n"
"v_cvt_pk_bf16_f32 v194, v196, v197\n"
"v_mfma_f32_16x16x32_bf16 v[12:15], a[252:255], a[156:159], v[12:15]\n"
"ds_read_b64_tr_b16 a[236:237], v247 offset:28928\n"
"v_cvt_pk_bf16_f32 v195, v198, v199\n"
"v_accvgpr_read_b32 v208, a16\n"
"v_mfma_f32_16x16x32_bf16 v[28:31], a[252:255], a[160:163], v[28:31]\n"
"s_add_u32 m0, s66, 39936\n"
"s_add_u32 s62, s44, 101376\n"
"ds_read_b128 a[128:131], v226 offset:7168\n"
"v_mfma_f32_16x16x32_bf16 v[24:27], a[248:251], a[160:163], v[24:27]\n"
"buffer_load_dwordx4 v252, s[76:79], s62 offen lds\n"
"v_mfma_f32_16x16x32_bf16 v[20:23], a[244:247], a[160:163], v[20:23]\n"
"s_waitcnt lgkmcnt(7)\n"
"ds_read_b128 a[132:135], v226 offset:8192\n"
"v_accvgpr_read_b32 v209, a17\n"
"v_mfma_f32_16x16x32_bf16 v[16:19], a[240:243], a[160:163], v[16:19]\n"
"ds_read_b128 a[136:139], v226 offset:9216\n"
"v_accvgpr_read_b32 v210, a18\n"
"v_accvgpr_read_b32 v211, a19\n"
"v_mfma_f32_16x16x32_bf16 v[32:35], a[240:243], a[164:167], v[32:35]\n"
"ds_read_b128 a[140:143], v226 offset:10240\n"
"v_accvgpr_read_b32 v232, a20\n"
"v_accvgpr_read_b32 v233, a21\n"
"v_mfma_f32_16x16x32_bf16 v[36:39], a[244:247], a[164:167], v[36:39]\n"
"ds_read_b128 a[144:147], v226 offset:11264\n"
"v_accvgpr_read_b32 v234, a22\n"
"v_accvgpr_read_b32 v235, a23\n"
"v_mfma_f32_16x16x32_bf16 v[40:43], a[248:251], a[164:167], v[40:43]\n"
"ds_write_b128 v224, v[192:195] offset:16384\n"
"v_mfma_f32_16x16x32_bf16 v[44:47], a[252:255], a[164:167], v[44:47]\n"
"s_add_u32 m0, s66, 28672\n"
"s_add_u32 s62, s45, 73728\n"
"v_mul_f32 v208, v239, v208\n"
"v_mul_f32 v209, v239, v209\n"
"v_mfma_f32_16x16x32_bf16 v[60:63], a[252:255], a[168:171], v[60:63]\n"
"s_waitcnt lgkmcnt(6)\n"
"buffer_load_dwordx4 v252, s[76:79], s62 offen lds\n"
"v_mfma_f32_16x16x32_bf16 v[56:59], a[248:251], a[168:171], v[56:59]\n"
"v_mul_f32 v210, v239, v210\n"
"v_mul_f32 v211, v239, v211\n"
"v_mul_f32 v232, v239, v232\n"
"v_mfma_f32_16x16x32_bf16 v[52:55], a[244:247], a[168:171], v[52:55]\n"
"s_waitcnt vmcnt(9)\n"
"ds_read_b64_tr_b16 a[226:227], v253 offset:30720\n"
"v_mul_f32 v233, v239, v233\n"
"v_mfma_f32_16x16x32_bf16 v[48:51], a[240:243], a[168:171], v[48:51]\n"
"ds_read_b64_tr_b16 a[230:231], v247 offset:30720\n"
"v_mul_f32 v234, v239, v234\n"
"v_mul_f32 v235, v239, v235\n"
"v_mfma_f32_16x16x32_bf16 v[64:67], a[240:243], a[172:175], v[64:67]\n"
"ds_read_b64_tr_b16 a[234:235], v253 offset:30976\n"
"v_cvt_pk_bf16_f32 v208, v208, v209\n"
"v_cvt_pk_bf16_f32 v209, v210, v211\n"
"v_mfma_f32_16x16x32_bf16 v[68:71], a[244:247], a[172:175], v[68:71]\n"
"ds_read_b64_tr_b16 a[238:239], v247 offset:30976\n"
"v_cvt_pk_bf16_f32 v210, v232, v233\n"
"v_cvt_pk_bf16_f32 v211, v234, v235\n"
"v_mfma_f32_16x16x32_bf16 v[72:75], a[248:251], a[172:175], v[72:75]\n"
"s_add_u32 m0, s66, 29696\n"
"s_add_u32 s62, s45, 82944\n"
"ds_read_b128 a[148:151], v226 offset:12288\n"
"v_mfma_f32_16x16x32_bf16 v[76:79], a[252:255], a[172:175], v[76:79]\n"
"buffer_load_dwordx4 v252, s[76:79], s62 offen lds\n"
"v_mfma_f32_16x16x32_bf16 v[92:95], a[252:255], a[176:179], v[92:95]\n"
"ds_read_b128 a[152:155], v226 offset:13312\n"
"v_accvgpr_read_b32 v212, a48\n"
"v_mfma_f32_16x16x32_bf16 v[88:91], a[248:251], a[176:179], v[88:91]\n"
"ds_write_b128 v224, v[208:211] offset:17408\n"
"v_mfma_f32_16x16x32_bf16 v[84:87], a[244:247], a[176:179], v[84:87]\n"
"v_accvgpr_read_b32 v213, a49\n"
"v_accvgpr_read_b32 v214, a50\n"
"v_mfma_f32_16x16x32_bf16 v[80:83], a[240:243], a[176:179], v[80:83]\n"
"v_accvgpr_read_b32 v215, a51\n"
"v_accvgpr_read_b32 v228, a52\n"
"v_mfma_f32_16x16x32_bf16 v[96:99], a[240:243], a[180:183], v[96:99]\n"
"v_accvgpr_read_b32 v229, a53\n"
"v_accvgpr_read_b32 v230, a54\n"
"v_mfma_f32_16x16x32_bf16 v[100:103], a[244:247], a[180:183], v[100:103]\n"
"s_add_u32 m0, s66, 30720\n"
"s_add_u32 s62, s45, 92160\n"
"v_accvgpr_read_b32 v231, a55\n"
"v_mfma_f32_16x16x32_bf16 v[104:107], a[248:251], a[180:183], v[104:107]\n"
"s_waitcnt lgkmcnt(3)\n"
"buffer_load_dwordx4 v252, s[76:79], s62 offen lds\n"
"v_mfma_f32_16x16x32_bf16 v[108:111], a[252:255], a[180:183], v[108:111]\n"
"; oproj step 6\n"
"s_waitcnt lgkmcnt(1)\n"
"v_mul_f32 v212, v240, v212\n"
"v_mul_f32 v213, v240, v213\n"
"v_mfma_f32_16x16x32_bf16 v[0:3], a[224:227], a[128:131], v[0:3]\n"
"s_waitcnt vmcnt(9)\n"
"ds_read_b64_tr_b16 a[240:241], v253 offset:32768\n"
"v_mfma_f32_16x16x32_bf16 v[4:7], a[228:231], a[128:131], v[4:7]\n"
"ds_read_b64_tr_b16 a[244:245], v247 offset:32768\n"
"v_mul_f32 v214, v240, v214\n"
"v_mfma_f32_16x16x32_bf16 v[8:11], a[232:235], a[128:131], v[8:11]\n"
"ds_read_b64_tr_b16 a[248:249], v253 offset:33024\n"
"v_mul_f32 v215, v240, v215\n"
"v_mfma_f32_16x16x32_bf16 v[12:15], a[236:239], a[128:131], v[12:15]\n"
"ds_read_b64_tr_b16 a[252:253], v247 offset:33024\n"
"v_mul_f32 v228, v240, v228\n"
"v_mfma_f32_16x16x32_bf16 v[28:31], a[236:239], a[132:135], v[28:31]\n"
"s_add_u32 m0, s66, 31744\n"
"s_add_u32 s62, s45, 101376\n"
"ds_read_b128 a[156:159], v227 offset:7168\n"
"v_mfma_f32_16x16x32_bf16 v[24:27], a[232:235], a[132:135], v[24:27]\n"
"buffer_load_dwordx4 v252, s[76:79], s62 offen lds\n"
"v_mfma_f32_16x16x32_bf16 v[20:23], a[228:231], a[132:135], v[20:23]\n"
"ds_read_b128 a[160:163], v227 offset:8192\n"
"v_mul_f32 v229, v240, v229\n"
"v_mfma_f32_16x16x32_bf16 v[16:19], a[224:227], a[132:135], v[16:19]\n"
"ds_read_b128 a[164:167], v227 offset:9216\n"
"v_mul_f32 v230, v240, v230\n"
"v_mfma_f32_16x16x32_bf16 v[32:35], a[224:227], a[136:139], v[32:35]\n"
"ds_read_b128 a[168:171], v227 offset:10240\n"
"v_mul_f32 v231, v240, v231\n"
"v_mfma_f32_16x16x32_bf16 v[36:39], a[228:231], a[136:139], v[36:39]\n"
"ds_read_b128 a[172:175], v227 offset:11264\n"
"v_cvt_pk_bf16_f32 v212, v212, v213\n"
"v_mfma_f32_16x16x32_bf16 v[40:43], a[232:235], a[136:139], v[40:43]\n"
"v_cvt_pk_bf16_f32 v213, v214, v215\n"
"v_cvt_pk_bf16_f32 v214, v228, v229\n"
"v_mfma_f32_16x16x32_bf16 v[44:47], a[236:239], a[136:139], v[44:47]\n"
"s_add_u32 m0, s66, 32768\n"
"s_add_u32 s62, s46, 73728\n"
"v_cvt_pk_bf16_f32 v215, v230, v231\n"
"v_mfma_f32_16x16x32_bf16 v[60:63], a[236:239], a[140:143], v[60:63]\n"
"s_waitcnt lgkmcnt(5)\n"
"buffer_load_dwordx4 v252, s[76:79], s62 offen lds\n"
"v_mfma_f32_16x16x32_bf16 v[56:59], a[232:235], a[140:143], v[56:59]\n"
"ds_write_b128 v224, v[212:215] offset:18432\n"
"v_mfma_f32_16x16x32_bf16 v[52:55], a[228:231], a[140:143], v[52:55]\n"
"s_waitcnt vmcnt(9)\n"
"ds_read_b64_tr_b16 a[242:243], v253 offset:34816\n"
"v_mfma_f32_16x16x32_bf16 v[48:51], a[224:227], a[140:143], v[48:51]\n"
"ds_read_b64_tr_b16 a[246:247], v247 offset:34816\n"
"v_accvgpr_read_b32 v216, a80\n"
"v_mfma_f32_16x16x32_bf16 v[64:67], a[224:227], a[144:147], v[64:67]\n"
"ds_read_b64_tr_b16 a[250:251], v253 offset:35072\n"
"v_accvgpr_read_b32 v217, a81\n"
"v_mfma_f32_16x16x32_bf16 v[68:71], a[228:231], a[144:147], v[68:71]\n"
"ds_read_b64_tr_b16 a[254:255], v247 offset:35072\n"
"v_accvgpr_read_b32 v218, a82\n"
"v_mfma_f32_16x16x32_bf16 v[72:75], a[232:235], a[144:147], v[72:75]\n"
"s_add_u32 m0, s66, 33792\n"
"s_add_u32 s62, s46, 82944\n"
"ds_read_b128 a[176:179], v227 offset:12288\n"
"v_mfma_f32_16x16x32_bf16 v[76:79], a[236:239], a[144:147], v[76:79]\n"
"buffer_load_dwordx4 v252, s[76:79], s62 offen lds\n"
"v_mfma_f32_16x16x32_bf16 v[92:95], a[236:239], a[148:151], v[92:95]\n"
"ds_read_b128 a[180:183], v227 offset:13312\n"
"v_accvgpr_read_b32 v219, a83\n"
"v_mfma_f32_16x16x32_bf16 v[88:91], a[232:235], a[148:151], v[88:91]\n"
"v_accvgpr_read_b32 v232, a84\n"
"v_accvgpr_read_b32 v233, a85\n"
"v_mfma_f32_16x16x32_bf16 v[84:87], a[228:231], a[148:151], v[84:87]\n"
"v_accvgpr_read_b32 v234, a86\n"
"v_accvgpr_read_b32 v235, a87\n"
"v_mfma_f32_16x16x32_bf16 v[80:83], a[224:227], a[148:151], v[80:83]\n"
"v_mul_f32 v216, v241, v216\n"
"v_mul_f32 v217, v241, v217\n"
"v_mfma_f32_16x16x32_bf16 v[96:99], a[224:227], a[152:155], v[96:99]\n"
"v_mul_f32 v218, v241, v218\n"
"v_mul_f32 v219, v241, v219\n"
"v_mfma_f32_16x16x32_bf16 v[100:103], a[228:231], a[152:155], v[100:103]\n"
"s_add_u32 m0, s66, 34816\n"
"s_add_u32 s62, s46, 92160\n"
"v_mul_f32 v232, v241, v232\n"
"v_mfma_f32_16x16x32_bf16 v[104:107], a[232:235], a[152:155], v[104:107]\n"
"s_waitcnt lgkmcnt(2)\n"
"buffer_load_dwordx4 v252, s[76:79], s62 offen lds\n"
"v_mfma_f32_16x16x32_bf16 v[108:111], a[236:239], a[152:155], v[108:111]\n"
"; oproj step 7\n"
"s_waitcnt lgkmcnt(0)\n"
"v_mul_f32 v233, v241, v233\n"
"v_mul_f32 v234, v241, v234\n"
"v_mfma_f32_16x16x32_bf16 v[0:3], a[240:243], a[156:159], v[0:3]\n"
"s_waitcnt vmcnt(9)\n"
"ds_read_b64_tr_b16 a[224:225], v253 offset:36864\n"
"v_mfma_f32_16x16x32_bf16 v[4:7], a[244:247], a[156:159], v[4:7]\n"
"ds_read_b64_tr_b16 a[228:229], v247 offset:36864\n"
"v_mul_f32 v235, v241, v235\n"
"v_mfma_f32_16x16x32_bf16 v[8:11], a[248:251], a[156:159], v[8:11]\n"
"ds_read_b64_tr_b16 a[232:233], v253 offset:37120\n"
"v_cvt_pk_bf16_f32 v216, v216, v217\n"
"v_mfma_f32_16x16x32_bf16 v[12:15], a[252:255], a[156:159], v[12:15]\n"
"ds_read_b64_tr_b16 a[236:237], v247 offset:37120\n"
"v_cvt_pk_bf16_f32 v217, v218, v219\n"
"v_mfma_f32_16x16x32_bf16 v[28:31], a[252:255], a[160:163], v[28:31]\n"
"s_add_u32 m0, s66, 35840\n"
"s_add_u32 s62, s46, 101376\n"
"v_cvt_pk_bf16_f32 v218, v232, v233\n"
"v_mfma_f32_16x16x32_bf16 v[24:27], a[248:251], a[160:163], v[24:27]\n"
"buffer_load_dwordx4 v252, s[76:79], s62 offen lds\n"
"v_mfma_f32_16x16x32_bf16 v[20:23], a[244:247], a[160:163], v[20:23]\n"
"v_cvt_pk_bf16_f32 v219, v234, v235\n"
"v_accvgpr_read_b32 v220, a112\n"
"v_mfma_f32_16x16x32_bf16 v[16:19], a[240:243], a[160:163], v[16:19]\n"
"ds_write_b128 v224, v[216:219] offset:19456\n"
"v_mfma_f32_16x16x32_bf16 v[32:35], a[240:243], a[164:167], v[32:35]\n"
"v_accvgpr_read_b32 v221, a113\n"
"v_accvgpr_read_b32 v222, a114\n"
"v_mfma_f32_16x16x32_bf16 v[36:39], a[244:247], a[164:167], v[36:39]\n"
"v_accvgpr_read_b32 v223, a115\n"
"v_accvgpr_read_b32 v228, a116\n"
"v_mfma_f32_16x16x32_bf16 v[40:43], a[248:251], a[164:167], v[40:43]\n"
"v_accvgpr_read_b32 v229, a117\n"
"v_accvgpr_read_b32 v230, a118\n"
"v_mfma_f32_16x16x32_bf16 v[44:47], a[252:255], a[164:167], v[44:47]\n"
"s_add_u32 m0, s66, 36864\n"
"s_add_u32 s62, s47, 73728\n"
"v_accvgpr_read_b32 v231, a119\n"
"v_mfma_f32_16x16x32_bf16 v[60:63], a[252:255], a[168:171], v[60:63]\n"
"s_waitcnt lgkmcnt(1)\n"
"buffer_load_dwordx4 v252, s[76:79], s62 offen lds\n"
"v_mfma_f32_16x16x32_bf16 v[56:59], a[248:251], a[168:171], v[56:59]\n"
"v_mul_f32 v220, v242, v220\n"
"v_mul_f32 v221, v242, v221\n"
"v_mfma_f32_16x16x32_bf16 v[52:55], a[244:247], a[168:171], v[52:55]\n"
"s_waitcnt vmcnt(9)\n"
"ds_read_b64_tr_b16 a[226:227], v253 offset:38912\n"
"v_mfma_f32_16x16x32_bf16 v[48:51], a[240:243], a[168:171], v[48:51]\n"
"ds_read_b64_tr_b16 a[230:231], v247 offset:38912\n"
"v_mul_f32 v222, v242, v222\n"
"v_mfma_f32_16x16x32_bf16 v[64:67], a[240:243], a[172:175], v[64:67]\n"
"ds_read_b64_tr_b16 a[234:235], v253 offset:39168\n"
"v_mul_f32 v223, v242, v223\n"
"v_mfma_f32_16x16x32_bf16 v[68:71], a[244:247], a[172:175], v[68:71]\n"
"ds_read_b64_tr_b16 a[238:239], v247 offset:39168\n"
"v_mul_f32 v228, v242, v228\n"
"v_mfma_f32_16x16x32_bf16 v[72:75], a[248:251], a[172:175], v[72:75]\n"
"s_add_u32 m0, s66, 37888\n"
"s_add_u32 s62, s47, 82944\n"
"v_mul_f32 v229, v242, v229\n"
"v_mfma_f32_16x16x32_bf16 v[76:79], a[252:255], a[172:175], v[76:79]\n"
"buffer_load_dwordx4 v252, s[76:79], s62 offen lds\n"
"v_mfma_f32_16x16x32_bf16 v[92:95], a[252:255], a[176:179], v[92:95]\n"
"v_mul_f32 v230, v242, v230\n"
"v_mul_f32 v231, v242, v231\n"
"v_mfma_f32_16x16x32_bf16 v[88:91], a[248:251], a[176:179], v[88:91]\n"
"v_cvt_pk_bf16_f32 v220, v220, v221\n"
"v_cvt_pk_bf16_f32 v221, v222, v223\n"
"v_mfma_f32_16x16x32_bf16 v[84:87], a[244:247], a[176:179], v[84:87]\n"
"v_cvt_pk_bf16_f32 v222, v228, v229\n"
"v_cvt_pk_bf16_f32 v223, v230, v231\n"
"v_mul_f32 v136, v236, v136\n"
"v_mfma_f32_16x16x32_bf16 v[80:83], a[240:243], a[176:179], v[80:83]\n"
"ds_write_b128 v224, v[220:223] offset:20480\n"
"v_mfma_f32_16x16x32_bf16 v[96:99], a[240:243], a[180:183], v[96:99]\n"
"v_mul_f32 v137, v236, v137\n"
"v_mul_f32 v138, v236, v138\n"
"v_mul_f32 v139, v236, v139\n"
"v_mfma_f32_16x16x32_bf16 v[100:103], a[244:247], a[180:183], v[100:103]\n"
"s_add_u32 m0, s66, 38912\n"
"s_add_u32 s62, s47, 92160\n"
"v_mul_f32 v140, v236, v140\n"
"v_mul_f32 v141, v236, v141\n"
"v_mfma_f32_16x16x32_bf16 v[104:107], a[248:251], a[180:183], v[104:107]\n"
"s_waitcnt lgkmcnt(1)\n"
"buffer_load_dwordx4 v252, s[76:79], s62 offen lds\n"
"v_mfma_f32_16x16x32_bf16 v[108:111], a[252:255], a[180:183], v[108:111]\n"
"; oproj step 8\n"
"v_mul_f32 v142, v236, v142\n"
"v_mul_f32 v143, v236, v143\n"
"v_cvt_pk_bf16_f32 v136, v136, v137\n"
"v_mfma_f32_16x16x32_bf16 v[0:3], a[224:227], v[128:131], v[0:3]\n"
"s_waitcnt vmcnt(9)\n"
"ds_read_b64_tr_b16 a[240:241], v253 offset:28672\n"
"v_cvt_pk_bf16_f32 v137, v138, v139\n"
"v_mfma_f32_16x16x32_bf16 v[4:7], a[228:231], v[128:131], v[4:7]\n"
"ds_read_b64_tr_b16 a[244:245], v247 offset:28672\n"
"v_cvt_pk_bf16_f32 v138, v140, v141\n"
"v_cvt_pk_bf16_f32 v139, v142, v143\n"
"v_mfma_f32_16x16x32_bf16 v[8:11], a[232:235], v[128:131], v[8:11]\n"
"ds_read_b64_tr_b16 a[248:249], v253 offset:28928\n"
"v_mul_f32 v168, v237, v168\n"
"v_mul_f32 v169, v237, v169\n"
"v_mfma_f32_16x16x32_bf16 v[12:15], a[236:239], v[128:131], v[12:15]\n"
"ds_read_b64_tr_b16 a[252:253], v247 offset:28928\n"
"v_mul_f32 v170, v237, v170\n"
"v_mul_f32 v171, v237, v171\n"
"v_mfma_f32_16x16x32_bf16 v[28:31], a[236:239], v[160:163], v[28:31]\n"
"s_add_u32 m0, s66, 39936\n"
"s_add_u32 s62, s47, 101376\n"
"v_mul_f32 v172, v237, v172\n"
"v_mul_f32 v173, v237, v173\n"
"v_mfma_f32_16x16x32_bf16 v[24:27], a[232:235], v[160:163], v[24:27]\n"
"buffer_load_dwordx4 v252, s[76:79], s62 offen lds\n"
"v_mfma_f32_16x16x32_bf16 v[20:23], a[228:231], v[160:163], v[20:23]\n"
"v_mul_f32 v174, v237, v174\n"
"v_mul_f32 v175, v237, v175\n"
"v_cvt_pk_bf16_f32 v168, v168, v169\n"
"v_mfma_f32_16x16x32_bf16 v[16:19], a[224:227], v[160:163], v[16:19]\n"
"v_cvt_pk_bf16_f32 v169, v170, v171\n"
"v_cvt_pk_bf16_f32 v170, v172, v173\n"
"v_cvt_pk_bf16_f32 v171, v174, v175\n"
"v_mfma_f32_16x16x32_bf16 v[32:35], a[224:227], v[192:195], v[32:35]\n"
"v_mul_f32 v200, v238, v200\n"
"v_mul_f32 v201, v238, v201\n"
"v_mfma_f32_16x16x32_bf16 v[36:39], a[228:231], v[192:195], v[36:39]\n"
"s_waitcnt lgkmcnt(4)\n"
"s_barrier\n"
"v_mul_f32 v202, v238, v202\n"
"v_mfma_f32_16x16x32_bf16 v[40:43], a[232:235], v[192:195], v[40:43]\n"
"ds_read_b128 a[156:159], v225 offset:14336\n"
"v_mul_f32 v203, v238, v203\n"
"v_mfma_f32_16x16x32_bf16 v[44:47], a[236:239], v[192:195], v[44:47]\n"
"s_add_u32 m0, s66, 28672\n"
"s_add_u32 s62, s44, 110592\n"
"ds_read_b128 a[160:163], v225 offset:15360\n"
"v_mfma_f32_16x16x32_bf16 v[60:63], a[236:239], v[208:211], v[60:63]\n"
"s_waitcnt lgkmcnt(2)\n"
"buffer_load_dwordx4 v252, s[76:79], s62 offen lds\n"
"v_mfma_f32_16x16x32_bf16 v[56:59], a[232:235], v[208:211], v[56:59]\n"
"ds_read_b128 a[164:167], v225 offset:16384\n"
"v_mul_f32 v204, v238, v204\n"
"v_mfma_f32_16x16x32_bf16 v[52:55], a[228:231], v[208:211], v[52:55]\n"
"s_waitcnt vmcnt(9)\n"
"ds_read_b64_tr_b16 a[242:243], v253 offset:30720\n"
"v_mfma_f32_16x16x32_bf16 v[48:51], a[224:227], v[208:211], v[48:51]\n"
"ds_read_b64_tr_b16 a[246:247], v247 offset:30720\n"
"v_mul_f32 v205, v238, v205\n"
"v_mfma_f32_16x16x32_bf16 v[64:67], a[224:227], v[212:215], v[64:67]\n"
"ds_read_b64_tr_b16 a[250:251], v253 offset:30976\n"
"v_mul_f32 v206, v238, v206\n"
"v_mfma_f32_16x16x32_bf16 v[68:71], a[228:231], v[212:215], v[68:71]\n"
"ds_read_b64_tr_b16 a[254:255], v247 offset:30976\n"
"v_mul_f32 v207, v238, v207\n"
"v_mfma_f32_16x16x32_bf16 v[72:75], a[232:235], v[212:215], v[72:75]\n"
"s_add_u32 m0, s66, 29696\n"
"s_add_u32 s62, s44, 119808\n"
"ds_read_b128 a[168:171], v225 offset:17408\n"
"v_mfma_f32_16x16x32_bf16 v[76:79], a[236:239], v[212:215], v[76:79]\n"
"buffer_load_dwordx4 v252, s[76:79], s62 offen lds\n"
"v_mfma_f32_16x16x32_bf16 v[92:95], a[236:239], v[216:219], v[92:95]\n"
"ds_read_b128 a[172:175], v225 offset:18432\n"
"v_cvt_pk_bf16_f32 v200, v200, v201\n"
"v_mfma_f32_16x16x32_bf16 v[88:91], a[232:235], v[216:219], v[88:91]\n"
"ds_read_b128 a[176:179], v225 offset:19456\n"
"v_cvt_pk_bf16_f32 v201, v202, v203\n"
"v_mfma_f32_16x16x32_bf16 v[84:87], a[228:231], v[216:219], v[84:87]\n"
"ds_read_b128 a[180:183], v225 offset:20480\n"
"v_cvt_pk_bf16_f32 v202, v204, v205\n"
"v_mfma_f32_16x16x32_bf16 v[80:83], a[224:227], v[216:219], v[80:83]\n"
"ds_write_b128 v224, v[136:139] offset:21504\n"
"v_mfma_f32_16x16x32_bf16 v[96:99], a[224:227], v[220:223], v[96:99]\n"
"ds_write_b128 v224, v[168:171] offset:22528\n"
"v_mfma_f32_16x16x32_bf16 v[100:103], a[228:231], v[220:223], v[100:103]\n"
"s_add_u32 m0, s66, 30720\n"
"s_add_u32 s62, s44, 129024\n"
"v_cvt_pk_bf16_f32 v203, v206, v207\n"
"v_mfma_f32_16x16x32_bf16 v[104:107], a[232:235], v[220:223], v[104:107]\n"
"s_waitcnt lgkmcnt(6)\n"
"buffer_load_dwordx4 v252, s[76:79], s62 offen lds\n"
"v_mfma_f32_16x16x32_bf16 v[108:111], a[236:239], v[220:223], v[108:111]\n"
"; oproj step 9\n"
"ds_write_b128 v224, v[200:203] offset:23552\n"
"v_mfma_f32_16x16x32_bf16 v[0:3], a[240:243], a[156:159], v[0:3]\n"
"s_waitcnt vmcnt(9)\n"
"ds_read_b64_tr_b16 a[224:225], v253 offset:32768\n"
"v_mfma_f32_16x16x32_bf16 v[4:7], a[244:247], a[156:159], v[4:7]\n"
"ds_read_b64_tr_b16 a[228:229], v247 offset:32768\n"
"v_accvgpr_read_b32 v208, a24\n"
"v_mfma_f32_16x16x32_bf16 v[8:11], a[248:251], a[156:159], v[8:11]\n"
"ds_read_b64_tr_b16 a[232:233], v253 offset:33024\n"
"v_accvgpr_read_b32 v209, a25\n"
"v_mfma_f32_16x16x32_bf16 v[12:15], a[252:255], a[156:159], v[12:15]\n"
"ds_read_b64_tr_b16 a[236:237], v247 offset:33024\n"
"v_accvgpr_read_b32 v210, a26\n"
"v_mfma_f32_16x16x32_bf16 v[28:31], a[252:255], a[160:163], v[28:31]\n"
"s_add_u32 m0, s66, 31744\n"
"s_add_u32 s62, s44, 138240\n"
"ds_read_b128 a[128:131], v226 offset:14336\n"
"v_mfma_f32_16x16x32_bf16 v[24:27], a[248:251], a[160:163], v[24:27]\n"
"buffer_load_dwordx4 v252, s[76:79], s62 offen lds\n"
"v_mfma_f32_16x16x32_bf16 v[20:23], a[244:247], a[160:163], v[20:23]\n"
"s_waitcnt lgkmcnt(8)\n"
"ds_read_b128 a[132:135], v226 offset:15360\n"
"s_nop 0\n"
"Lh9:\n"
"s_cbranch_execz Lw2\n"
"v_mfma_f32_16x16x32_bf16 v[16:19], a[240:243], a[160:163], v[16:19]\n"
"ds_read_b128 a[136:139], v226 offset:16384\n"
"v_accvgpr_read_b32 v211, a27\n"
"v_mfma_f32_16x16x32_bf16 v[32:35], a[240:243], a[164:167], v[32:35]\n"
"ds_read_b128 a[140:143], v226 offset:17408\n"
"v_accvgpr_read_b32 v232, a28\n"
"v_mfma_f32_16x16x32_bf16 v[36:39], a[244:247], a[164:167], v[36:39]\n"
"ds_read_b128 a[144:147], v226 offset:18432\n"
"v_accvgpr_read_b32 v233, a29\n"
"v_mfma_f32_16x16x32_bf16 v[40:43], a[248:251], a[164:167], v[40:43]\n"
"v_accvgpr_read_b32 v234, a30\n"
"v_accvgpr_read_b32 v235, a31\n"
"v_mfma_f32_16x16x32_bf16 v[44:47], a[252:255], a[164:167], v[44:47]\n"
"s_add_u32 m0, s66, 32768\n"
"s_add_u32 s62, s45, 110592\n"
"v_mul_f32 v208, v239, v208\n"
"v_mfma_f32_16x16x32_bf16 v[60:63], a[252:255], a[168:171], v[60:63]\n"
"s_waitcnt lgkmcnt(5)\n"
"buffer_load_dwordx4 v252, s[76:79], s62 offen lds\n"
"v_mfma_f32_16x16x32_bf16 v[56:59], a[248:251], a[168:171], v[56:59]\n"
"v_mul_f32 v209, v239, v209\n"
"v_mul_f32 v210, v239, v210\n"
"v_mfma_f32_16x16x32_bf16 v[52:55], a[244:247], a[168:171], v[52:55]\n"
"s_waitcnt vmcnt(9)\n"
"ds_read_b64_tr_b16 a[226:227], v253 offset:34816\n"
"v_mfma_f32_16x16x32_bf16 v[48:51], a[240:243], a[168:171], v[48:51]\n"
"ds_read_b64_tr_b16 a[230:231], v247 offset:34816\n"
"v_mul_f32 v211, v239, v211\n"
"v_mfma_f32_16x16x32_bf16 v[64:67], a[240:243], a[172:175], v[64:67]\n"
"ds_read_b64_tr_b16 a[234:235], v253 offset:35072\n"
"v_mul_f32 v232, v239, v232\n"
"v_mfma_f32_16x16x32_bf16 v[68:71], a[244:247], a[172:175], v[68:71]\n"
"ds_read_b64_tr_b16 a[238:239], v247 offset:35072\n"
"v_mul_f32 v233, v239, v233\n"
"v_mfma_f32_16x16x32_bf16 v[72:75], a[248:251], a[172:175], v[72:75]\n"
"s_add_u32 m0, s66, 33792\n"
"s_add_u32 s62, s45, 119808\n"
"ds_read_b128 a[148:151], v226 offset:19456\n"
"v_mfma_f32_16x16x32_bf16 v[76:79], a[252:255], a[172:175], v[76:79]\n"
"buffer_load_dwordx4 v252, s[76:79], s62 offen lds\n"
"v_mfma_f32_16x16x32_bf16 v[92:95], a[252:255], a[176:179], v[92:95]\n"
"ds_read_b128 a[152:155], v226 offset:20480\n"
"v_mul_f32 v234, v239, v234\n"
"v_mfma_f32_16x16x32_bf16 v[88:91], a[248:251], a[176:179], v[88:91]\n"
"v_mul_f32 v235, v239, v235\n"
"v_cvt_pk_bf16_f32 v208, v208, v209\n"
"v_mfma_f32_16x16x32_bf16 v[84:87], a[244:247], a[176:179], v[84:87]\n"
"v_cvt_pk_bf16_f32 v209, v210, v211\n"
"v_cvt_pk_bf16_f32 v210, v232, v233\n"
"v_mfma_f32_16x16x32_bf16 v[80:83], a[240:243], a[176:179], v[80:83]\n"
"v_cvt_pk_bf16_f32 v211, v234, v235\n"
"v_accvgpr_read_b32 v212, a56\n"
"v_accvgpr_read_b32 v213, a57\n"
"v_mfma_f32_16x16x32_bf16 v[96:99], a[240:243], a[180:183], v[96:99]\n"
"ds_write_b128 v224, v[208:211] offset:24576\n"
"v_mfma_f32_16x16x32_bf16 v[100:103], a[244:247], a[180:183], v[100:103]\n"
"s_add_u32 m0, s66, 34816\n"
"s_add_u32 s62, s45, 129024\n"
"v_accvgpr_read_b32 v214, a58\n"
"v_accvgpr_read_b32 v215, a59\n"
"v_mfma_f32_16x16x32_bf16 v[104:107], a[248:251], a[180:183], v[104:107]\n"
"s_waitcnt lgkmcnt(3)\n"
"buffer_load_dwordx4 v252, s[76:79], s62 offen lds\n"
"v_mfma_f32_16x16x32_bf16 v[108:111], a[252:255], a[180:183], v[108:111]\n"
"; oproj step 10\n"
"s_waitcnt lgkmcnt(1)\n"
"v_accvgpr_read_b32 v228, a60\n"
"v_accvgpr_read_b32 v229, a61\n"
"v_accvgpr_read_b32 v230, a62\n"
"v_mfma_f32_16x16x32_bf16 v[0:3], a[224:227], a[128:131], v[0:3]\n"
"s_waitcnt vmcnt(9)\n"
"ds_read_b64_tr_b16 a[240:241], v253 offset:36864\n"
"v_accvgpr_read_b32 v231, a63\n"
"v_mfma_f32_16x16x32_bf16 v[4:7], a[228:231], a[128:131], v[4:7]\n"
"ds_read_b64_tr_b16 a[244:245], v247 offset:36864\n"
"v_mul_f32 v212, v240, v212\n"
"v_mul_f32 v213, v240, v213\n"
"v_mfma_f32_16x16x32_bf16 v[8:11], a[232:235], a[128:131], v[8:11]\n"
"ds_read_b64_tr_b16 a[248:249], v253 offset:37120\n"
"v_mul_f32 v214, v240, v214\n"
"v_mul_f32 v215, v240, v215\n"
"v_mfma_f32_16x16x32_bf16 v[12:15], a[236:239], a[128:131], v[12:15]\n"
"ds_read_b64_tr_b16 a[252:253], v247 offset:37120\n"
"v_mul_f32 v228, v240, v228\n"
"v_mul_f32 v229, v240, v229\n"
"v_mfma_f32_16x16x32_bf16 v[28:31], a[236:239], a[132:135], v[28:31]\n"
"s_add_u32 m0, s66, 35840\n"
"s_add_u32 s62, s45, 138240\n"
"ds_read_b128 a[156:159], v227 offset:14336\n"
"v_mfma_f32_16x16x32_bf16 v[24:27], a[232:235], a[132:135], v[24:27]\n"
"buffer_load_dwordx4 v252, s[76:79], s62 offen lds\n"
"v_mfma_f32_16x16x32_bf16 v[20:23], a[228:231], a[132:135], v[20:23]\n"
"ds_read_b128 a[160:163], v227 offset:15360\n"
"v_mul_f32 v230, v240, v230\n"
"v_mul_f32 v231, v240, v231\n"
"v_mfma_f32_16x16x32_bf16 v[16:19], a[224:227], a[132:135], v[16:19]\n"
"ds_read_b128 a[164:167], v227 offset:16384\n"
"v_cvt_pk_bf16_f32 v212, v212, v213\n"
"v_cvt_pk_bf16_f32 v213, v214, v215\n"
"v_mfma_f32_16x16x32_bf16 v[32:35], a[224:227], a[136:139], v[32:35]\n"
"ds_read_b128 a[168:171], v227 offset:17408\n"
"v_cvt_pk_bf16_f32 v214, v228, v229\n"
"v_cvt_pk_bf16_f32 v215, v230, v231\n"
"v_mfma_f32_16x16x32_bf16 v[36:39], a[228:231], a[136:139], v[36:39]\n"
"ds_read_b128 a[172:175], v227 offset:18432\n"
"v_accvgpr_read_b32 v216, a88\n"
"v_mfma_f32_16x16x32_bf16 v[40:43], a[232:235], a[136:139], v[40:43]\n"
"ds_write_b128 v224, v[212:215] offset:25600\n"
"v_mfma_f32_16x16x32_bf16 v[44:47], a[236:239], a[136:139], v[44:47]\n"
"s_add_u32 m0, s66, 36864\n"
"s_add_u32 s62, s46, 110592\n"
"v_accvgpr_read_b32 v217, a89\n"
"v_mfma_f32_16x16x32_bf16 v[60:63], a[236:239], a[140:143], v[60:63]\n"
"s_waitcnt lgkmcnt(6)\n"
"buffer_load_dwordx4 v252, s[76:79], s62 offen lds\n"
"v_mfma_f32_16x16x32_bf16 v[56:59], a[232:235], a[140:143], v[56:59]\n"
"v_accvgpr_read_b32 v218, a90\n"
"v_accvgpr_read_b32 v219, a91\n"
"v_mfma_f32_16x16x32_bf16 v[52:55], a[228:231], a[140:143], v[52:55]\n"
"s_waitcnt vmcnt(9)\n"
"ds_read_b64_tr_b16 a[242:243], v253 offset:38912\n"
"v_mfma_f32_16x16x32_bf16 v[48:51], a[224:227], a[140:143], v[48:51]\n"
"ds_read_b64_tr_b16 a[246:247], v247 offset:38912\n"
"v_accvgpr_read_b32 v232, a92\n"
"v_mfma_f32_16x16x32_bf16 v[64:67], a[224:227], a[144:147], v[64:67]\n"
"ds_read_b64_tr_b16 a[250:251], v253 offset:39168\n"
"v_accvgpr_read_b32 v233, a93\n"
"v_mfma_f32_16x16x32_bf16 v[68:71], a[228:231], a[144:147], v[68:71]\n"
"ds_read_b64_tr_b16 a[254:255], v247 offset:39168\n"
"v_accvgpr_read_b32 v234, a94\n"
"v_mfma_f32_16x16x32_bf16 v[72:75], a[232:235], a[144:147], v[72:75]\n"
"s_add_u32 m0, s66, 37888\n"
"s_add_u32 s62, s46, 119808\n"
"ds_read_b128 a[176:179], v227 offset:19456\n"
"v_mfma_f32_16x16x32_bf16 v[76:79], a[236:239], a[144:147], v[76:79]\n"
"buffer_load_dwordx4 v252, s[76:79], s62 offen lds\n"
"v_mfma_f32_16x16x32_bf16 v[92:95], a[236:239], a[148:151], v[92:95]\n"
"ds_read_b128 a[180:183], v227 offset:20480\n"
"v_accvgpr_read_b32 v235, a95\n"
"v_mfma_f32_16x16x32_bf16 v[88:91], a[232:235], a[148:151], v[88:91]\n"
"v_mul_f32 v216, v241, v216\n"
"v_mul_f32 v217, v241, v217\n"
"v_mfma_f32_16x16x32_bf16 v[84:87], a[228:231], a[148:151], v[84:87]\n"
"v_mul_f32 v218, v241, v218\n"
"v_mul_f32 v219, v241, v219\n"
"v_mfma_f32_16x16x32_bf16 v[80:83], a[224:227], a[148:151], v[80:83]\n"
"v_mul_f32 v232, v241, v232\n"
"v_mul_f32 v233, v241, v233\n"
"v_mfma_f32_16x16x32_bf16 v[96:99], a[224:227], a[152:155], v[96:99]\n"
"v_mul_f32 v234, v241, v234\n"
"v_mul_f32 v235, v241, v235\n"
"v_mfma_f32_16x16x32_bf16 v[100:103], a[228:231], a[152:155], v[100:103]\n"
"s_add_u32 m0, s66, 38912\n"
"s_add_u32 s62, s46, 129024\n"
"v_cvt_pk_bf16_f32 v216, v216, v217\n"
"v_mfma_f32_16x16x32_bf16 v[104:107], a[232:235], a[152:155], v[104:107]\n"
"s_waitcnt lgkmcnt(2)\n"
"buffer_load_dwordx4 v252, s[76:79], s62 offen lds\n"
"v_mfma_f32_16x16x32_bf16 v[108:111], a[236:239], a[152:155], v[108:111]\n"
"; oproj step 11\n"
"s_waitcnt lgkmcnt(0)\n"
"v_cvt_pk_bf16_f32 v217, v218, v219\n"
"v_cvt_pk_bf16_f32 v218, v232, v233\n"
"v_mfma_f32_16x16x32_bf16 v[0:3], a[240:243], a[156:159], v[0:3]\n"
"s_waitcnt vmcnt(9)\n"
"ds_read_b64_tr_b16 a[224:225], v253 offset:28672\n"
"v_mfma_f32_16x16x32_bf16 v[4:7], a[244:247], a[156:159], v[4:7]\n"
"ds_read_b64_tr_b16 a[228:229], v247 offset:28672\n"
"v_cvt_pk_bf16_f32 v219, v234, v235\n"
"v_mfma_f32_16x16x32_bf16 v[8:11], a[248:251], a[156:159], v[8:11]\n"
"ds_read_b64_tr_b16 a[232:233], v253 offset:28928\n"
"v_accvgpr_read_b32 v220, a120\n"
"v_mfma_f32_16x16x32_bf16 v[12:15], a[252:255], a[156:159], v[12:15]\n"
"ds_read_b64_tr_b16 a[236:237], v247 offset:28928\n"
"v_accvgpr_read_b32 v221, a121\n"
"v_mfma_f32_16x16x32_bf16 v[28:31], a[252:255], a[160:163], v[28:31]\n"
"s_add_u32 m0, s66, 39936\n"
"s_add_u32 s62, s46, 138240\n"
"ds_write_b128 v224, v[216:219] offset:26624\n"
"v_mfma_f32_16x16x32_bf16 v[24:27], a[248:251], a[160:163], v[24:27]\n"
"buffer_load_dwordx4 v252, s[76:79], s62 offen lds\n"
"v_mfma_f32_16x16x32_bf16 v[20:23], a[244:247], a[160:163], v[20:23]\n"
"v_accvgpr_read_b32 v222, a122\n"
"v_accvgpr_read_b32 v223, a123\n"
"v_mfma_f32_16x16x32_bf16 v[16:19], a[240:243], a[160:163], v[16:19]\n"
"v_accvgpr_read_b32 v228, a124\n"
"v_accvgpr_read_b32 v229, a125\n"
"v_mfma_f32_16x16x32_bf16 v[32:35], a[240:243], a[164:167], v[32:35]\n"
"v_accvgpr_read_b32 v230, a126\n"
"v_accvgpr_read_b32 v231, a127\n"
"v_mfma_f32_16x16x32_bf16 v[36:39], a[244:247], a[164:167], v[36:39]\n"
"v_mul_f32 v220, v242, v220\n"
"v_mul_f32 v221, v242, v221\n"
"v_mfma_f32_16x16x32_bf16 v[40:43], a[248:251], a[164:167], v[40:43]\n"
"v_mul_f32 v222, v242, v222\n"
"v_mul_f32 v223, v242, v223\n"
"v_mfma_f32_16x16x32_bf16 v[44:47], a[252:255], a[164:167], v[44:47]\n"
"s_add_u32 m0, s66, 28672\n"
"s_add_u32 s62, s47, 110592\n"
"v_mul_f32 v228, v242, v228\n"
"v_mfma_f32_16x16x32_bf16 v[60:63], a[252:255], a[168:171], v[60:63]\n"
"s_waitcnt lgkmcnt(1)\n"
"buffer_load_dwordx4 v252, s[76:79], s62 offen lds\n"
"v_mfma_f32_16x16x32_bf16 v[56:59], a[248:251], a[168:171], v[56:59]\n"
"v_mul_f32 v229, v242, v229\n"
"v_mul_f32 v230, v242, v230\n"
"v_mfma_f32_16x16x32_bf16 v[52:55], a[244:247], a[168:171], v[52:55]\n"
"s_waitcnt vmcnt(9)\n"
"ds_read_b64_tr_b16 a[226:227], v253 offset:30720\n"
"v_mfma_f32_16x16x32_bf16 v[48:51], a[240:243], a[168:171], v[48:51]\n"
"ds_read_b64_tr_b16 a[230:231], v247 offset:30720\n"
"v_mul_f32 v231, v242, v231\n"
"v_mfma_f32_16x16x32_bf16 v[64:67], a[240:243], a[172:175], v[64:67]\n"
"ds_read_b64_tr_b16 a[234:235], v253 offset:30976\n"
"v_cvt_pk_bf16_f32 v220, v220, v221\n"
"v_mfma_f32_16x16x32_bf16 v[68:71], a[244:247], a[172:175], v[68:71]\n"
"ds_read_b64_tr_b16 a[238:239], v247 offset:30976\n"
"v_cvt_pk_bf16_f32 v221, v222, v223\n"
"v_mfma_f32_16x16x32_bf16 v[72:75], a[248:251], a[172:175], v[72:75]\n"
"s_add_u32 m0, s66, 29696\n"
"s_add_u32 s62, s47, 119808\n"
"v_cvt_pk_bf16_f32 v222, v228, v229\n"
"v_mfma_f32_16x16x32_bf16 v[76:79], a[252:255], a[172:175], v[76:79]\n"
"buffer_load_dwordx4 v252, s[76:79], s62 offen lds\n"
"v_mfma_f32_16x16x32_bf16 v[92:95], a[252:255], a[176:179], v[92:95]\n"
"v_cvt_pk_bf16_f32 v223, v230, v231\n"
"v_mfma_f32_16x16x32_bf16 v[88:91], a[248:251], a[176:179], v[88:91]\n"
"ds_write_b128 v224, v[220:223] offset:27648\n"
"v_mfma_f32_16x16x32_bf16 v[84:87], a[244:247], a[176:179], v[84:87]\n"
"v_mfma_f32_16x16x32_bf16 v[80:83], a[240:243], a[176:179], v[80:83]\n"
"v_mfma_f32_16x16x32_bf16 v[96:99], a[240:243], a[180:183], v[96:99]\n"
"v_mfma_f32_16x16x32_bf16 v[100:103], a[244:247], a[180:183], v[100:103]\n"
"s_add_u32 m0, s66, 30720\n"
"s_add_u32 s62, s47, 129024\n"
"v_mfma_f32_16x16x32_bf16 v[104:107], a[248:251], a[180:183], v[104:107]\n"
"s_waitcnt lgkmcnt(1)\n"
"buffer_load_dwordx4 v252, s[76:79], s62 offen lds\n"
"v_mfma_f32_16x16x32_bf16 v[108:111], a[252:255], a[180:183], v[108:111]\n"
"; oproj step 12\n"
"v_mfma_f32_16x16x32_bf16 v[0:3], a[224:227], v[136:139], v[0:3]\n"
"s_waitcnt vmcnt(9)\n"
"ds_read_b64_tr_b16 a[240:241], v253 offset:32768\n"
"v_mfma_f32_16x16x32_bf16 v[4:7], a[228:231], v[136:139], v[4:7]\n"
"ds_read_b64_tr_b16 a[244:245], v247 offset:32768\n"
"v_mfma_f32_16x16x32_bf16 v[8:11], a[232:235], v[136:139], v[8:11]\n"
"ds_read_b64_tr_b16 a[248:249], v253 offset:33024\n"
"v_mfma_f32_16x16x32_bf16 v[12:15], a[236:239], v[136:139], v[12:15]\n"
"ds_read_b64_tr_b16 a[252:253], v247 offset:33024\n"
"v_mfma_f32_16x16x32_bf16 v[28:31], a[236:239], v[168:171], v[28:31]\n"
"s_add_u32 m0, s66, 31744\n"
"s_add_u32 s62, s47, 138240\n"
"v_mfma_f32_16x16x32_bf16 v[24:27], a[232:235], v[168:171], v[24:27]\n"
"buffer_load_dwordx4 v252, s[76:79], s62 offen lds\n"
"v_mfma_f32_16x16x32_bf16 v[20:23], a[228:231], v[168:171], v[20:23]\n"
"v_mfma_f32_16x16x32_bf16 v[16:19], a[224:227], v[168:171], v[16:19]\n"
"v_mfma_f32_16x16x32_bf16 v[32:35], a[224:227], v[200:203], v[32:35]\n"
"v_mfma_f32_16x16x32_bf16 v[36:39], a[228:231], v[200:203], v[36:39]\n"
"s_waitcnt lgkmcnt(4)\n"
"s_barrier\n"
"v_mfma_f32_16x16x32_bf16 v[40:43], a[232:235], v[200:203], v[40:43]\n"
"ds_read_b128 a[156:159], v225 offset:21504\n"
"v_lshrrev_b32 v249, 2, %[tA]\n"
"v_mfma_f32_16x16x32_bf16 v[44:47], a[236:239], v[200:203], v[44:47]\n"
"s_add_u32 m0, s66, 32768\n"
"s_add_u32 s62, s40, 0\n"
"ds_read_b128 a[160:163], v225 offset:22528\n"
"v_mfma_f32_16x16x32_bf16 v[60:63], a[236:239], v[208:211], v[60:63]\n"
"s_waitcnt lgkmcnt(2)\n"
"buffer_load_dwordx4 v252, s[76:79], s62 offen lds\n"
"v_mfma_f32_16x16x32_bf16 v[56:59], a[232:235], v[208:211], v[56:59]\n"
"ds_read_b128 a[164:167], v225 offset:23552\n"
"v_and_b32 v249, 28, v249\n"
"v_mfma_f32_16x16x32_bf16 v[52:55], a[228:231], v[208:211], v[52:55]\n"
"s_waitcnt vmcnt(9)\n"
"ds_read_b64_tr_b16 a[242:243], v253 offset:34816\n"
"v_mfma_f32_16x16x32_bf16 v[48:51], a[224:227], v[208:211], v[48:51]\n"
"ds_read_b64_tr_b16 a[246:247], v247 offset:34816\n"
"v_lshrrev_b32 v244, 1, %[tA]\n"
"v_mfma_f32_16x16x32_bf16 v[64:67], a[224:227], v[212:215], v[64:67]\n"
"ds_read_b64_tr_b16 a[250:251], v253 offset:35072\n"
"v_bfe_u32 v251, %[tA], 4, 4\n"
"v_mfma_f32_16x16x32_bf16 v[68:71], a[228:231], v[212:215], v[68:71]\n"
"ds_read_b64_tr_b16 a[254:255], v247 offset:35072\n"
"v_mul_u32_u24 v251, 2304, v251\n"
"v_mfma_f32_16x16x32_bf16 v[72:75], a[232:235], v[212:215], v[72:75]\n"
"s_add_u32 m0, s66, 33792\n"
"s_add_u32 s62, s40, 9216\n"
"ds_read_b128 a[168:171], v225 offset:24576\n"
"v_mfma_f32_16x16x32_bf16 v[76:79], a[236:239], v[212:215], v[76:79]\n"
"buffer_load_dwordx4 v252, s[76:79], s62 offen lds\n"
"v_mfma_f32_16x16x32_bf16 v[92:95], a[236:239], v[216:219], v[92:95]\n"
"ds_read_b128 a[172:175], v225 offset:25600\n"
"v_lshrrev_b32 v250, 8, %[tA]\n"
"v_mfma_f32_16x16x32_bf16 v[88:91], a[232:235], v[216:219], v[88:91]\n"
"ds_read_b128 a[176:179], v225 offset:26624\n"
"v_lshl_add_u32 v251, v250, 3, v251\n"
"v_mfma_f32_16x16x32_bf16 v[84:87], a[228:231], v[216:219], v[84:87]\n"
"ds_read_b128 a[180:183], v225 offset:27648\n"
"v_lshrrev_b32 v245, s9, %[tA]\n"
"v_mfma_f32_16x16x32_bf16 v[80:83], a[224:227], v[216:219], v[80:83]\n"
"s_mov_b64 s[80:81], s[20:21]\n"
"s_mov_b32 s82, s8\n"
"s_mov_b32 s65, 0xffff0000\n"
"v_mfma_f32_16x16x32_bf16 v[96:99], a[224:227], v[220:223], v[96:99]\n"
"s_add_u32 s63, s36, 1\n"
"s_and_b32 s63, s63, 3\n"
"s_mul_i32 s91, s63, 3584\n"
"v_mfma_f32_16x16x32_bf16 v[100:103], a[228:231], v[220:223], v[100:103]\n"
"s_add_u32 m0, s66, 34816\n"
"s_add_u32 s62, s40, 18432\n"
"s_add_u32 s91, s91, s52\n"
"v_mfma_f32_16x16x32_bf16 v[104:107], a[232:235], v[220:223], v[104:107]\n"
"s_waitcnt lgkmcnt(4)\n"
"buffer_load_dwordx4 v252, s[76:79], s62 offen lds\n"
"v_mfma_f32_16x16x32_bf16 v[108:111], a[236:239], v[220:223], v[108:111]\n"
"; oproj step 13\n"
"s_add_u32 s91, s91, 14336\n"
"s_and_b32 s74, s63, 1\n"
"v_mfma_f32_16x16x32_bf16 v[0:3], a[240:243], a[156:159], v[0:3]\n"
"s_waitcnt vmcnt(9)\n"
"s_lshl_b32 s74, s74, 5\n"
"ds_read_b64_tr_b16 a[224:225], v253 offset:36864\n"
"v_mfma_f32_16x16x32_bf16 v[4:7], a[244:247], a[156:159], v[4:7]\n"
"ds_read_b64_tr_b16 a[228:229], v247 offset:36864\n"
"v_xor_b32 v228, s74, v253\n"
"v_mfma_f32_16x16x32_bf16 v[8:11], a[248:251], a[156:159], v[8:11]\n"
"s_lshr_b32 s63, s63, 1\n"
"s_lshl_b32 s63, s63, 8\n"
"ds_read_b64_tr_b16 a[232:233], v253 offset:37120\n"
"v_mfma_f32_16x16x32_bf16 v[12:15], a[252:255], a[156:159], v[12:15]\n"
"ds_read_b64_tr_b16 a[236:237], v247 offset:37120\n"
"v_add_u32 v228, s63, v228\n"
"v_mfma_f32_16x16x32_bf16 v[28:31], a[252:255], a[160:163], v[28:31]\n"
"s_add_u32 m0, s66, 35840\n"
"s_add_u32 s62, s40, 27648\n"
"ds_read_b128 a[128:131], v226 offset:21504\n"
"v_mfma_f32_16x16x32_bf16 v[24:27], a[248:251], a[160:163], v[24:27]\n"
"buffer_load_dwordx4 v252, s[76:79], s62 offen lds\n"
"s_add_u32 s63, s36, 2\n"
"v_mfma_f32_16x16x32_bf16 v[20:23], a[244:247], a[160:163], v[20:23]\n"
"s_waitcnt lgkmcnt(5)\n"
"s_and_b32 s63, s63, 3\n"
"ds_read_b128 a[132:135], v226 offset:22528\n"
"v_mfma_f32_16x16x32_bf16 v[16:19], a[240:243], a[160:163], v[16:19]\n"
"s_mul_i32 s92, s63, 3584\n"
"s_add_u32 s92, s92, s52\n"
"ds_read_b128 a[136:139], v226 offset:23552\n"
"v_mfma_f32_16x16x32_bf16 v[32:35], a[240:243], a[164:167], v[32:35]\n"
"s_add_u32 s92, s92, 14336\n"
"s_and_b32 s74, s63, 1\n"
"ds_read_b128 a[140:143], v226 offset:24576\n"
"v_mfma_f32_16x16x32_bf16 v[36:39], a[244:247], a[164:167], v[36:39]\n"
"s_lshl_b32 s74, s74, 5\n"
"ds_read_b128 a[144:147], v226 offset:25600\n"
"v_mfma_f32_16x16x32_bf16 v[40:43], a[248:251], a[164:167], v[40:43]\n"
"v_xor_b32 v229, s74, v253\n"
"s_lshr_b32 s63, s63, 1\n"
"v_mfma_f32_16x16x32_bf16 v[44:47], a[252:255], a[164:167], v[44:47]\n"
"s_add_u32 m0, s66, 36864\n"
"s_add_u32 s62, s41, 0\n"
"s_lshl_b32 s63, s63, 8\n"
"v_mfma_f32_16x16x32_bf16 v[60:63], a[252:255], a[168:171], v[60:63]\n"
"s_waitcnt lgkmcnt(5)\n"
"buffer_load_dwordx4 v252, s[76:79], s62 offen lds\n"
"v_mfma_f32_16x16x32_bf16 v[56:59], a[248:251], a[168:171], v[56:59]\n"
"v_add_u32 v229, s63, v229\n"
"s_add_u32 s63, s36, 3\n"
"v_mfma_f32_16x16x32_bf16 v[52:55], a[244:247], a[168:171], v[52:55]\n"
"s_waitcnt vmcnt(9)\n"
"s_and_b32 s63, s63, 3\n"
"ds_read_b64_tr_b16 a[226:227], v253 offset:38912\n"
"v_mfma_f32_16x16x32_bf16 v[48:51], a[240:243], a[168:171], v[48:51]\n"
"s_mul_i32 s93, s63, 3584\n"
"s_add_u32 s93, s93, s52\n"
"ds_read_b64_tr_b16 a[230:231], v247 offset:38912\n"
"v_mfma_f32_16x16x32_bf16 v[64:67], a[240:243], a[172:175], v[64:67]\n"
"s_add_u32 s93, s93, 14336\n"
"s_and_b32 s74, s63, 1\n"
"ds_read_b64_tr_b16 a[234:235], v253 offset:39168\n"
"v_mfma_f32_16x16x32_bf16 v[68:71], a[244:247], a[172:175], v[68:71]\n"
"s_lshl_b32 s74, s74, 5\n"
"ds_read_b64_tr_b16 a[238:239], v247 offset:39168\n"
"v_mfma_f32_16x16x32_bf16 v[72:75], a[248:251], a[172:175], v[72:75]\n"
"s_add_u32 m0, s66, 37888\n"
"s_add_u32 s62, s41, 9216\n"
"ds_read_b128 a[148:151], v226 offset:26624\n"
"v_mfma_f32_16x16x32_bf16 v[76:79], a[252:255], a[172:175], v[76:79]\n"
"buffer_load_dwordx4 v252, s[76:79], s62 offen lds\n"
"v_mfma_f32_16x16x32_bf16 v[92:95], a[252:255], a[176:179], v[92:95]\n"
"ds_read_b128 a[152:155], v226 offset:27648\n"
"v_xor_b32 v230, s74, v253\n"
"v_mfma_f32_16x16x32_bf16 v[88:91], a[248:251], a[176:179], v[88:91]\n"
"s_lshr_b32 s63, s63, 1\n"
"s_lshl_b32 s63, s63, 8\n"
"v_add_u32 v230, s63, v230\n"
"v_mfma_f32_16x16x32_bf16 v[84:87], a[244:247], a[176:179], v[84:87]\n"
"s_add_u32 s63, s36, 4\n"
"s_and_b32 s63, s63, 3\n"
"s_mul_i32 s86, s63, 3584\n"
"v_mfma_f32_16x16x32_bf16 v[80:83], a[240:243], a[176:179], v[80:83]\n"
"s_add_u32 s86, s86, s52\n"
"s_add_u32 s86, s86, 14336\n"
"s_and_b32 s74, s63, 1\n"
"v_mfma_f32_16x16x32_bf16 v[96:99], a[240:243], a[180:183], v[96:99]\n"
"s_lshl_b32 s74, s74, 5\n"
"v_xor_b32 v231, s74, v253\n"
"s_lshr_b32 s63, s63, 1\n"
"v_mfma_f32_16x16x32_bf16 v[100:103], a[244:247], a[180:183], v[100:103]\n"
"s_add_u32 m0, s66, 38912\n"
"s_add_u32 s62, s41, 18432\n"
"s_lshl_b32 s63, s63, 8\n"
"v_mfma_f32_16x16x32_bf16 v[104:107], a[248:251], a[180:183], v[104:107]\n"
"s_waitcnt lgkmcnt(2)\n"
"buffer_load_dwordx4 v252, s[76:79], s62 offen lds\n"
"v_mfma_f32_16x16x32_bf16 v[108:111], a[252:255], a[180:183], v[108:111]\n"
"; oproj step 14\n"
"s_waitcnt lgkmcnt(0)\n"
"v_add_u32 v231, s63, v231\n"
"s_add_u32 s94, s52, 0\n"
"v_mfma_f32_16x16x32_bf16 v[0:3], a[224:227], a[128:131], v[0:3]\n"
"s_waitcnt vmcnt(9)\n"
"s_add_u32 s95, s52, 3584\n"
"ds_read_b64_tr_b16 a[240:241], v253 offset:28672\n"
"v_mfma_f32_16x16x32_bf16 v[4:7], a[228:231], a[128:131], v[4:7]\n"
"s_add_u32 s88, s52, 7168\n"
"s_add_u32 s90, s52, 10752\n"
"ds_read_b64_tr_b16 a[244:245], v247 offset:28672\n"
"v_mfma_f32_16x16x32_bf16 v[8:11], a[232:235], a[128:131], v[8:11]\n"
"ds_read_b64_tr_b16 a[248:249], v253 offset:28928\n"
"v_mfma_f32_16x16x32_bf16 v[12:15], a[236:239], a[128:131], v[12:15]\n"
"ds_read_b64_tr_b16 a[252:253], v247 offset:28928\n"
"v_mfma_f32_16x16x32_bf16 v[28:31], a[236:239], a[132:135], v[28:31]\n"
"s_add_u32 m0, s66, 39936\n"
"s_add_u32 s62, s41, 27648\n"
"ds_read_b128 a[156:159], v227 offset:21504\n"
"v_mfma_f32_16x16x32_bf16 v[24:27], a[232:235], a[132:135], v[24:27]\n"
"buffer_load_dwordx4 v252, s[76:79], s62 offen lds\n"
"v_mfma_f32_16x16x32_bf16 v[20:23], a[228:231], a[132:135], v[20:23]\n"
"ds_read_b128 a[160:163], v227 offset:22528\n"
"v_mfma_f32_16x16x32_bf16 v[16:19], a[224:227], a[132:135], v[16:19]\n"
"ds_read_b128 a[164:167], v227 offset:23552\n"
"v_mfma_f32_16x16x32_bf16 v[32:35], a[224:227], a[136:139], v[32:35]\n"
"ds_read_b128 a[168:171], v227 offset:24576\n"
"v_mfma_f32_16x16x32_bf16 v[36:39], a[228:231], a[136:139], v[36:39]\n"
"ds_read_b128 a[172:175], v227 offset:25600\n"
"v_mfma_f32_16x16x32_bf16 v[40:43], a[232:235], a[136:139], v[40:43]\n"
"v_mfma_f32_16x16x32_bf16 v[44:47], a[236:239], a[136:139], v[44:47]\n"
"s_add_u32 m0, s66, 28672\n"
"s_add_u32 s62, s42, 0\n"
"v_mfma_f32_16x16x32_bf16 v[60:63], a[236:239], a[140:143], v[60:63]\n"
"s_waitcnt lgkmcnt(5)\n"
"buffer_load_dwordx4 v252, s[76:79], s62 offen lds\n"
"v_mfma_f32_16x16x32_bf16 v[56:59], a[232:235], a[140:143], v[56:59]\n"
"v_mfma_f32_16x16x32_bf16 v[52:55], a[228:231], a[140:143], v[52:55]\n"
"s_waitcnt vmcnt(9)\n"
"ds_read_b64_tr_b16 a[242:243], v253 offset:30720\n"
"v_mfma_f32_16x16x32_bf16 v[48:51], a[224:227], a[140:143], v[48:51]\n"
"ds_read_b64_tr_b16 a[246:247], v247 offset:30720\n"
"v_mfma_f32_16x16x32_bf16 v[64:67], a[224:227], a[144:147], v[64:67]\n"
"ds_read_b64_tr_b16 a[250:251], v253 offset:30976\n"
"v_mfma_f32_16x16x32_bf16 v[68:71], a[228:231], a[144:147], v[68:71]\n"
"ds_read_b64_tr_b16 a[254:255], v247 offset:30976\n"
"v_mfma_f32_16x16x32_bf16 v[72:75], a[232:235], a[144:147], v[72:75]\n"
"s_add_u32 m0, s66, 29696\n"
"s_add_u32 s62, s42, 9216\n"
"ds_read_b128 a[176:179], v227 offset:26624\n"
"v_mfma_f32_16x16x32_bf16 v[76:79], a[236:239], a[144:147], v[76:79]\n"
"buffer_load_dwordx4 v252, s[76:79], s62 offen lds\n"
"v_mfma_f32_16x16x32_bf16 v[92:95], a[236:239], a[148:151], v[92:95]\n"
"ds_read_b128 a[180:183], v227 offset:27648\n"
"v_mfma_f32_16x16x32_bf16 v[88:91], a[232:235], a[148:151], v[88:91]\n"
"v_mfma_f32_16x16x32_bf16 v[84:87], a[228:231], a[148:151], v[84:87]\n"
"v_mfma_f32_16x16x32_bf16 v[80:83], a[224:227], a[148:151], v[80:83]\n"
"v_mfma_f32_16x16x32_bf16 v[96:99], a[224:227], a[152:155], v[96:99]\n"
"v_mfma_f32_16x16x32_bf16 v[100:103], a[228:231], a[152:155], v[100:103]\n"
"s_add_u32 m0, s66, 30720\n"
"s_add_u32 s62, s42, 18432\n"
"v_mfma_f32_16x16x32_bf16 v[104:107], a[232:235], a[152:155], v[104:107]\n"
"s_waitcnt lgkmcnt(2)\n"
"buffer_load_dwordx4 v252, s[76:79], s62 offen lds\n"
"v_mfma_f32_16x16x32_bf16 v[108:111], a[236:239], a[152:155], v[108:111]\n"
"; oproj step 15\n"
"s_waitcnt lgkmcnt(0)\n"
"v_mfma_f32_16x16x32_bf16 v[0:3], a[240:243], a[156:159], v[0:3]\n"
"s_waitcnt vmcnt(9)\n"
"ds_read_b64_tr_b16 a[224:225], v228 offset:32768\n"
"v_mfma_f32_16x16x32_bf16 v[4:7], a[244:247], a[156:159], v[4:7]\n"
"ds_read_b64_tr_b16 a[228:229], v229 offset:32768\n"
"v_mfma_f32_16x16x32_bf16 v[8:11], a[248:251], a[156:159], v[8:11]\n"
"ds_read_b64_tr_b16 a[232:233], v230 offset:32768\n"
"v_mfma_f32_16x16x32_bf16 v[12:15], a[252:255], a[156:159], v[12:15]\n"
"ds_read_b64_tr_b16 a[8:9], v231 offset:32768\n"
"v_mfma_f32_16x16x32_bf16 v[28:31], a[252:255], a[160:163], v[28:31]\n"
"s_add_u32 m0, s66, 31744\n"
"s_add_u32 s62, s42, 27648\n"
"ds_read_b128 a[128:131], v224 offset:0\n"
"v_mfma_f32_16x16x32_bf16 v[24:27], a[248:251], a[160:163], v[24:27]\n"
"buffer_load_dwordx4 v252, s[76:79], s62 offen lds\n"
"v_mfma_f32_16x16x32_bf16 v[20:23], a[244:247], a[160:163], v[20:23]\n"
"ds_read_b128 a[132:135], v224 offset:1024\n"
"v_mfma_f32_16x16x32_bf16 v[16:19], a[240:243], a[160:163], v[16:19]\n"
"ds_read_b128 a[136:139], v224 offset:2048\n"
"v_mfma_f32_16x16x32_bf16 v[32:35], a[240:243], a[164:167], v[32:35]\n"
"ds_read_b128 a[140:143], v224 offset:3072\n"
"v_mfma_f32_16x16x32_bf16 v[36:39], a[244:247], a[164:167], v[36:39]\n"
"ds_read_b128 a[144:147], v224 offset:4096\n"
"v_mfma_f32_16x16x32_bf16 v[40:43], a[248:251], a[164:167], v[40:43]\n"
"v_mfma_f32_16x16x32_bf16 v[44:47], a[252:255], a[164:167], v[44:47]\n"
"s_add_u32 m0, s66, 32768\n"
"s_add_u32 s62, s43, 0\n"
"v_mfma_f32_16x16x32_bf16 v[60:63], a[252:255], a[168:171], v[60:63]\n"
"s_waitcnt lgkmcnt(5)\n"
"buffer_load_dwordx4 v252, s[76:79], s62 offen lds\n"
"v_mfma_f32_16x16x32_bf16 v[56:59], a[248:251], a[168:171], v[56:59]\n"
"v_cvt_pk_bf16_f32 v0, v0, v1\n"
"v_cvt_pk_bf16_f32 v1, v2, v3\n"
"v_mfma_f32_16x16x32_bf16 v[52:55], a[244:247], a[168:171], v[52:55]\n"
"s_waitcnt vmcnt(9)\n"
"ds_read_b64_tr_b16 a[226:227], v228 offset:34816\n"
"v_mfma_f32_16x16x32_bf16 v[48:51], a[240:243], a[168:171], v[48:51]\n"
"ds_read_b64_tr_b16 a[230:231], v229 offset:34816\n"
"v_cvt_pk_bf16_f32 v2, v16, v17\n"
"v_mfma_f32_16x16x32_bf16 v[64:67], a[240:243], a[172:175], v[64:67]\n"
"ds_read_b64_tr_b16 a[234:235], v230 offset:34816\n"
"v_cvt_pk_bf16_f32 v3, v18, v19\n"
"v_mfma_f32_16x16x32_bf16 v[68:71], a[244:247], a[172:175], v[68:71]\n"
"ds_read_b64_tr_b16 a[10:11], v231 offset:34816\n"
"v_mfma_f32_16x16x32_bf16 v[72:75], a[248:251], a[172:175], v[72:75]\n"
"s_add_u32 m0, s66, 33792\n"
"s_add_u32 s62, s43, 9216\n"
"ds_read_b128 a[148:151], v224 offset:5120\n"
"v_mfma_f32_16x16x32_bf16 v[76:79], a[252:255], a[172:175], v[76:79]\n"
"buffer_load_dwordx4 v252, s[76:79], s62 offen lds\n"
"v_mfma_f32_16x16x32_bf16 v[92:95], a[252:255], a[176:179], v[92:95]\n"
"ds_read_b128 a[152:155], v224 offset:6144\n"
"v_mfma_f32_16x16x32_bf16 v[88:91], a[248:251], a[176:179], v[88:91]\n"
"buffer_store_dwordx4 v[0:3], %[tA], s[32:35], s94 offen sc1\n"
"v_mfma_f32_16x16x32_bf16 v[84:87], a[244:247], a[176:179], v[84:87]\n"
"v_mfma_f32_16x16x32_bf16 v[80:83], a[240:243], a[176:179], v[80:83]\n"
"v_mfma_f32_16x16x32_bf16 v[96:99], a[240:243], a[180:183], v[96:99]\n"
"v_mfma_f32_16x16x32_bf16 v[100:103], a[244:247], a[180:183], v[100:103]\n"
"s_add_u32 m0, s66, 34816\n"
"s_add_u32 s62, s43, 18432\n"
"v_mfma_f32_16x16x32_bf16 v[104:107], a[248:251], a[180:183], v[104:107]\n"
"s_waitcnt lgkmcnt(2)\n"
"buffer_load_dwordx4 v252, s[76:79], s62 offen lds\n"
"v_mfma_f32_16x16x32_bf16 v[108:111], a[252:255], a[180:183], v[108:111]\n"
"; oproj step 16\n"
"s_waitcnt lgkmcnt(0)\n"
"v_mfma_f32_16x16x32_bf16 v[112:115], a[224:227], a[128:131], 0\n"
"s_waitcnt vmcnt(10)\n"
"ds_read_b64_tr_b16 a[240:241], v228 offset:36864\n"
"v_mfma_f32_16x16x32_bf16 v[116:119], a[228:231], a[128:131], 0\n"
"ds_read_b64_tr_b16 a[244:245], v229 offset:36864\n"
"v_mfma_f32_16x16x32_bf16 v[120:123], a[232:235], a[128:131], 0\n"
"ds_read_b64_tr_b16 a[248:249], v230 offset:36864\n"
"v_mfma_f32_16x16x32_bf16 v[132:135], a[232:235], a[132:135], 0\n"
"ds_read_b64_tr_b16 a[12:13], v231 offset:36864\n"
"v_mfma_f32_16x16x32_bf16 v[128:131], a[228:231], a[132:135], 0\n"
"ds_read_b128 a[156:159], v225 offset:0\n"
"v_cvt_pk_bf16_f32 v4, v4, v5\n"
"v_mfma_f32_16x16x32_bf16 v[124:127], a[224:227], a[132:135], 0\n"
"s_add_u32 m0, s66, 35840\n"
"s_add_u32 s62, s43, 27648\n"
"ds_read_b128 a[160:163], v225 offset:1024\n"
"v_mfma_f32_16x16x32_bf16 v[136:139], a[224:227], a[136:139], 0\n"
"buffer_load_dwordx4 v252, s[76:79], s62 offen lds\n"
"v_mfma_f32_16x16x32_bf16 v[140:143], a[228:231], a[136:139], 0\n"
"ds_read_b128 a[164:167], v225 offset:2048\n"
"v_cvt_pk_bf16_f32 v5, v6, v7\n"
"v_mfma_f32_16x16x32_bf16 v[144:147], a[232:235], a[136:139], 0\n"
"v_cvt_pk_bf16_f32 v6, v20, v21\n"
"v_cvt_pk_bf16_f32 v7, v22, v23\n"
"v_mfma_f32_16x16x32_bf16 v[156:159], a[232:235], a[140:143], 0\n"
"s_waitcnt vmcnt(9)\n"
"ds_read_b64_tr_b16 a[242:243], v228 offset:38912\n"
"v_mfma_f32_16x16x32_bf16 v[152:155], a[228:231], a[140:143], 0\n"
"Lh10:\n"
"s_cbranch_execz Lw2\n"
"s_add_u32 m0, s66, 36864\n"
"s_add_u32 s62, s40, 36864\n"
"ds_read_b64_tr_b16 a[246:247], v229 offset:38912\n"
"v_mfma_f32_16x16x32_bf16 v[148:151], a[224:227], a[140:143], 0\n"
"s_waitcnt lgkmcnt(5)\n"
"buffer_load_dwordx4 v252, s[76:79], s62 offen lds\n"
"v_mfma_f32_16x16x32_bf16 v[160:163], a[224:227], a[144:147], 0\n"
"ds_read_b64_tr_b16 a[250:251], v230 offset:38912\n"
"v_mfma_f32_16x16x32_bf16 v[164:167], a[228:231], a[144:147], 0\n"
"ds_read_b64_tr_b16 a[14:15], v231 offset:38912\n"
"v_mfma_f32_16x16x32_bf16 v[168:171], a[232:235], a[144:147], 0\n"
"ds_read_b128 a[168:171], v225 offset:3072\n"
"v_mfma_f32_16x16x32_bf16 v[180:183], a[232:235], a[148:151], 0\n"
"s_add_u32 m0, s66, 37888\n"
"s_add_u32 s62, s40, 46080\n"
"ds_read_b128 a[172:175], v225 offset:4096\n"
"v_mfma_f32_16x16x32_bf16 v[176:179], a[228:231], a[148:151], 0\n"
"buffer_load_dwordx4 v252, s[76:79], s62 offen lds\n"
"v_mfma_f32_16x16x32_bf16 v[172:175], a[224:227], a[148:151], 0\n"
"ds_read_b128 a[176:179], v225 offset:5120\n"
"v_mfma_f32_16x16x32_bf16 v[184:187], a[224:227], a[152:155], 0\n"
"ds_read_b128 a[180:183], v225 offset:6144\n"
"v_mfma_f32_16x16x32_bf16 v[188:191], a[228:231], a[152:155], 0\n"
"s_waitcnt vmcnt(9)\n"
"buffer_store_dwordx4 v[4:7], %[tA], s[32:35], s95 offen sc1\n"
"v_mfma_f32_16x16x32_bf16 v[192:195], a[232:235], a[152:155], 0\n"
"; oproj step 17\n"
"s_waitcnt lgkmcnt(5)\n"
"v_mfma_f32_16x16x32_bf16 v[112:115], a[240:243], a[156:159], v[112:115]\n"
"s_add_u32 m0, s66, 38912\n"
"s_add_u32 s62, s40, 55296\n"
"ds_read_b64_tr_b16 a[224:225], v228 offset:28672\n"
"v_mfma_f32_16x16x32_bf16 v[116:119], a[244:247], a[156:159], v[116:119]\n"
"s_waitcnt lgkmcnt(5)\n"
"buffer_load_dwordx4 v252, s[76:79], s62 offen lds\n"
"v_mfma_f32_16x16x32_bf16 v[120:123], a[248:251], a[156:159], v[120:123]\n"
"ds_read_b64_tr_b16 a[228:229], v229 offset:28672\n"
"v_mfma_f32_16x16x32_bf16 v[132:135], a[248:251], a[160:163], v[132:135]\n"
"ds_read_b64_tr_b16 a[232:233], v230 offset:28672\n"
"v_cvt_pk_bf16_f32 v8, v8, v9\n"
"v_mfma_f32_16x16x32_bf16 v[128:131], a[244:247], a[160:163], v[128:131]\n"
"s_waitcnt lgkmcnt(3)\n"
"ds_read_b64_tr_b16 a[16:17], v231 offset:28672\n"
"v_mfma_f32_16x16x32_bf16 v[124:127], a[240:243], a[160:163], v[124:127]\n"
"s_add_u32 m0, s66, 39936\n"
"s_add_u32 s62, s40, 64512\n"
"ds_read_b128 a[128:131], v226 offset:0\n"
"v_mfma_f32_16x16x32_bf16 v[136:139], a[240:243], a[164:167], v[136:139]\n"
"buffer_load_dwordx4 v252, s[76:79], s62 offen lds\n"
"v_mfma_f32_16x16x32_bf16 v[140:143], a[244:247], a[164:167], v[140:143]\n"
"ds_read_b128 a[132:135], v226 offset:1024\n"
"v_cvt_pk_bf16_f32 v9, v10, v11\n"
"v_mfma_f32_16x16x32_bf16 v[144:147], a[248:251], a[164:167], v[144:147]\n"
"ds_read_b128 a[136:139], v226 offset:2048\n"
"v_cvt_pk_bf16_f32 v10, v24, v25\n"
"v_mfma_f32_16x16x32_bf16 v[156:159], a[248:251], a[168:171], v[156:159]\n"
"s_waitcnt vmcnt(10)\n"
"ds_read_b64_tr_b16 a[226:227], v228 offset:30720\n"
"v_mfma_f32_16x16x32_bf16 v[152:155], a[244:247], a[168:171], v[152:155]\n"
"s_add_u32 m0, s66, 28672\n"
"s_add_u32 s62, s41, 36864\n"
"ds_read_b64_tr_b16 a[230:231], v229 offset:30720\n"
"v_mfma_f32_16x16x32_bf16 v[148:151], a[240:243], a[168:171], v[148:151]\n"
"s_waitcnt lgkmcnt(5)\n"
"buffer_load_dwordx4 v252, s[76:79], s62 offen lds\n"
"v_mfma_f32_16x16x32_bf16 v[160:163], a[240:243], a[172:175], v[160:163]\n"
"ds_read_b64_tr_b16 a[234:235], v230 offset:30720\n"
"v_cvt_pk_bf16_f32 v11, v26, v27\n"
"v_mfma_f32_16x16x32_bf16 v[164:167], a[244:247], a[172:175], v[164:167]\n"
"ds_read_b64_tr_b16 a[18:19], v231 offset:30720\n"
"v_mfma_f32_16x16x32_bf16 v[168:171], a[248:251], a[172:175], v[168:171]\n"
"ds_read_b128 a[140:143], v226 offset:3072\n"
"v_mfma_f32_16x16x32_bf16 v[180:183], a[248:251], a[176:179], v[180:183]\n"
"s_add_u32 m0, s66, 29696\n"
"s_add_u32 s62, s41, 46080\n"
"ds_read_b128 a[144:147], v226 offset:4096\n"
"v_mfma_f32_16x16x32_bf16 v[176:179], a[244:247], a[176:179], v[176:179]\n"
"buffer_load_dwordx4 v252, s[76:79], s62 offen lds\n"
"v_mfma_f32_16x16x32_bf16 v[172:175], a[240:243], a[176:179], v[172:175]\n"
"ds_read_b128 a[148:151], v226 offset:5120\n"
"v_mfma_f32_16x16x32_bf16 v[184:187], a[240:243], a[180:183], v[184:187]\n"
"ds_read_b128 a[152:155], v226 offset:6144\n"
"v_mfma_f32_16x16x32_bf16 v[188:191], a[244:247], a[180:183], v[188:191]\n"
"s_waitcnt vmcnt(10)\n"
"buffer_store_dwordx4 v[8:11], %[tA], s[32:35], s88 offen sc1\n"
"v_mfma_f32_16x16x32_bf16 v[192:195], a[248:251], a[180:183], v[192:195]\n"
"; oproj step 18\n"
"s_waitcnt lgkmcnt(5)\n"
"v_mfma_f32_16x16x32_bf16 v[112:115], a[224:227], a[128:131], v[112:115]\n"
"s_add_u32 m0, s66, 30720\n"
"s_add_u32 s62, s41, 55296\n"
"ds_read_b64_tr_b16 a[240:241], v228 offset:32768\n"
"v_mfma_f32_16x16x32_bf16 v[116:119], a[228:231], a[128:131], v[116:119]\n"
"s_waitcnt lgkmcnt(5)\n"
"buffer_load_dwordx4 v252, s[76:79], s62 offen lds\n"
"v_mfma_f32_16x16x32_bf16 v[120:123], a[232:235], a[128:131], v[120:123]\n"
"ds_read_b64_tr_b16 a[244:245], v229 offset:32768\n"
"v_cvt_pk_bf16_f32 v12, v12, v13\n"
"v_mfma_f32_16x16x32_bf16 v[132:135], a[232:235], a[132:135], v[132:135]\n"
"ds_read_b64_tr_b16 a[248:249], v230 offset:32768\n"
"v_cvt_pk_bf16_f32 v13, v14, v15\n"
"v_mfma_f32_16x16x32_bf16 v[128:131], a[228:231], a[132:135], v[128:131]\n"
"s_waitcnt lgkmcnt(3)\n"
"ds_read_b64_tr_b16 a[20:21], v231 offset:32768\n"
"v_mfma_f32_16x16x32_bf16 v[124:127], a[224:227], a[132:135], v[124:127]\n"
"s_add_u32 m0, s66, 31744\n"
"s_add_u32 s62, s41, 64512\n"
"ds_read_b128 a[156:159], v227 offset:0\n"
"v_mfma_f32_16x16x32_bf16 v[136:139], a[224:227], a[136:139], v[136:139]\n"
"buffer_load_dwordx4 v252, s[76:79], s62 offen lds\n"
"v_mfma_f32_16x16x32_bf16 v[140:143], a[228:231], a[136:139], v[140:143]\n"
"ds_read_b128 a[160:163], v227 offset:1024\n"
"v_cvt_pk_bf16_f32 v14, v28, v29\n"
"v_mfma_f32_16x16x32_bf16 v[144:147], a[232:235], a[136:139], v[144:147]\n"
"ds_read_b128 a[164:167], v227 offset:2048\n"
"v_cvt_pk_bf16_f32 v15, v30, v31\n"
"v_mfma_f32_16x16x32_bf16 v[156:159], a[232:235], a[140:143], v[156:159]\n"
"s_waitcnt vmcnt(10)\n"
"ds_read_b64_tr_b16 a[242:243], v228 offset:34816\n"
"v_mfma_f32_16x16x32_bf16 v[152:155], a[228:231], a[140:143], v[152:155]\n"
"s_add_u32 m0, s66, 32768\n"
"s_add_u32 s62, s42, 36864\n"
"ds_read_b64_tr_b16 a[246:247], v229 offset:34816\n"
"v_mfma_f32_16x16x32_bf16 v[148:151], a[224:227], a[140:143], v[148:151]\n"
"s_waitcnt lgkmcnt(5)\n"
"buffer_load_dwordx4 v252, s[76:79], s62 offen lds\n"
"v_mfma_f32_16x16x32_bf16 v[160:163], a[224:227], a[144:147], v[160:163]\n"
"ds_read_b64_tr_b16 a[250:251], v230 offset:34816\n"
"v_mfma_f32_16x16x32_bf16 v[164:167], a[228:231], a[144:147], v[164:167]\n"
"ds_read_b64_tr_b16 a[22:23], v231 offset:34816\n"
"v_mfma_f32_16x16x32_bf16 v[168:171], a[232:235], a[144:147], v[168:171]\n"
"ds_read_b128 a[168:171], v227 offset:3072\n"
"v_mfma_f32_16x16x32_bf16 v[180:183], a[232:235], a[148:151], v[180:183]\n"
"s_add_u32 m0, s66, 33792\n"
"s_add_u32 s62, s42, 46080\n"
"ds_read_b128 a[172:175], v227 offset:4096\n"
"v_mfma_f32_16x16x32_bf16 v[176:179], a[228:231], a[148:151], v[176:179]\n"
"buffer_load_dwordx4 v252, s[76:79], s62 offen lds\n"
"v_mfma_f32_16x16x32_bf16 v[172:175], a[224:227], a[148:151], v[172:175]\n"
"ds_read_b128 a[176:179], v227 offset:5120\n"
"v_mfma_f32_16x16x32_bf16 v[184:187], a[224:227], a[152:155], v[184:187]\n"
"ds_read_b128 a[180:183], v227 offset:6144\n"
"v_mfma_f32_16x16x32_bf16 v[188:191], a[228:231], a[152:155], v[188:191]\n"
"s_waitcnt vmcnt(10)\n"
"buffer_store_dwordx4 v[12:15], %[tA], s[32:35], s90 offen sc1\n"
"v_mfma_f32_16x16x32_bf16 v[192:195], a[232:235], a[152:155], v[192:195]\n"
"; oproj step 19\n"
"s_waitcnt lgkmcnt(5)\n"
"v_mfma_f32_16x16x32_bf16 v[112:115], a[240:243], a[156:159], v[112:115]\n"
"s_add_u32 m0, s66, 34816\n"
"s_add_u32 s62, s42, 55296\n"
"ds_read_b64_tr_b16 a[224:225], v228 offset:36864\n"
"v_mfma_f32_16x16x32_bf16 v[116:119], a[244:247], a[156:159], v[116:119]\n"
"s_waitcnt lgkmcnt(5)\n"
"buffer_load_dwordx4 v252, s[76:79], s62 offen lds\n"
"v_mfma_f32_16x16x32_bf16 v[120:123], a[248:251], a[156:159], v[120:123]\n"
"ds_read_b64_tr_b16 a[228:229], v229 offset:36864\n"
"v_cvt_pk_bf16_f32 v32, v32, v33\n"
"v_mfma_f32_16x16x32_bf16 v[132:135], a[248:251], a[160:163], v[132:135]\n"
"ds_read_b64_tr_b16 a[232:233], v230 offset:36864\n"
"v_cvt_pk_bf16_f32 v33, v34, v35\n"
"v_mfma_f32_16x16x32_bf16 v[128:131], a[244:247], a[160:163], v[128:131]\n"
"s_waitcnt lgkmcnt(3)\n"
"ds_read_b64_tr_b16 a[40:41], v231 offset:36864\n"
"v_mfma_f32_16x16x32_bf16 v[124:127], a[240:243], a[160:163], v[124:127]\n"
"s_add_u32 m0, s66, 35840\n"
"s_add_u32 s62, s42, 64512\n"
"ds_read_b128 a[128:131], v224 offset:7168\n"
"v_mfma_f32_16x16x32_bf16 v[136:139], a[240:243], a[164:167], v[136:139]\n"
"buffer_load_dwordx4 v252, s[76:79], s62 offen lds\n"
"v_mfma_f32_16x16x32_bf16 v[140:143], a[244:247], a[164:167], v[140:143]\n"
"ds_read_b128 a[132:135], v224 offset:8192\n"
"v_cvt_pk_bf16_f32 v34, v48, v49\n"
"v_mfma_f32_16x16x32_bf16 v[144:147], a[248:251], a[164:167], v[144:147]\n"
"ds_read_b128 a[136:139], v224 offset:9216\n"
"v_cvt_pk_bf16_f32 v35, v50, v51\n"
"v_mfma_f32_16x16x32_bf16 v[156:159], a[248:251], a[168:171], v[156:159]\n"
"s_waitcnt vmcnt(10)\n"
"ds_read_b64_tr_b16 a[226:227], v228 offset:38912\n"
"v_mfma_f32_16x16x32_bf16 v[152:155], a[244:247], a[168:171], v[152:155]\n"
"s_add_u32 m0, s66, 36864\n"
"s_add_u32 s62, s43, 36864\n"
"ds_read_b64_tr_b16 a[230:231], v229 offset:38912\n"
"v_mfma_f32_16x16x32_bf16 v[148:151], a[240:243], a[168:171], v[148:151]\n"
"s_waitcnt lgkmcnt(5)\n"
"buffer_load_dwordx4 v252, s[76:79], s62 offen lds\n"
"v_mfma_f32_16x16x32_bf16 v[160:163], a[240:243], a[172:175], v[160:163]\n"
"ds_read_b64_tr_b16 a[234:235], v230 offset:38912\n"
"v_mfma_f32_16x16x32_bf16 v[164:167], a[244:247], a[172:175], v[164:167]\n"
"ds_read_b64_tr_b16 a[42:43], v231 offset:38912\n"
"v_mfma_f32_16x16x32_bf16 v[168:171], a[248:251], a[172:175], v[168:171]\n"
"ds_read_b128 a[140:143], v224 offset:10240\n"
"v_mfma_f32_16x16x32_bf16 v[180:183], a[248:251], a[176:179], v[180:183]\n"
"s_add_u32 m0, s66, 37888\n"
"s_add_u32 s62, s43, 46080\n"
"ds_read_b128 a[144:147], v224 offset:11264\n"
"v_mfma_f32_16x16x32_bf16 v[176:179], a[244:247], a[176:179], v[176:179]\n"
"buffer_load_dwordx4 v252, s[76:79], s62 offen lds\n"
"v_mfma_f32_16x16x32_bf16 v[172:175], a[240:243], a[176:179], v[172:175]\n"
"ds_read_b128 a[148:151], v224 offset:12288\n"
"v_mfma_f32_16x16x32_bf16 v[184:187], a[240:243], a[180:183], v[184:187]\n"
"ds_read_b128 a[152:155], v224 offset:13312\n"
"v_mfma_f32_16x16x32_bf16 v[188:191], a[244:247], a[180:183], v[188:191]\n"
"s_waitcnt vmcnt(10)\n"
"buffer_store_dwordx4 v[32:35], %[tA], s[32:35], s94 offen offset:1024 sc1\n"
"v_mfma_f32_16x16x32_bf16 v[192:195], a[248:251], a[180:183], v[192:195]\n"
"; oproj step 20\n"
"s_waitcnt lgkmcnt(5)\n"
"v_mfma_f32_16x16x32_bf16 v[112:115], a[224:227], a[128:131], v[112:115]\n"
"s_add_u32 m0, s66, 38912\n"
"s_add_u32 s62, s43, 55296\n"
"ds_read_b64_tr_b16 a[240:241], v228 offset:28672\n"
"v_mfma_f32_16x16x32_bf16 v[116:119], a[228:231], a[128:131], v[116:119]\n"
"s_waitcnt lgkmcnt(5)\n"
"buffer_load_dwordx4 v252, s[76:79], s62 offen lds\n"
"v_mfma_f32_16x16x32_bf16 v[120:123], a[232:235], a[128:131], v[120:123]\n"
"ds_read_b64_tr_b16 a[244:245], v229 offset:28672\n"
"v_cvt_pk_bf16_f32 v36, v36, v37\n"
"v_mfma_f32_16x16x32_bf16 v[132:135], a[232:235], a[132:135], v[132:135]\n"
"ds_read_b64_tr_b16 a[248:249], v230 offset:28672\n"
"v_cvt_pk_bf16_f32 v37, v38, v39\n"
"v_mfma_f32_16x16x32_bf16 v[128:131], a[228:231], a[132:135], v[128:131]\n"
"s_waitcnt lgkmcnt(3)\n"
"ds_read_b64_tr_b16 a[44:45], v231 offset:28672\n"
"v_mfma_f32_16x16x32_bf16 v[124:127], a[224:227], a[132:135], v[124:127]\n"
"s_add_u32 m0, s66, 39936\n"
"s_add_u32 s62, s43, 64512\n"
"ds_read_b128 a[156:159], v225 offset:7168\n"
"v_mfma_f32_16x16x32_bf16 v[136:139], a[224:227], a[136:139], v[136:139]\n"
"buffer_load_dwordx4 v252, s[76:79], s62 offen lds\n"
"v_mfma_f32_16x16x32_bf16 v[140:143], a[228:231], a[136:139], v[140:143]\n"
"ds_read_b128 a[160:163], v225 offset:8192\n"
"v_cvt_pk_bf16_f32 v38, v52, v53\n"
"v_mfma_f32_16x16x32_bf16 v[144:147], a[232:235], a[136:139], v[144:147]\n"
"ds_read_b128 a[164:167], v225 offset:9216\n"
"v_cvt_pk_bf16_f32 v39, v54, v55\n"
"v_mfma_f32_16x16x32_bf16 v[156:159], a[232:235], a[140:143], v[156:159]\n"
"s_waitcnt vmcnt(10)\n"
"ds_read_b64_tr_b16 a[242:243], v228 offset:30720\n"
"v_mfma_f32_16x16x32_bf16 v[152:155], a[228:231], a[140:143], v[152:155]\n"
"s_add_u32 m0, s66, 28672\n"
"s_add_u32 s62, s40, 73728\n"
"ds_read_b64_tr_b16 a[246:247], v229 offset:30720\n"
"v_mfma_f32_16x16x32_bf16 v[148:151], a[224:227], a[140:143], v[148:151]\n"
"s_waitcnt lgkmcnt(5)\n"
"buffer_load_dwordx4 v252, s[76:79], s62 offen lds\n"
"v_mfma_f32_16x16x32_bf16 v[160:163], a[224:227], a[144:147], v[160:163]\n"
"ds_read_b64_tr_b16 a[250:251], v230 offset:30720\n"
"v_mfma_f32_16x16x32_bf16 v[164:167], a[228:231], a[144:147], v[164:167]\n"
"ds_read_b64_tr_b16 a[46:47], v231 offset:30720\n"
"v_mfma_f32_16x16x32_bf16 v[168:171], a[232:235], a[144:147], v[168:171]\n"
"ds_read_b128 a[168:171], v225 offset:10240\n"
"v_mfma_f32_16x16x32_bf16 v[180:183], a[232:235], a[148:151], v[180:183]\n"
"s_add_u32 m0, s66, 29696\n"
"s_add_u32 s62, s40, 82944\n"
"ds_read_b128 a[172:175], v225 offset:11264\n"
"v_mfma_f32_16x16x32_bf16 v[176:179], a[228:231], a[148:151], v[176:179]\n"
"buffer_load_dwordx4 v252, s[76:79], s62 offen lds\n"
"v_mfma_f32_16x16x32_bf16 v[172:175], a[224:227], a[148:151], v[172:175]\n"
"ds_read_b128 a[176:179], v225 offset:12288\n"
"v_mfma_f32_16x16x32_bf16 v[184:187], a[224:227], a[152:155], v[184:187]\n"
"ds_read_b128 a[180:183], v225 offset:13312\n"
"v_mfma_f32_16x16x32_bf16 v[188:191], a[228:231], a[152:155], v[188:191]\n"
"s_waitcnt vmcnt(10)\n"
"buffer_store_dwordx4 v[36:39], %[tA], s[32:35], s95 offen offset:1024 sc1\n"
"v_mfma_f32_16x16x32_bf16 v[192:195], a[232:235], a[152:155], v[192:195]\n"
"; oproj step 21\n"
"s_waitcnt lgkmcnt(5)\n"
"v_cvt_pk_bf16_f32 v40, v40, v41\n"
"v_cvt_pk_bf16_f32 v41, v42, v43\n"
"v_mfma_f32_16x16x32_bf16 v[112:115], a[240:243], a[156:159], v[112:115]\n"
"s_add_u32 m0, s66, 30720\n"
"s_add_u32 s62, s40, 92160\n"
"ds_read_b64_tr_b16 a[224:225], v228 offset:32768\n"
"v_mfma_f32_16x16x32_bf16 v[116:119], a[244:247], a[156:159], v[116:119]\n"
"s_waitcnt lgkmcnt(5)\n"
"buffer_load_dwordx4 v252, s[76:79], s62 offen lds\n"
"v_mfma_f32_16x16x32_bf16 v[120:123], a[248:251], a[156:159], v[120:123]\n"
"ds_read_b64_tr_b16 a[228:229], v229 offset:32768\n"
"v_cvt_pk_bf16_f32 v42, v56, v57\n"
"v_mfma_f32_16x16x32_bf16 v[132:135], a[248:251], a[160:163], v[132:135]\n"
"ds_read_b64_tr_b16 a[232:233], v230 offset:32768\n"
"v_cvt_pk_bf16_f32 v43, v58, v59\n"
"v_mfma_f32_16x16x32_bf16 v[128:131], a[244:247], a[160:163], v[128:131]\n"
"s_waitcnt lgkmcnt(3)\n"
"ds_read_b64_tr_b16 a[48:49], v231 offset:32768\n"
"v_mfma_f32_16x16x32_bf16 v[124:127], a[240:243], a[160:163], v[124:127]\n"
"s_add_u32 m0, s66, 31744\n"
"s_add_u32 s62, s40, 101376\n"
"ds_read_b128 a[128:131], v226 offset:7168\n"
"v_mfma_f32_16x16x32_bf16 v[136:139], a[240:243], a[164:167], v[136:139]\n"
"buffer_load_dwordx4 v252, s[76:79], s62 offen lds\n"
"v_mfma_f32_16x16x32_bf16 v[140:143], a[244:247], a[164:167], v[140:143]\n"
"ds_read_b128 a[132:135], v226 offset:8192\n"
"v_mfma_f32_16x16x32_bf16 v[144:147], a[248:251], a[164:167], v[144:147]\n"
"ds_read_b128 a[136:139], v226 offset:9216\n"
"v_mfma_f32_16x16x32_bf16 v[156:159], a[248:251], a[168:171], v[156:159]\n"
"s_waitcnt vmcnt(10)\n"
"ds_read_b64_tr_b16 a[226:227], v228 offset:34816\n"
"v_mfma_f32_16x16x32_bf16 v[152:155], a[244:247], a[168:171], v[152:155]\n"
"s_add_u32 m0, s66, 32768\n"
"s_add_u32 s62, s41, 73728\n"
"ds_read_b64_tr_b16 a[230:231], v229 offset:34816\n"
"v_mfma_f32_16x16x32_bf16 v[148:151], a[240:243], a[168:171], v[148:151]\n"
"s_waitcnt lgkmcnt(5)\n"
"buffer_load_dwordx4 v252, s[76:79], s62 offen lds\n"
"v_mfma_f32_16x16x32_bf16 v[160:163], a[240:243], a[172:175], v[160:163]\n"
"ds_read_b64_tr_b16 a[234:235], v230 offset:34816\n"
"v_mfma_f32_16x16x32_bf16 v[164:167], a[244:247], a[172:175], v[164:167]\n"
"ds_read_b64_tr_b16 a[50:51], v231 offset:34816\n"
"v_mfma_f32_16x16x32_bf16 v[168:171], a[248:251], a[172:175], v[168:171]\n"
"ds_read_b128 a[140:143], v226 offset:10240\n"
"v_mfma_f32_16x16x32_bf16 v[180:183], a[248:251], a[176:179], v[180:183]\n"
"s_add_u32 m0, s66, 33792\n"
"s_add_u32 s62, s41, 82944\n"
"ds_read_b128 a[144:147], v226 offset:11264\n"
"v_mfma_f32_16x16x32_bf16 v[176:179], a[244:247], a[176:179], v[176:179]\n"
"buffer_load_dwordx4 v252, s[76:79], s62 offen lds\n"
"v_mfma_f32_16x16x32_bf16 v[172:175], a[240:243], a[176:179], v[172:175]\n"
"ds_read_b128 a[148:151], v226 offset:12288\n"
"v_mfma_f32_16x16x32_bf16 v[184:187], a[240:243], a[180:183], v[184:187]\n"
"ds_read_b128 a[152:155], v226 offset:13312\n"
"v_mfma_f32_16x16x32_bf16 v[188:191], a[244:247], a[180:183], v[188:191]\n"
"s_waitcnt vmcnt(10)\n"
"buffer_store_dwordx4 v[40:43], %[tA], s[32:35], s88 offen offset:1024 sc1\n"
"v_mfma_f32_16x16x32_bf16 v[192:195], a[248:251], a[180:183], v[192:195]\n"
"; oproj step 22\n"
"s_waitcnt lgkmcnt(5)\n"
"v_cvt_pk_bf16_f32 v44, v44, v45\n"
"v_cvt_pk_bf16_f32 v45, v46, v47\n"
"v_mfma_f32_16x16x32_bf16 v[112:115], a[224:227], a[128:131], v[112:115]\n"
"s_add_u32 m0, s66, 34816\n"
"s_add_u32 s62, s41, 92160\n"
"ds_read_b64_tr_b16 a[240:241], v228 offset:36864\n"
"v_mfma_f32_16x16x32_bf16 v[116:119], a[228:231], a[128:131], v[116:119]\n"
"s_waitcnt lgkmcnt(5)\n"
"buffer_load_dwordx4 v252, s[76:79], s62 offen lds\n"
"v_mfma_f32_16x16x32_bf16 v[120:123], a[232:235], a[128:131], v[120:123]\n"
"ds_read_b64_tr_b16 a[244:245], v229 offset:36864\n"
"v_cvt_pk_bf16_f32 v46, v60, v61\n"
"v_mfma_f32_16x16x32_bf16 v[132:135], a[232:235], a[132:135], v[132:135]\n"
"ds_read_b64_tr_b16 a[248:249], v230 offset:36864\n"
"v_cvt_pk_bf16_f32 v47, v62, v63\n"
"v_mfma_f32_16x16x32_bf16 v[128:131], a[228:231], a[132:135], v[128:131]\n"
"s_waitcnt lgkmcnt(3)\n"
"ds_read_b64_tr_b16 a[52:53], v231 offset:36864\n"
"v_mfma_f32_16x16x32_bf16 v[124:127], a[224:227], a[132:135], v[124:127]\n"
"s_add_u32 m0, s66, 35840\n"
"s_add_u32 s62, s41, 101376\n"
"ds_read_b128 a[156:159], v227 offset:7168\n"
"v_mfma_f32_16x16x32_bf16 v[136:139], a[224:227], a[136:139], v[136:139]\n"
"buffer_load_dwordx4 v252, s[76:79], s62 offen lds\n"
"v_mfma_f32_16x16x32_bf16 v[140:143], a[228:231], a[136:139], v[140:143]\n"
"ds_read_b128 a[160:163], v227 offset:8192\n"
"v_mfma_f32_16x16x32_bf16 v[144:147], a[232:235], a[136:139], v[144:147]\n"
"ds_read_b128 a[164:167], v227 offset:9216\n"
"v_mfma_f32_16x16x32_bf16 v[156:159], a[232:235], a[140:143], v[156:159]\n"
"s_waitcnt vmcnt(10)\n"
"ds_read_b64_tr_b16 a[242:243], v228 offset:38912\n"
"v_mfma_f32_16x16x32_bf16 v[152:155], a[228:231], a[140:143], v[152:155]\n"
"s_add_u32 m0, s66, 36864\n"
"s_add_u32 s62, s42, 73728\n"
"ds_read_b64_tr_b16 a[246:247], v229 offset:38912\n"
"v_mfma_f32_16x16x32_bf16 v[148:151], a[224:227], a[140:143], v[148:151]\n"
"s_waitcnt lgkmcnt(5)\n"
"buffer_load_dwordx4 v252, s[76:79], s62 offen lds\n"
"v_mfma_f32_16x16x32_bf16 v[160:163], a[224:227], a[144:147], v[160:163]\n"
"ds_read_b64_tr_b16 a[250:251], v230 offset:38912\n"
"v_mfma_f32_16x16x32_bf16 v[164:167], a[228:231], a[144:147], v[164:167]\n"
"ds_read_b64_tr_b16 a[54:55], v231 offset:38912\n"
"v_mfma_f32_16x16x32_bf16 v[168:171], a[232:235], a[144:147], v[168:171]\n"
"ds_read_b128 a[168:171], v227 offset:10240\n"
"v_mfma_f32_16x16x32_bf16 v[180:183], a[232:235], a[148:151], v[180:183]\n"
"s_add_u32 m0, s66, 37888\n"
"s_add_u32 s62, s42, 82944\n"
"ds_read_b128 a[172:175], v227 offset:11264\n"
"v_mfma_f32_16x16x32_bf16 v[176:179], a[228:231], a[148:151], v[176:179]\n"
"buffer_load_dwordx4 v252, s[76:79], s62 offen lds\n"
"v_mfma_f32_16x16x32_bf16 v[172:175], a[224:227], a[148:151], v[172:175]\n"
"ds_read_b128 a[176:179], v227 offset:12288\n"
"v_mfma_f32_16x16x32_bf16 v[184:187], a[224:227], a[152:155], v[184:187]\n"
"ds_read_b128 a[180:183], v227 offset:13312\n"
"v_mfma_f32_16x16x32_bf16 v[188:191], a[228:231], a[152:155], v[188:191]\n"
"s_waitcnt vmcnt(10)\n"
"buffer_store_dwordx4 v[44:47], %[tA], s[32:35], s90 offen offset:1024 sc1\n"
"v_mfma_f32_16x16x32_bf16 v[192:195], a[232:235], a[152:155], v[192:195]\n"
"; oproj step 23\n"
"s_waitcnt lgkmcnt(5)\n"
"v_cvt_pk_bf16_f32 v64, v64, v65\n"
"v_cvt_pk_bf16_f32 v65, v66, v67\n"
"v_mfma_f32_16x16x32_bf16 v[112:115], a[240:243], a[156:159], v[112:115]\n"
"s_add_u32 m0, s66, 38912\n"
"s_add_u32 s62, s42, 92160\n"
"ds_read_b64_tr_b16 a[224:225], v228 offset:28672\n"
"v_mfma_f32_16x16x32_bf16 v[116:119], a[244:247], a[156:159], v[116:119]\n"
"s_waitcnt lgkmcnt(5)\n"
"buffer_load_dwordx4 v252, s[76:79], s62 offen lds\n"
"v_mfma_f32_16x16x32_bf16 v[120:123], a[248:251], a[156:159], v[120:123]\n"
"ds_read_b64_tr_b16 a[228:229], v229 offset:28672\n"
"v_cvt_pk_bf16_f32 v66, v80, v81\n"
"v_mfma_f32_16x16x32_bf16 v[132:135], a[248:251], a[160:163], v[132:135]\n"
"ds_read_b64_tr_b16 a[232:233], v230 offset:28672\n"
"v_cvt_pk_bf16_f32 v67, v82, v83\n"
"v_mfma_f32_16x16x32_bf16 v[128:131], a[244:247], a[160:163], v[128:131]\n"
"s_waitcnt lgkmcnt(3)\n"
"ds_read_b64_tr_b16 a[72:73], v231 offset:28672\n"
"v_mfma_f32_16x16x32_bf16 v[124:127], a[240:243], a[160:163], v[124:127]\n"
"s_add_u32 m0, s66, 39936\n"
"s_add_u32 s62, s42, 101376\n"
"ds_read_b128 a[128:131], v224 offset:14336\n"
"v_mfma_f32_16x16x32_bf16 v[136:139], a[240:243], a[164:167], v[136:139]\n"
"buffer_load_dwordx4 v252, s[76:79], s62 offen lds\n"
"v_mfma_f32_16x16x32_bf16 v[140:143], a[244:247], a[164:167], v[140:143]\n"
"ds_read_b128 a[132:135], v224 offset:15360\n"
"v_mfma_f32_16x16x32_bf16 v[144:147], a[248:251], a[164:167], v[144:147]\n"
"ds_read_b128 a[136:139], v224 offset:16384\n"
"v_mfma_f32_16x16x32_bf16 v[156:159], a[248:251], a[168:171], v[156:159]\n"
"s_waitcnt vmcnt(10)\n"
"ds_read_b64_tr_b16 a[226:227], v228 offset:30720\n"
"v_mfma_f32_16x16x32_bf16 v[152:155], a[244:247], a[168:171], v[152:155]\n"
"s_add_u32 m0, s66, 28672\n"
"s_add_u32 s62, s43, 73728\n"
"ds_read_b64_tr_b16 a[230:231], v229 offset:30720\n"
"v_mfma_f32_16x16x32_bf16 v[148:151], a[240:243], a[168:171], v[148:151]\n"
"s_waitcnt lgkmcnt(5)\n"
"buffer_load_dwordx4 v252, s[76:79], s62 offen lds\n"
"v_mfma_f32_16x16x32_bf16 v[160:163], a[240:243], a[172:175], v[160:163]\n"
"ds_read_b64_tr_b16 a[234:235], v230 offset:30720\n"
"v_mfma_f32_16x16x32_bf16 v[164:167], a[244:247], a[172:175], v[164:167]\n"
"ds_read_b64_tr_b16 a[74:75], v231 offset:30720\n"
"v_mfma_f32_16x16x32_bf16 v[168:171], a[248:251], a[172:175], v[168:171]\n"
"ds_read_b128 a[140:143], v224 offset:17408\n"
"v_mfma_f32_16x16x32_bf16 v[180:183], a[248:251], a[176:179], v[180:183]\n"
"s_add_u32 m0, s66, 29696\n"
"s_add_u32 s62, s43, 82944\n"
"ds_read_b128 a[144:147], v224 offset:18432\n"
"v_mfma_f32_16x16x32_bf16 v[176:179], a[244:247], a[176:179], v[176:179]\n"
"buffer_load_dwordx4 v252, s[76:79], s62 offen lds\n"
"v_mfma_f32_16x16x32_bf16 v[172:175], a[240:243], a[176:179], v[172:175]\n"
"ds_read_b128 a[148:151], v224 offset:19456\n"
"v_mfma_f32_16x16x32_bf16 v[184:187], a[240:243], a[180:183], v[184:187]\n"
"ds_read_b128 a[152:155], v224 offset:20480\n"
"v_mfma_f32_16x16x32_bf16 v[188:191], a[244:247], a[180:183], v[188:191]\n"
"s_waitcnt vmcnt(10)\n"
"buffer_store_dwordx4 v[64:67], %[tA], s[32:35], s94 offen offset:2048 sc1\n"
"v_mfma_f32_16x16x32_bf16 v[192:195], a[248:251], a[180:183], v[192:195]\n"
"; oproj step 24\n"
"s_waitcnt lgkmcnt(5)\n"
"v_cvt_pk_bf16_f32 v68, v68, v69\n"
"v_cvt_pk_bf16_f32 v69, v70, v71\n"
"v_mfma_f32_16x16x32_bf16 v[112:115], a[224:227], a[128:131], v[112:115]\n"
"s_add_u32 m0, s66, 30720\n"
"s_add_u32 s62, s43, 92160\n"
"ds_read_b64_tr_b16 a[240:241], v228 offset:32768\n"
"v_mfma_f32_16x16x32_bf16 v[116:119], a[228:231], a[128:131], v[116:119]\n"
"s_waitcnt lgkmcnt(5)\n"
"buffer_load_dwordx4 v252, s[76:79], s62 offen lds\n"
"v_mfma_f32_16x16x32_bf16 v[120:123], a[232:235], a[128:131], v[120:123]\n"
"ds_read_b64_tr_b16 a[244:245], v229 offset:32768\n"
"v_cvt_pk_bf16_f32 v70, v84, v85\n"
"v_mfma_f32_16x16x32_bf16 v[132:135], a[232:235], a[132:135], v[132:135]\n"
"ds_read_b64_tr_b16 a[248:249], v230 offset:32768\n"
"v_cvt_pk_bf16_f32 v71, v86, v87\n"
"v_mfma_f32_16x16x32_bf16 v[128:131], a[228:231], a[132:135], v[128:131]\n"
"s_waitcnt lgkmcnt(3)\n"
"ds_read_b64_tr_b16 a[76:77], v231 offset:32768\n"
"v_mfma_f32_16x16x32_bf16 v[124:127], a[224:227], a[132:135], v[124:127]\n"
"s_add_u32 m0, s66, 31744\n"
"s_add_u32 s62, s43, 101376\n"
"ds_read_b128 a[156:159], v225 offset:14336\n"
"v_mfma_f32_16x16x32_bf16 v[136:139], a[224:227], a[136:139], v[136:139]\n"
"buffer_load_dwordx4 v252, s[76:79], s62 offen lds\n"
"v_mfma_f32_16x16x32_bf16 v[140:143], a[228:231], a[136:139], v[140:143]\n"
"ds_read_b128 a[160:163], v225 offset:15360\n"
"v_mfma_f32_16x16x32_bf16 v[144:147], a[232:235], a[136:139], v[144:147]\n"
"ds_read_b128 a[164:167], v225 offset:16384\n"
"v_mfma_f32_16x16x32_bf16 v[156:159], a[232:235], a[140:143], v[156:159]\n"
"s_waitcnt vmcnt(10)\n"
"ds_read_b64_tr_b16 a[242:243], v228 offset:34816\n"
"v_mfma_f32_16x16x32_bf16 v[152:155], a[228:231], a[140:143], v[152:155]\n"
"s_add_u32 m0, s66, 32768\n"
"s_add_u32 s62, s40, 110592\n"
"ds_read_b64_tr_b16 a[246:247], v229 offset:34816\n"
"v_mfma_f32_16x16x32_bf16 v[148:151], a[224:227], a[140:143], v[148:151]\n"
"s_waitcnt lgkmcnt(5)\n"
"buffer_load_dwordx4 v252, s[76:79], s62 offen lds\n"
"v_mfma_f32_16x16x32_bf16 v[160:163], a[224:227], a[144:147], v[160:163]\n"
"ds_read_b64_tr_b16 a[250:251], v230 offset:34816\n"
"v_mfma_f32_16x16x32_bf16 v[164:167], a[228:231], a[144:147], v[164:167]\n"
"ds_read_b64_tr_b16 a[78:79], v231 offset:34816\n"
"v_mfma_f32_16x16x32_bf16 v[168:171], a[232:235], a[144:147], v[168:171]\n"
"ds_read_b128 a[168:171], v225 offset:17408\n"
"v_mfma_f32_16x16x32_bf16 v[180:183], a[232:235], a[148:151], v[180:183]\n"
"s_add_u32 m0, s66, 33792\n"
"s_add_u32 s62, s40, 119808\n"
"ds_read_b128 a[172:175], v225 offset:18432\n"
"v_mfma_f32_16x16x32_bf16 v[176:179], a[228:231], a[148:151], v[176:179]\n"
"buffer_load_dwordx4 v252, s[76:79], s62 offen lds\n"
"v_mfma_f32_16x16x32_bf16 v[172:175], a[224:227], a[148:151], v[172:175]\n"
"ds_read_b128 a[176:179], v225 offset:19456\n"
"v_mfma_f32_16x16x32_bf16 v[184:187], a[224:227], a[152:155], v[184:187]\n"
"ds_read_b128 a[180:183], v225 offset:20480\n"
"v_mfma_f32_16x16x32_bf16 v[188:191], a[228:231], a[152:155], v[188:191]\n"
"s_waitcnt vmcnt(10)\n"
"buffer_store_dwordx4 v[68:71], %[tA], s[32:35], s95 offen offset:2048 sc1\n"
"v_mfma_f32_16x16x32_bf16 v[192:195], a[232:235], a[152:155], v[192:195]\n"
"; oproj step 25\n"
"s_waitcnt lgkmcnt(5)\n"
"v_cvt_pk_bf16_f32 v72, v72, v73\n"
"v_cvt_pk_bf16_f32 v73, v74, v75\n"
"v_mfma_f32_16x16x32_bf16 v[112:115], a[240:243], a[156:159], v[112:115]\n"
"s_add_u32 m0, s66, 34816\n"
"s_add_u32 s62, s40, 129024\n"
"ds_read_b64_tr_b16 a[224:225], v228 offset:36864\n"
"v_mfma_f32_16x16x32_bf16 v[116:119], a[244:247], a[156:159], v[116:119]\n"
"s_waitcnt lgkmcnt(5)\n"
"buffer_load_dwordx4 v252, s[76:79], s62 offen lds\n"
"v_mfma_f32_16x16x32_bf16 v[120:123], a[248:251], a[156:159], v[120:123]\n"
"ds_read_b64_tr_b16 a[228:229], v229 offset:36864\n"
"v_cvt_pk_bf16_f32 v74, v88, v89\n"
"v_mfma_f32_16x16x32_bf16 v[132:135], a[248:251], a[160:163], v[132:135]\n"
"ds_read_b64_tr_b16 a[232:233], v230 offset:36864\n"
"v_cvt_pk_bf16_f32 v75, v90, v91\n"
"v_mfma_f32_16x16x32_bf16 v[128:131], a[244:247], a[160:163], v[128:131]\n"
"s_waitcnt lgkmcnt(3)\n"
"ds_read_b64_tr_b16 a[80:81], v231 offset:36864\n"
"v_mfma_f32_16x16x32_bf16 v[124:127], a[240:243], a[160:163], v[124:127]\n"
"s_add_u32 m0, s66, 35840\n"
"s_add_u32 s62, s40, 138240\n"
"ds_read_b128 a[128:131], v226 offset:14336\n"
"v_mfma_f32_16x16x32_bf16 v[136:139], a[240:243], a[164:167], v[136:139]\n"
"buffer_load_dwordx4 v252, s[76:79], s62 offen lds\n"
"v_mfma_f32_16x16x32_bf16 v[140:143], a[244:247], a[164:167], v[140:143]\n"
"ds_read_b128 a[132:135], v226 offset:15360\n"
"v_mfma_f32_16x16x32_bf16 v[144:147], a[248:251], a[164:167], v[144:147]\n"
"ds_read_b128 a[136:139], v226 offset:16384\n"
"v_mfma_f32_16x16x32_bf16 v[156:159], a[248:251], a[168:171], v[156:159]\n"
"s_waitcnt vmcnt(10)\n"
"ds_read_b64_tr_b16 a[226:227], v228 offset:38912\n"
"v_mfma_f32_16x16x32_bf16 v[152:155], a[244:247], a[168:171], v[152:155]\n"
"s_add_u32 m0, s66, 36864\n"
"s_add_u32 s62, s41, 110592\n"
"ds_read_b64_tr_b16 a[230:231], v229 offset:38912\n"
"v_mfma_f32_16x16x32_bf16 v[148:151], a[240:243], a[168:171], v[148:151]\n"
"s_waitcnt lgkmcnt(5)\n"
"buffer_load_dwordx4 v252, s[76:79], s62 offen lds\n"
"v_mfma_f32_16x16x32_bf16 v[160:163], a[240:243], a[172:175], v[160:163]\n"
"ds_read_b64_tr_b16 a[234:235], v230 offset:38912\n"
"Lh11:\n"
"s_cbranch_execz Lw2\n"
"v_mfma_f32_16x16x32_bf16 v[164:167], a[244:247], a[172:175], v[164:167]\n"
"ds_read_b64_tr_b16 a[82:83], v231 offset:38912\n"
"v_mfma_f32_16x16x32_bf16 v[168:171], a[248:251], a[172:175], v[168:171]\n"
"ds_read_b128 a[140:143], v226 offset:17408\n"
"v_mfma_f32_16x16x32_bf16 v[180:183], a[248:251], a[176:179], v[180:183]\n"
"s_add_u32 m0, s66, 37888\n"
"s_add_u32 s62, s41, 119808\n"
"ds_read_b128 a[144:147], v226 offset:18432\n"
"v_mfma_f32_16x16x32_bf16 v[176:179], a[244:247], a[176:179], v[176:179]\n"
"buffer_load_dwordx4 v252, s[76:79], s62 offen lds\n"
"v_mfma_f32_16x16x32_bf16 v[172:175], a[240:243], a[176:179], v[172:175]\n"
"ds_read_b128 a[148:151], v226 offset:19456\n"
"v_mfma_f32_16x16x32_bf16 v[184:187], a[240:243], a[180:183], v[184:187]\n"
"ds_read_b128 a[152:155], v226 offset:20480\n"
"v_mfma_f32_16x16x32_bf16 v[188:191], a[244:247], a[180:183], v[188:191]\n"
"s_waitcnt vmcnt(10)\n"
"buffer_store_dwordx4 v[72:75], %[tA], s[32:35], s88 offen offset:2048 sc1\n"
"v_mfma_f32_16x16x32_bf16 v[192:195], a[248:251], a[180:183], v[192:195]\n"
"; oproj step 26\n"
"s_waitcnt lgkmcnt(5)\n"
"v_cvt_pk_bf16_f32 v76, v76, v77\n"
"v_cvt_pk_bf16_f32 v77, v78, v79\n"
"v_mfma_f32_16x16x32_bf16 v[112:115], a[224:227], a[128:131], v[112:115]\n"
"s_add_u32 m0, s66, 38912\n"
"s_add_u32 s62, s41, 129024\n"
"ds_read_b64_tr_b16 a[240:241], v228 offset:28672\n"
"v_mfma_f32_16x16x32_bf16 v[116:119], a[228:231], a[128:131], v[116:119]\n"
"s_waitcnt lgkmcnt(5)\n"
"buffer_load_dwordx4 v252, s[76:79], s62 offen lds\n"
"v_mfma_f32_16x16x32_bf16 v[120:123], a[232:235], a[128:131], v[120:123]\n"
"ds_read_b64_tr_b16 a[244:245], v229 offset:28672\n"
"v_cvt_pk_bf16_f32 v78, v92, v93\n"
"v_mfma_f32_16x16x32_bf16 v[132:135], a[232:235], a[132:135], v[132:135]\n"
"ds_read_b64_tr_b16 a[248:249], v230 offset:28672\n"
"v_cvt_pk_bf16_f32 v79, v94, v95\n"
"v_mfma_f32_16x16x32_bf16 v[128:131], a[228:231], a[132:135], v[128:131]\n"
"s_waitcnt lgkmcnt(3)\n"
"ds_read_b64_tr_b16 a[84:85], v231 offset:28672\n"
"v_mfma_f32_16x16x32_bf16 v[124:127], a[224:227], a[132:135], v[124:127]\n"
"s_add_u32 m0, s66, 39936\n"
"s_add_u32 s62, s41, 138240\n"
"ds_read_b128 a[156:159], v227 offset:14336\n"
"v_mfma_f32_16x16x32_bf16 v[136:139], a[224:227], a[136:139], v[136:139]\n"
"buffer_load_dwordx4 v252, s[76:79], s62 offen lds\n"
"v_mfma_f32_16x16x32_bf16 v[140:143], a[228:231], a[136:139], v[140:143]\n"
"ds_read_b128 a[160:163], v227 offset:15360\n"
"v_mfma_f32_16x16x32_bf16 v[144:147], a[232:235], a[136:139], v[144:147]\n"
"ds_read_b128 a[164:167], v227 offset:16384\n"
"v_mfma_f32_16x16x32_bf16 v[156:159], a[232:235], a[140:143], v[156:159]\n"
"s_waitcnt vmcnt(10)\n"
"ds_read_b64_tr_b16 a[242:243], v228 offset:30720\n"
"v_mfma_f32_16x16x32_bf16 v[152:155], a[228:231], a[140:143], v[152:155]\n"
"s_add_u32 m0, s66, 28672\n"
"s_add_u32 s62, s42, 110592\n"
"ds_read_b64_tr_b16 a[246:247], v229 offset:30720\n"
"v_mfma_f32_16x16x32_bf16 v[148:151], a[224:227], a[140:143], v[148:151]\n"
"s_waitcnt lgkmcnt(5)\n"
"buffer_load_dwordx4 v252, s[76:79], s62 offen lds\n"
"v_mfma_f32_16x16x32_bf16 v[160:163], a[224:227], a[144:147], v[160:163]\n"
"ds_read_b64_tr_b16 a[250:251], v230 offset:30720\n"
"v_mfma_f32_16x16x32_bf16 v[164:167], a[228:231], a[144:147], v[164:167]\n"
"ds_read_b64_tr_b16 a[86:87], v231 offset:30720\n"
"v_mfma_f32_16x16x32_bf16 v[168:171], a[232:235], a[144:147], v[168:171]\n"
"ds_read_b128 a[168:171], v227 offset:17408\n"
"v_mfma_f32_16x16x32_bf16 v[180:183], a[232:235], a[148:151], v[180:183]\n"
"s_add_u32 m0, s66, 29696\n"
"s_add_u32 s62, s42, 119808\n"
"ds_read_b128 a[172:175], v227 offset:18432\n"
"v_mfma_f32_16x16x32_bf16 v[176:179], a[228:231], a[148:151], v[176:179]\n"
"buffer_load_dwordx4 v252, s[76:79], s62 offen lds\n"
"v_mfma_f32_16x16x32_bf16 v[172:175], a[224:227], a[148:151], v[172:175]\n"
"ds_read_b128 a[176:179], v227 offset:19456\n"
"v_mfma_f32_16x16x32_bf16 v[184:187], a[224:227], a[152:155], v[184:187]\n"
"ds_read_b128 a[180:183], v227 offset:20480\n"
"v_mfma_f32_16x16x32_bf16 v[188:191], a[228:231], a[152:155], v[188:191]\n"
"s_waitcnt vmcnt(10)\n"
"buffer_store_dwordx4 v[76:79], %[tA], s[32:35], s90 offen offset:2048 sc1\n"
"v_mfma_f32_16x16x32_bf16 v[192:195], a[232:235], a[152:155], v[192:195]\n"
"; oproj step 27\n"
"s_waitcnt lgkmcnt(5)\n"
"v_cvt_pk_bf16_f32 v96, v96, v97\n"
"v_cvt_pk_bf16_f32 v97, v98, v99\n"
"v_mfma_f32_16x16x32_bf16 v[112:115], a[240:243], a[156:159], v[112:115]\n"
"s_add_u32 m0, s66, 30720\n"
"s_add_u32 s62, s42, 129024\n"
"ds_read_b64_tr_b16 a[224:225], v228 offset:32768\n"
"v_mfma_f32_16x16x32_bf16 v[116:119], a[244:247], a[156:159], v[116:119]\n"
"s_waitcnt lgkmcnt(5)\n"
"buffer_load_dwordx4 v252, s[76:79], s62 offen lds\n"
"v_mfma_f32_16x16x32_bf16 v[120:123], a[248:251], a[156:159], v[120:123]\n"
"ds_read_b64_tr_b16 a[228:229], v229 offset:32768\n"
"v_mfma_f32_16x16x32_bf16 v[132:135], a[248:251], a[160:163], v[132:135]\n"
"ds_read_b64_tr_b16 a[232:233], v230 offset:32768\n"
"v_mfma_f32_16x16x32_bf16 v[128:131], a[244:247], a[160:163], v[128:131]\n"
"s_waitcnt lgkmcnt(3)\n"
"ds_read_b64_tr_b16 a[104:105], v231 offset:32768\n"
"v_mfma_f32_16x16x32_bf16 v[124:127], a[240:243], a[160:163], v[124:127]\n"
"s_add_u32 m0, s66, 31744\n"
"s_add_u32 s62, s42, 138240\n"
"ds_read_b128 a[128:131], v224 offset:21504\n"
"v_mfma_f32_16x16x32_bf16 v[136:139], a[240:243], a[164:167], v[136:139]\n"
"buffer_load_dwordx4 v252, s[76:79], s62 offen lds\n"
"v_mfma_f32_16x16x32_bf16 v[140:143], a[244:247], a[164:167], v[140:143]\n"
"ds_read_b128 a[132:135], v224 offset:22528\n"
"v_mfma_f32_16x16x32_bf16 v[144:147], a[248:251], a[164:167], v[144:147]\n"
"ds_read_b128 a[136:139], v224 offset:23552\n"
"v_mfma_f32_16x16x32_bf16 v[156:159], a[248:251], a[168:171], v[156:159]\n"
"s_waitcnt vmcnt(10)\n"
"ds_read_b64_tr_b16 a[226:227], v228 offset:34816\n"
"v_mfma_f32_16x16x32_bf16 v[152:155], a[244:247], a[168:171], v[152:155]\n"
"s_add_u32 m0, s66, 32768\n"
"s_add_u32 s62, s43, 110592\n"
"ds_read_b64_tr_b16 a[230:231], v229 offset:34816\n"
"v_mfma_f32_16x16x32_bf16 v[148:151], a[240:243], a[168:171], v[148:151]\n"
"s_waitcnt lgkmcnt(5)\n"
"buffer_load_dwordx4 v252, s[76:79], s62 offen lds\n"
"v_mfma_f32_16x16x32_bf16 v[160:163], a[240:243], a[172:175], v[160:163]\n"
"ds_read_b64_tr_b16 a[234:235], v230 offset:34816\n"
"v_mfma_f32_16x16x32_bf16 v[164:167], a[244:247], a[172:175], v[164:167]\n"
"ds_read_b64_tr_b16 a[106:107], v231 offset:34816\n"
"v_mfma_f32_16x16x32_bf16 v[168:171], a[248:251], a[172:175], v[168:171]\n"
"ds_read_b128 a[140:143], v224 offset:24576\n"
"v_mfma_f32_16x16x32_bf16 v[180:183], a[248:251], a[176:179], v[180:183]\n"
"s_add_u32 m0, s66, 33792\n"
"s_add_u32 s62, s43, 119808\n"
"ds_read_b128 a[144:147], v224 offset:25600\n"
"v_mfma_f32_16x16x32_bf16 v[176:179], a[244:247], a[176:179], v[176:179]\n"
"buffer_load_dwordx4 v252, s[76:79], s62 offen lds\n"
"v_mfma_f32_16x16x32_bf16 v[172:175], a[240:243], a[176:179], v[172:175]\n"
"ds_read_b128 a[148:151], v224 offset:26624\n"
"v_mfma_f32_16x16x32_bf16 v[184:187], a[240:243], a[180:183], v[184:187]\n"
"ds_read_b128 a[152:155], v224 offset:27648\n"
"v_mfma_f32_16x16x32_bf16 v[188:191], a[244:247], a[180:183], v[188:191]\n"
"s_waitcnt vmcnt(10)\n"
"buffer_store_dwordx2 v[96:97], v244, s[32:35], s94 offen offset:3072 sc1\n"
"v_mfma_f32_16x16x32_bf16 v[192:195], a[248:251], a[180:183], v[192:195]\n"
"; oproj step 28\n"
"s_waitcnt lgkmcnt(5)\n"
"v_cvt_pk_bf16_f32 v100, v100, v101\n"
"v_cvt_pk_bf16_f32 v101, v102, v103\n"
"v_mfma_f32_16x16x32_bf16 v[112:115], a[224:227], a[128:131], v[112:115]\n"
"s_add_u32 m0, s66, 34816\n"
"s_add_u32 s62, s43, 129024\n"
"ds_read_b64_tr_b16 a[240:241], v228 offset:36864\n"
"v_mfma_f32_16x16x32_bf16 v[116:119], a[228:231], a[128:131], v[116:119]\n"
"s_waitcnt lgkmcnt(5)\n"
"buffer_load_dwordx4 v252, s[76:79], s62 offen lds\n"
"v_mfma_f32_16x16x32_bf16 v[120:123], a[232:235], a[128:131], v[120:123]\n"
"ds_read_b64_tr_b16 a[244:245], v229 offset:36864\n"
"v_mfma_f32_16x16x32_bf16 v[132:135], a[232:235], a[132:135], v[132:135]\n"
"ds_read_b64_tr_b16 a[248:249], v230 offset:36864\n"
"v_mfma_f32_16x16x32_bf16 v[128:131], a[228:231], a[132:135], v[128:131]\n"
"s_waitcnt lgkmcnt(3)\n"
"ds_read_b64_tr_b16 a[108:109], v231 offset:36864\n"
"v_mfma_f32_16x16x32_bf16 v[124:127], a[224:227], a[132:135], v[124:127]\n"
"s_add_u32 m0, s66, 35840\n"
"s_add_u32 s62, s43, 138240\n"
"ds_read_b128 a[156:159], v225 offset:21504\n"
"v_mfma_f32_16x16x32_bf16 v[136:139], a[224:227], a[136:139], v[136:139]\n"
"buffer_load_dwordx4 v252, s[76:79], s62 offen lds\n"
"v_mfma_f32_16x16x32_bf16 v[140:143], a[228:231], a[136:139], v[140:143]\n"
"ds_read_b128 a[160:163], v225 offset:22528\n"
"v_mfma_f32_16x16x32_bf16 v[144:147], a[232:235], a[136:139], v[144:147]\n"
"ds_read_b128 a[164:167], v225 offset:23552\n"
"v_mfma_f32_16x16x32_bf16 v[156:159], a[232:235], a[140:143], v[156:159]\n"
"s_waitcnt vmcnt(10)\n"
"ds_read_b64_tr_b16 a[242:243], v228 offset:38912\n"
"v_mfma_f32_16x16x32_bf16 v[152:155], a[228:231], a[140:143], v[152:155]\n"
"ds_read_b64_tr_b16 a[246:247], v229 offset:38912\n"
"v_mfma_f32_16x16x32_bf16 v[148:151], a[224:227], a[140:143], v[148:151]\n"
"ds_read_b64_tr_b16 a[250:251], v230 offset:38912\n"
"v_mfma_f32_16x16x32_bf16 v[160:163], a[224:227], a[144:147], v[160:163]\n"
"ds_read_b64_tr_b16 a[110:111], v231 offset:38912\n"
"v_mfma_f32_16x16x32_bf16 v[164:167], a[228:231], a[144:147], v[164:167]\n"
"ds_read_b128 a[168:171], v225 offset:24576\n"
"v_mfma_f32_16x16x32_bf16 v[168:171], a[232:235], a[144:147], v[168:171]\n"
"ds_read_b128 a[172:175], v225 offset:25600\n"
"v_mfma_f32_16x16x32_bf16 v[180:183], a[232:235], a[148:151], v[180:183]\n"
"ds_read_b128 a[176:179], v225 offset:26624\n"
"v_mfma_f32_16x16x32_bf16 v[176:179], a[228:231], a[148:151], v[176:179]\n"
"ds_read_b128 a[180:183], v225 offset:27648\n"
"v_mfma_f32_16x16x32_bf16 v[172:175], a[224:227], a[148:151], v[172:175]\n"
"buffer_store_dwordx2 v[100:101], v244, s[32:35], s95 offen offset:3072 sc1\n"
"v_mfma_f32_16x16x32_bf16 v[184:187], a[224:227], a[152:155], v[184:187]\n"
"v_cvt_pk_bf16_f32 v104, v104, v105\n"
"v_cvt_pk_bf16_f32 v105, v106, v107\n"
"v_mfma_f32_16x16x32_bf16 v[188:191], a[228:231], a[152:155], v[188:191]\n"
"s_waitcnt vmcnt(9)\n"
"buffer_store_dwordx2 v[104:105], v244, s[32:35], s88 offen offset:3072 sc1\n"
"v_mfma_f32_16x16x32_bf16 v[192:195], a[232:235], a[152:155], v[192:195]\n"
"; oproj step 29\n"
"s_waitcnt lgkmcnt(5)\n"
"v_cvt_pk_bf16_f32 v108, v108, v109\n"
"v_cvt_pk_bf16_f32 v109, v110, v111\n"
"v_mfma_f32_16x16x32_bf16 v[112:115], a[240:243], a[156:159], v[112:115]\n"
"ds_read_b64_tr_b16 a[224:225], v228 offset:28672\n"
"v_mfma_f32_16x16x32_bf16 v[116:119], a[244:247], a[156:159], v[116:119]\n"
"ds_read_b64_tr_b16 a[228:229], v229 offset:28672\n"
"v_mfma_f32_16x16x32_bf16 v[120:123], a[248:251], a[156:159], v[120:123]\n"
"ds_read_b64_tr_b16 a[232:233], v230 offset:28672\n"
"v_mfma_f32_16x16x32_bf16 v[132:135], a[248:251], a[160:163], v[132:135]\n"
"ds_read_b64_tr_b16 a[112:113], v231 offset:28672\n"
"v_mfma_f32_16x16x32_bf16 v[128:131], a[244:247], a[160:163], v[128:131]\n"
"s_waitcnt lgkmcnt(4)\n"
"ds_read_b128 a[128:131], v226 offset:21504\n"
"v_mfma_f32_16x16x32_bf16 v[124:127], a[240:243], a[160:163], v[124:127]\n"
"ds_read_b128 a[132:135], v226 offset:22528\n"
"v_mfma_f32_16x16x32_bf16 v[136:139], a[240:243], a[164:167], v[136:139]\n"
"ds_read_b128 a[136:139], v226 offset:23552\n"
"v_mfma_f32_16x16x32_bf16 v[140:143], a[244:247], a[164:167], v[140:143]\n"
"buffer_store_dwordx2 v[108:109], v244, s[32:35], s90 offen offset:3072 sc1\n"
"v_mfma_f32_16x16x32_bf16 v[144:147], a[248:251], a[164:167], v[144:147]\n"
"v_mfma_f32_16x16x32_bf16 v[156:159], a[248:251], a[168:171], v[156:159]\n"
"s_waitcnt vmcnt(8)\n"
"ds_read_b64_tr_b16 a[226:227], v228 offset:30720\n"
"v_mfma_f32_16x16x32_bf16 v[152:155], a[244:247], a[168:171], v[152:155]\n"
"ds_read_b64_tr_b16 a[230:231], v229 offset:30720\n"
"v_mfma_f32_16x16x32_bf16 v[148:151], a[240:243], a[168:171], v[148:151]\n"
"ds_read_b64_tr_b16 a[234:235], v230 offset:30720\n"
"v_mfma_f32_16x16x32_bf16 v[160:163], a[240:243], a[172:175], v[160:163]\n"
"ds_read_b64_tr_b16 a[114:115], v231 offset:30720\n"
"v_mfma_f32_16x16x32_bf16 v[164:167], a[244:247], a[172:175], v[164:167]\n"
"ds_read_b128 a[140:143], v226 offset:24576\n"
"v_mfma_f32_16x16x32_bf16 v[168:171], a[248:251], a[172:175], v[168:171]\n"
"ds_read_b128 a[144:147], v226 offset:25600\n"
"v_mfma_f32_16x16x32_bf16 v[180:183], a[248:251], a[176:179], v[180:183]\n"
"ds_read_b128 a[148:151], v226 offset:26624\n"
"v_mfma_f32_16x16x32_bf16 v[176:179], a[244:247], a[176:179], v[176:179]\n"
"ds_read_b128 a[152:155], v226 offset:27648\n"
"v_mfma_f32_16x16x32_bf16 v[172:175], a[240:243], a[176:179], v[172:175]\n"
"v_mfma_f32_16x16x32_bf16 v[184:187], a[240:243], a[180:183], v[184:187]\n"
"v_mfma_f32_16x16x32_bf16 v[188:191], a[244:247], a[180:183], v[188:191]\n"
"s_waitcnt vmcnt(6)\n"
"v_mfma_f32_16x16x32_bf16 v[192:195], a[248:251], a[180:183], v[192:195]\n"
"; oproj step 30\n"
"s_waitcnt lgkmcnt(5)\n"
"v_mfma_f32_16x16x32_bf16 v[112:115], a[224:227], a[128:131], v[112:115]\n"
"ds_read_b64_tr_b16 a[240:241], v228 offset:32768\n"
"v_mfma_f32_16x16x32_bf16 v[116:119], a[228:231], a[128:131], v[116:119]\n"
"ds_read_b64_tr_b16 a[244:245], v229 offset:32768\n"
"v_mfma_f32_16x16x32_bf16 v[120:123], a[232:235], a[128:131], v[120:123]\n"
"ds_read_b64_tr_b16 a[248:249], v230 offset:32768\n"
"v_mfma_f32_16x16x32_bf16 v[132:135], a[232:235], a[132:135], v[132:135]\n"
"ds_read_b64_tr_b16 a[116:117], v231 offset:32768\n"
"v_mfma_f32_16x16x32_bf16 v[128:131], a[228:231], a[132:135], v[128:131]\n"
"s_waitcnt lgkmcnt(4)\n"
"ds_read_b128 a[156:159], v227 offset:21504\n"
"v_mfma_f32_16x16x32_bf16 v[124:127], a[224:227], a[132:135], v[124:127]\n"
"ds_read_b128 a[160:163], v227 offset:22528\n"
"v_mfma_f32_16x16x32_bf16 v[136:139], a[224:227], a[136:139], v[136:139]\n"
"ds_read_b128 a[164:167], v227 offset:23552\n"
"v_mfma_f32_16x16x32_bf16 v[140:143], a[228:231], a[136:139], v[140:143]\n"
"v_mfma_f32_16x16x32_bf16 v[144:147], a[232:235], a[136:139], v[144:147]\n"
"v_mfma_f32_16x16x32_bf16 v[156:159], a[232:235], a[140:143], v[156:159]\n"
"s_waitcnt vmcnt(3)\n"
"ds_read_b64_tr_b16 a[242:243], v228 offset:34816\n"
"v_mfma_f32_16x16x32_bf16 v[152:155], a[228:231], a[140:143], v[152:155]\n"
"ds_read_b64_tr_b16 a[246:247], v229 offset:34816\n"
"v_mfma_f32_16x16x32_bf16 v[148:151], a[224:227], a[140:143], v[148:151]\n"
"ds_read_b64_tr_b16 a[250:251], v230 offset:34816\n"
"v_mfma_f32_16x16x32_bf16 v[160:163], a[224:227], a[144:147], v[160:163]\n"
"ds_read_b64_tr_b16 a[118:119], v231 offset:34816\n"
"v_mfma_f32_16x16x32_bf16 v[164:167], a[228:231], a[144:147], v[164:167]\n"
"ds_read_b128 a[168:171], v227 offset:24576\n"
"v_mfma_f32_16x16x32_bf16 v[168:171], a[232:235], a[144:147], v[168:171]\n"
"ds_read_b128 a[172:175], v227 offset:25600\n"
"v_mfma_f32_16x16x32_bf16 v[180:183], a[232:235], a[148:151], v[180:183]\n"
"ds_read_b128 a[176:179], v227 offset:26624\n"
"v_mfma_f32_16x16x32_bf16 v[176:179], a[228:231], a[148:151], v[176:179]\n"
"ds_read_b128 a[180:183], v227 offset:27648\n"
"v_mfma_f32_16x16x32_bf16 v[172:175], a[224:227], a[148:151], v[172:175]\n"
"v_mfma_f32_16x16x32_bf16 v[184:187], a[224:227], a[152:155], v[184:187]\n"
"v_mfma_f32_16x16x32_bf16 v[188:191], a[228:231], a[152:155], v[188:191]\n"
"v_mfma_f32_16x16x32_bf16 v[192:195], a[232:235], a[152:155], v[192:195]\n"
"; oproj step 31\n"
"s_waitcnt lgkmcnt(5)\n"
"v_mfma_f32_16x16x32_bf16 v[112:115], a[240:243], a[156:159], v[112:115]\n"
"ds_read_b128 a[128:131], v224 offset:0\n"
"v_mfma_f32_16x16x32_bf16 v[116:119], a[244:247], a[156:159], v[116:119]\n"
"v_mfma_f32_16x16x32_bf16 v[120:123], a[248:251], a[156:159], v[120:123]\n"
"ds_read_b128 a[132:135], v225 offset:0\n"
"v_mfma_f32_16x16x32_bf16 v[132:135], a[248:251], a[160:163], v[132:135]\n"
"v_mfma_f32_16x16x32_bf16 v[128:131], a[244:247], a[160:163], v[128:131]\n"
"s_waitcnt lgkmcnt(2)\n"
"ds_read_b128 a[136:139], v226 offset:0\n"
"v_mfma_f32_16x16x32_bf16 v[124:127], a[240:243], a[160:163], v[124:127]\n"
"v_mfma_f32_16x16x32_bf16 v[136:139], a[240:243], a[164:167], v[136:139]\n"
"ds_read_b128 a[140:143], v227 offset:0\n"
"v_mfma_f32_16x16x32_bf16 v[140:143], a[244:247], a[164:167], v[140:143]\n"
"v_mfma_f32_16x16x32_bf16 v[144:147], a[248:251], a[164:167], v[144:147]\n"
"ds_read_b128 a[144:147], v224 offset:7168\n"
"v_mfma_f32_16x16x32_bf16 v[156:159], a[248:251], a[168:171], v[156:159]\n"
"v_mfma_f32_16x16x32_bf16 v[152:155], a[244:247], a[168:171], v[152:155]\n"
"ds_read_b128 a[148:151], v225 offset:7168\n"
"v_cvt_pk_bf16_f32 v112, v112, v113\n"
"v_mfma_f32_16x16x32_bf16 v[148:151], a[240:243], a[168:171], v[148:151]\n"
"v_cvt_pk_bf16_f32 v113, v114, v115\n"
"v_cvt_pk_bf16_f32 v114, v124, v125\n"
"v_mfma_f32_16x16x32_bf16 v[160:163], a[240:243], a[172:175], v[160:163]\n"
"ds_read_b128 a[152:155], v226 offset:7168\n"
"v_cvt_pk_bf16_f32 v115, v126, v127\n"
"v_mfma_f32_16x16x32_bf16 v[164:167], a[244:247], a[172:175], v[164:167]\n"
"buffer_store_dwordx4 v[112:115], %[tA], s[32:35], s91 offen sc1\n"
"v_mfma_f32_16x16x32_bf16 v[168:171], a[248:251], a[172:175], v[168:171]\n"
"ds_read_b128 a[24:27], v227 offset:7168\n"
"v_mfma_f32_16x16x32_bf16 v[180:183], a[248:251], a[176:179], v[180:183]\n"
"v_mfma_f32_16x16x32_bf16 v[176:179], a[244:247], a[176:179], v[176:179]\n"
"ds_read_b128 a[28:31], v224 offset:14336\n"
"v_mfma_f32_16x16x32_bf16 v[172:175], a[240:243], a[176:179], v[172:175]\n"
"v_mfma_f32_16x16x32_bf16 v[184:187], a[240:243], a[180:183], v[184:187]\n"
"ds_read_b128 a[56:59], v225 offset:14336\n"
"v_mfma_f32_16x16x32_bf16 v[188:191], a[244:247], a[180:183], v[188:191]\n"
"v_mfma_f32_16x16x32_bf16 v[192:195], a[248:251], a[180:183], v[192:195]\n"
"s_waitcnt lgkmcnt(6)\n"
"; pass E\n"
"v_mfma_f32_16x16x32_bf16 v[0:3], a[8:11], a[128:131], 0\n"
"ds_read_b128 a[60:63], v226 offset:14336\n"
"v_mfma_f32_16x16x32_bf16 v[0:3], a[12:15], a[132:135], v[0:3]\n"
"s_waitcnt vmcnt(1)\n"
"s_barrier\n"
"v_mov_b32 v248, 1\n"
"v_mfma_f32_16x16x32_bf16 v[0:3], a[16:19], a[136:139], v[0:3]\n"
"ds_read_b128 a[88:91], v227 offset:14336\n"
"v_mfma_f32_16x16x32_bf16 v[0:3], a[20:23], a[140:143], v[0:3]\n"
"buffer_store_dword v248, v243, s[32:35], s54 offen sc1\n"
"v_mfma_f32_16x16x32_bf16 v[196:199], a[192:195], a[128:131], 0\n"
"ds_read_b128 a[92:95], v224 offset:21504\n"
"v_mfma_f32_16x16x32_bf16 v[196:199], a[196:199], a[132:135], v[196:199]\n"
"v_mfma_f32_16x16x32_bf16 v[196:199], a[200:203], a[136:139], v[196:199]\n"
"ds_read_b128 a[120:123], v225 offset:21504\n"
"v_mfma_f32_16x16x32_bf16 v[196:199], a[204:207], a[140:143], v[196:199]\n"
"s_waitcnt lgkmcnt(6)\n"
"v_mfma_f32_16x16x32_bf16 v[0:3], a[40:43], a[144:147], v[0:3]\n"
"ds_read_b128 a[124:127], v226 offset:21504\n"
"v_mfma_f32_16x16x32_bf16 v[0:3], a[44:47], a[148:151], v[0:3]\n"
"v_cvt_pk_bf16_f32 v116, v116, v117\n"
"v_cvt_pk_bf16_f32 v117, v118, v119\n"
"v_mfma_f32_16x16x32_bf16 v[0:3], a[48:51], a[152:155], v[0:3]\n"
"ds_read_b128 a[156:159], v227 offset:21504\n"
"v_cvt_pk_bf16_f32 v118, v128, v129\n"
"v_mfma_f32_16x16x32_bf16 v[0:3], a[52:55], a[24:27], v[0:3]\n"
"v_cvt_pk_bf16_f32 v119, v130, v131\n"
"v_mfma_f32_16x16x32_bf16 v[196:199], a[208:211], a[144:147], v[196:199]\n"
"ds_read_b128 a[160:163], v224 offset:1024\n"
"v_mfma_f32_16x16x32_bf16 v[196:199], a[212:215], a[148:151], v[196:199]\n"
"buffer_store_dwordx4 v[116:119], %[tA], s[32:35], s92 offen sc1\n"
"v_mfma_f32_16x16x32_bf16 v[196:199], a[216:219], a[152:155], v[196:199]\n"
"ds_read_b128 a[164:167], v225 offset:1024\n"
"v_mfma_f32_16x16x32_bf16 v[196:199], a[220:223], a[24:27], v[196:199]\n"
"s_waitcnt lgkmcnt(6)\n"
"v_mfma_f32_16x16x32_bf16 v[0:3], a[72:75], a[28:31], v[0:3]\n"
"ds_read_b128 a[168:171], v226 offset:1024\n"
"v_mfma_f32_16x16x32_bf16 v[0:3], a[76:79], a[56:59], v[0:3]\n"
"v_mfma_f32_16x16x32_bf16 v[0:3], a[80:83], a[60:63], v[0:3]\n"
"ds_read_b128 a[172:175], v227 offset:1024\n"
"v_mfma_f32_16x16x32_bf16 v[0:3], a[84:87], a[88:91], v[0:3]\n"
"v_mfma_f32_16x16x32_bf16 v[196:199], a[0:3], a[28:31], v[196:199]\n"
"ds_read_b128 a[176:179], v224 offset:8192\n"
"v_mfma_f32_16x16x32_bf16 v[196:199], a[4:7], a[56:59], v[196:199]\n"
"v_mfma_f32_16x16x32_bf16 v[196:199], a[32:35], a[60:63], v[196:199]\n"
"ds_read_b128 a[180:183], v225 offset:8192\n"
"v_mfma_f32_16x16x32_bf16 v[196:199], a[36:39], a[88:91], v[196:199]\n"
"s_waitcnt lgkmcnt(6)\n"
"v_cvt_pk_bf16_f32 v120, v120, v121\n"
"v_cvt_pk_bf16_f32 v121, v122, v123\n"
"v_mfma_f32_16x16x32_bf16 v[0:3], a[104:107], a[92:95], v[0:3]\n"
"ds_read_b128 a[128:131], v226 offset:8192\n"
"v_cvt_pk_bf16_f32 v122, v132, v133\n"
"v_mfma_f32_16x16x32_bf16 v[0:3], a[108:111], a[120:123], v[0:3]\n"
"v_cvt_pk_bf16_f32 v123, v134, v135\n"
"v_mfma_f32_16x16x32_bf16 v[0:3], a[112:115], a[124:127], v[0:3]\n"
"ds_read_b128 a[132:135], v227 offset:8192\n"
"v_mfma_f32_16x16x32_bf16 v[0:3], a[116:119], a[156:159], v[0:3]\n"
"buffer_store_dwordx4 v[120:123], %[tA], s[32:35], s93 offen sc1\n"
"v_mfma_f32_16x16x32_bf16 v[196:199], a[64:67], a[92:95], v[196:199]\n"
"ds_read_b128 a[136:139], v224 offset:15360\n"
"v_mfma_f32_16x16x32_bf16 v[196:199], a[68:71], a[120:123], v[196:199]\n"
"v_mfma_f32_16x16x32_bf16 v[196:199], a[96:99], a[124:127], v[196:199]\n"
"ds_read_b128 a[140:143], v225 offset:15360\n"
"v_mfma_f32_16x16x32_bf16 v[196:199], a[100:103], a[156:159], v[196:199]\n"
"s_waitcnt lgkmcnt(6)\n"
"v_mfma_f32_16x16x32_bf16 v[4:7], a[8:11], a[160:163], 0\n"
"ds_read_b128 a[144:147], v226 offset:15360\n"
"v_mfma_f32_16x16x32_bf16 v[4:7], a[12:15], a[164:167], v[4:7]\n"
"v_mfma_f32_16x16x32_bf16 v[4:7], a[16:19], a[168:171], v[4:7]\n"
"ds_read_b128 a[148:151], v227 offset:15360\n"
"v_mfma_f32_16x16x32_bf16 v[4:7], a[20:23], a[172:175], v[4:7]\n"
"v_mfma_f32_16x16x32_bf16 v[200:203], a[192:195], a[160:163], 0\n"
"ds_read_b128 a[152:155], v224 offset:22528\n"
"v_mfma_f32_16x16x32_bf16 v[200:203], a[196:199], a[164:167], v[200:203]\n"
"v_cvt_pk_bf16_f32 v136, v136, v137\n"
"v_cvt_pk_bf16_f32 v137, v138, v139\n"
"v_mfma_f32_16x16x32_bf16 v[200:203], a[200:203], a[168:171], v[200:203]\n"
"ds_read_b128 a[24:27], v225 offset:22528\n"
"v_cvt_pk_bf16_f32 v138, v148, v149\n"
"v_mfma_f32_16x16x32_bf16 v[200:203], a[204:207], a[172:175], v[200:203]\n"
"s_waitcnt lgkmcnt(6)\n"
"v_cvt_pk_bf16_f32 v139, v150, v151\n"
"v_mfma_f32_16x16x32_bf16 v[4:7], a[40:43], a[176:179], v[4:7]\n"
"ds_read_b128 a[28:31], v226 offset:22528\n"
"v_mfma_f32_16x16x32_bf16 v[4:7], a[44:47], a[180:183], v[4:7]\n"
"buffer_store_dwordx4 v[136:139], %[tA], s[32:35], s91 offen offset:1024 sc1\n"
"v_mfma_f32_16x16x32_bf16 v[4:7], a[48:51], a[128:131], v[4:7]\n"
"ds_read_b128 a[56:59], v227 offset:22528\n"
"v_mfma_f32_16x16x32_bf16 v[4:7], a[52:55], a[132:135], v[4:7]\n"
"v_mfma_f32_16x16x32_bf16 v[200:203], a[208:211], a[176:179], v[200:203]\n"
"ds_read_b128 a[60:63], v224 offset:2048\n"
"v_mfma_f32_16x16x32_bf16 v[200:203], a[212:215], a[180:183], v[200:203]\n"
"v_mfma_f32_16x16x32_bf16 v[200:203], a[216:219], a[128:131], v[200:203]\n"
"ds_read_b128 a[88:91], v225 offset:2048\n"
"v_mfma_f32_16x16x32_bf16 v[200:203], a[220:223], a[132:135], v[200:203]\n"
"s_waitcnt lgkmcnt(6)\n"
"v_mfma_f32_16x16x32_bf16 v[4:7], a[72:75], a[136:139], v[4:7]\n"
"ds_read_b128 a[92:95], v226 offset:2048\n"
"v_mfma_f32_16x16x32_bf16 v[4:7], a[76:79], a[140:143], v[4:7]\n"
"v_mfma_f32_16x16x32_bf16 v[4:7], a[80:83], a[144:147], v[4:7]\n"
"ds_read_b128 a[120:123], v227 offset:2048\n"
"v_mfma_f32_16x16x32_bf16 v[4:7], a[84:87], a[148:151], v[4:7]\n"
"v_cvt_pk_bf16_f32 v140, v140, v141\n"
"v_cvt_pk_bf16_f32 v141, v142, v143\n"
"v_mfma_f32_16x16x32_bf16 v[200:203], a[0:3], a[136:139], v[200:203]\n"
"ds_read_b128 a[124:127], v224 offset:9216\n"
"v_cvt_pk_bf16_f32 v142, v152, v153\n"
"v_mfma_f32_16x16x32_bf16 v[200:203], a[4:7], a[140:143], v[200:203]\n"
"v_cvt_pk_bf16_f32 v143, v154, v155\n"
"v_mfma_f32_16x16x32_bf16 v[200:203], a[32:35], a[144:147], v[200:203]\n"
"ds_read_b128 a[156:159], v225 offset:9216\n"
"v_mfma_f32_16x16x32_bf16 v[200:203], a[36:39], a[148:151], v[200:203]\n"
"s_waitcnt lgkmcnt(6)\n"
"buffer_store_dwordx4 v[140:143], %[tA], s[32:35], s92 offen offset:1024 sc1\n"
"v_mfma_f32_16x16x32_bf16 v[4:7], a[104:107], a[152:155], v[4:7]\n"
"ds_read_b128 a[160:163], v226 offset:9216\n"
"v_mfma_f32_16x16x32_bf16 v[4:7], a[108:111], a[24:27], v[4:7]\n"
"v_mfma_f32_16x16x32_bf16 v[4:7], a[112:115], a[28:31], v[4:7]\n"
"ds_read_b128 a[164:167], v227 offset:9216\n"
"v_mfma_f32_16x16x32_bf16 v[4:7], a[116:119], a[56:59], v[4:7]\n"
"v_mfma_f32_16x16x32_bf16 v[200:203], a[64:67], a[152:155], v[200:203]\n"
"ds_read_b128 a[168:171], v224 offset:16384\n"
"v_mfma_f32_16x16x32_bf16 v[200:203], a[68:71], a[24:27], v[200:203]\n"
"v_mfma_f32_16x16x32_bf16 v[200:203], a[96:99], a[28:31], v[200:203]\n"
"ds_read_b128 a[172:175], v225 offset:16384\n"
"v_mfma_f32_16x16x32_bf16 v[200:203], a[100:103], a[56:59], v[200:203]\n"
"s_waitcnt lgkmcnt(6)\n"
"v_mfma_f32_16x16x32_bf16 v[8:11], a[8:11], a[60:63], 0\n"
"ds_read_b128 a[176:179], v226 offset:16384\n"
"v_mfma_f32_16x16x32_bf16 v[8:11], a[12:15], a[88:91], v[8:11]\n"
"v_cvt_pk_bf16_f32 v144, v144, v145\n"
"v_cvt_pk_bf16_f32 v145, v146, v147\n"
"v_mfma_f32_16x16x32_bf16 v[8:11], a[16:19], a[92:95], v[8:11]\n"
"ds_read_b128 a[180:183], v227 offset:16384\n"
"v_cvt_pk_bf16_f32 v146, v156, v157\n"
"v_mfma_f32_16x16x32_bf16 v[8:11], a[20:23], a[120:123], v[8:11]\n"
"v_cvt_pk_bf16_f32 v147, v158, v159\n"
"v_mfma_f32_16x16x32_bf16 v[204:207], a[192:195], a[60:63], 0\n"
"ds_read_b128 a[128:131], v224 offset:23552\n"
"v_mfma_f32_16x16x32_bf16 v[204:207], a[196:199], a[88:91], v[204:207]\n"
"buffer_store_dwordx4 v[144:147], %[tA], s[32:35], s93 offen offset:1024 sc1\n"
"v_mfma_f32_16x16x32_bf16 v[204:207], a[200:203], a[92:95], v[204:207]\n"
"ds_read_b128 a[132:135], v225 offset:23552\n"
"v_mfma_f32_16x16x32_bf16 v[204:207], a[204:207], a[120:123], v[204:207]\n"
"s_waitcnt lgkmcnt(6)\n"
"v_mfma_f32_16x16x32_bf16 v[8:11], a[40:43], a[124:127], v[8:11]\n"
"ds_read_b128 a[136:139], v226 offset:23552\n"
"v_mfma_f32_16x16x32_bf16 v[8:11], a[44:47], a[156:159], v[8:11]\n"
"v_mfma_f32_16x16x32_bf16 v[8:11], a[48:51], a[160:163], v[8:11]\n"
"ds_read_b128 a[140:143], v227 offset:23552\n"
"v_mfma_f32_16x16x32_bf16 v[8:11], a[52:55], a[164:167], v[8:11]\n"
"v_mfma_f32_16x16x32_bf16 v[204:207], a[208:211], a[124:127], v[204:207]\n"
"ds_read_b128 a[144:147], v224 offset:3072\n"
"v_mfma_f32_16x16x32_bf16 v[204:207], a[212:215], a[156:159], v[204:207]\n"
"v_mfma_f32_16x16x32_bf16 v[204:207], a[216:219], a[160:163], v[204:207]\n"
"ds_read_b128 a[148:151], v225 offset:3072\n"
"v_mfma_f32_16x16x32_bf16 v[204:207], a[220:223], a[164:167], v[204:207]\n"
"s_waitcnt lgkmcnt(6)\n"
"v_cvt_pk_bf16_f32 v160, v160, v161\n"
"v_cvt_pk_bf16_f32 v161, v162, v163\n"
"v_mfma_f32_16x16x32_bf16 v[8:11], a[72:75], a[168:171], v[8:11]\n"
"ds_read_b128 a[152:155], v226 offset:3072\n"
"v_cvt_pk_bf16_f32 v162, v172, v173\n"
"v_mfma_f32_16x16x32_bf16 v[8:11], a[76:79], a[172:175], v[8:11]\n"
"v_cvt_pk_bf16_f32 v163, v174, v175\n"
"v_mfma_f32_16x16x32_bf16 v[8:11], a[80:83], a[176:179], v[8:11]\n"
"ds_read_b128 a[24:27], v227 offset:3072\n"
"v_mfma_f32_16x16x32_bf16 v[8:11], a[84:87], a[180:183], v[8:11]\n"
"buffer_store_dwordx4 v[160:163], %[tA], s[32:35], s91 offen offset:2048 sc1\n"
"v_mfma_f32_16x16x32_bf16 v[204:207], a[0:3], a[168:171], v[204:207]\n"
"ds_read_b128 a[28:31], v224 offset:10240\n"
"v_mfma_f32_16x16x32_bf16 v[204:207], a[4:7], a[172:175], v[204:207]\n"
"v_mfma_f32_16x16x32_bf16 v[204:207], a[32:35], a[176:179], v[204:207]\n"
"ds_read_b128 a[56:59], v225 offset:10240\n"
"v_mfma_f32_16x16x32_bf16 v[204:207], a[36:39], a[180:183], v[204:207]\n"
"s_waitcnt lgkmcnt(6)\n"
"v_mfma_f32_16x16x32_bf16 v[8:11], a[104:107], a[128:131], v[8:11]\n"
"ds_read_b128 a[60:63], v226 offset:10240\n"
"v_mfma_f32_16x16x32_bf16 v[8:11], a[108:111], a[132:135], v[8:11]\n"
"v_mfma_f32_16x16x32_bf16 v[8:11], a[112:115], a[136:139], v[8:11]\n"
"ds_read_b128 a[88:91], v227 offset:10240\n"
"v_mfma_f32_16x16x32_bf16 v[8:11], a[116:119], a[140:143], v[8:11]\n"
"v_mfma_f32_16x16x32_bf16 v[204:207], a[64:67], a[128:131], v[204:207]\n"
"ds_read_b128 a[92:95], v224 offset:17408\n"
"v_mfma_f32_16x16x32_bf16 v[204:207], a[68:71], a[132:135], v[204:207]\n"
"v_cvt_pk_bf16_f32 v164, v164, v165\n"
"v_cvt_pk_bf16_f32 v165, v166, v167\n"
"v_mfma_f32_16x16x32_bf16 v[204:207], a[96:99], a[136:139], v[204:207]\n"
"ds_read_b128 a[120:123], v225 offset:17408\n"
"v_cvt_pk_bf16_f32 v166, v176, v177\n"
"v_mfma_f32_16x16x32_bf16 v[204:207], a[100:103], a[140:143], v[204:207]\n"
"s_waitcnt lgkmcnt(6)\n"
"v_cvt_pk_bf16_f32 v167, v178, v179\n"
"v_mfma_f32_16x16x32_bf16 v[12:15], a[8:11], a[144:147], 0\n"
"ds_read_b128 a[124:127], v226 offset:17408\n"
"v_mfma_f32_16x16x32_bf16 v[12:15], a[12:15], a[148:151], v[12:15]\n"
"buffer_store_dwordx4 v[164:167], %[tA], s[32:35], s92 offen offset:2048 sc1\n"
"v_mfma_f32_16x16x32_bf16 v[12:15], a[16:19], a[152:155], v[12:15]\n"
"ds_read_b128 a[156:159], v227 offset:17408\n"
"v_mfma_f32_16x16x32_bf16 v[12:15], a[20:23], a[24:27], v[12:15]\n"
"v_mfma_f32_16x16x32_bf16 v[208:211], a[192:195], a[144:147], 0\n"
"ds_read_b128 a[160:163], v224 offset:24576\n"
"v_mfma_f32_16x16x32_bf16 v[208:211], a[196:199], a[148:151], v[208:211]\n"
"v_mfma_f32_16x16x32_bf16 v[208:211], a[200:203], a[152:155], v[208:211]\n"
"ds_read_b128 a[164:167], v225 offset:24576\n"
"v_mfma_f32_16x16x32_bf16 v[208:211], a[204:207], a[24:27], v[208:211]\n"
"s_waitcnt lgkmcnt(6)\n"
"v_mfma_f32_16x16x32_bf16 v[12:15], a[40:43], a[28:31], v[12:15]\n"
"ds_read_b128 a[168:171], v226 offset:24576\n"
"v_mfma_f32_16x16x32_bf16 v[12:15], a[44:47], a[56:59], v[12:15]\n"
"v_mfma_f32_16x16x32_bf16 v[12:15], a[48:51], a[60:63], v[12:15]\n"
"ds_read_b128 a[172:175], v227 offset:24576\n"
"v_mfma_f32_16x16x32_bf16 v[12:15], a[52:55], a[88:91], v[12:15]\n"
"v_cvt_pk_bf16_f32 v168, v168, v169\n"
"v_cvt_pk_bf16_f32 v169, v170, v171\n"
"v_mfma_f32_16x16x32_bf16 v[208:211], a[208:211], a[28:31], v[208:211]\n"
"ds_read_b128 a[176:179], v224 offset:4096\n"
"v_cvt_pk_bf16_f32 v170, v180, v181\n"
"v_mfma_f32_16x16x32_bf16 v[208:211], a[212:215], a[56:59], v[208:211]\n"
"v_cvt_pk_bf16_f32 v171, v182, v183\n"
"v_mfma_f32_16x16x32_bf16 v[208:211], a[216:219], a[60:63], v[208:211]\n"
"ds_read_b128 a[180:183], v225 offset:4096\n"
"v_mfma_f32_16x16x32_bf16 v[208:211], a[220:223], a[88:91], v[208:211]\n"
"s_waitcnt lgkmcnt(6)\n"
"buffer_store_dwordx4 v[168:171], %[tA], s[32:35], s93 offen offset:2048 sc1\n"
"Lh12:\n"
"s_cbranch_execz Lw3\n"
"v_mfma_f32_16x16x32_bf16 v[12:15], a[72:75], a[92:95], v[12:15]\n"
"ds_read_b128 a[128:131], v226 offset:4096\n"
"v_mfma_f32_16x16x32_bf16 v[12:15], a[76:79], a[120:123], v[12:15]\n"
"v_mfma_f32_16x16x32_bf16 v[12:15], a[80:83], a[124:127], v[12:15]\n"
"ds_read_b128 a[132:135], v227 offset:4096\n"
"v_mfma_f32_16x16x32_bf16 v[12:15], a[84:87], a[156:159], v[12:15]\n"
"v_mfma_f32_16x16x32_bf16 v[208:211], a[0:3], a[92:95], v[208:211]\n"
"ds_read_b128 a[136:139], v224 offset:11264\n"
"v_mfma_f32_16x16x32_bf16 v[208:211], a[4:7], a[120:123], v[208:211]\n"
"v_mfma_f32_16x16x32_bf16 v[208:211], a[32:35], a[124:127], v[208:211]\n"
"ds_read_b128 a[140:143], v225 offset:11264\n"
"v_mfma_f32_16x16x32_bf16 v[208:211], a[36:39], a[156:159], v[208:211]\n"
"s_waitcnt lgkmcnt(6)\n"
"v_mfma_f32_16x16x32_bf16 v[12:15], a[104:107], a[160:163], v[12:15]\n"
"ds_read_b128 a[144:147], v226 offset:11264\n"
"v_mfma_f32_16x16x32_bf16 v[12:15], a[108:111], a[164:167], v[12:15]\n"
"v_cvt_pk_bf16_f32 v184, v184, v185\n"
"v_cvt_pk_bf16_f32 v185, v186, v187\n"
"v_mfma_f32_16x16x32_bf16 v[12:15], a[112:115], a[168:171], v[12:15]\n"
"ds_read_b128 a[148:151], v227 offset:11264\n"
"v_mfma_f32_16x16x32_bf16 v[12:15], a[116:119], a[172:175], v[12:15]\n"
"buffer_store_dwordx2 v[184:185], v244, s[32:35], s91 offen offset:3072 sc1\n"
"v_mfma_f32_16x16x32_bf16 v[208:211], a[64:67], a[160:163], v[208:211]\n"
"ds_read_b128 a[152:155], v224 offset:18432\n"
"v_mfma_f32_16x16x32_bf16 v[208:211], a[68:71], a[164:167], v[208:211]\n"
"v_mfma_f32_16x16x32_bf16 v[208:211], a[96:99], a[168:171], v[208:211]\n"
"ds_read_b128 a[24:27], v225 offset:18432\n"
"v_mfma_f32_16x16x32_bf16 v[208:211], a[100:103], a[172:175], v[208:211]\n"
"s_waitcnt lgkmcnt(6)\n"
"v_mfma_f32_16x16x32_bf16 v[16:19], a[8:11], a[176:179], 0\n"
"ds_read_b128 a[28:31], v226 offset:18432\n"
"v_cvt_pk_bf16_f32 v188, v188, v189\n"
"v_mfma_f32_16x16x32_bf16 v[16:19], a[12:15], a[180:183], v[16:19]\n"
"v_cvt_pk_bf16_f32 v189, v190, v191\n"
"v_mfma_f32_16x16x32_bf16 v[16:19], a[16:19], a[128:131], v[16:19]\n"
"ds_read_b128 a[56:59], v227 offset:18432\n"
"v_mfma_f32_16x16x32_bf16 v[16:19], a[20:23], a[132:135], v[16:19]\n"
"buffer_store_dwordx2 v[188:189], v244, s[32:35], s92 offen offset:3072 sc1\n"
"v_mfma_f32_16x16x32_bf16 v[212:215], a[192:195], a[176:179], 0\n"
"ds_read_b128 a[60:63], v224 offset:25600\n"
"v_mfma_f32_16x16x32_bf16 v[212:215], a[196:199], a[180:183], v[212:215]\n"
"v_mfma_f32_16x16x32_bf16 v[212:215], a[200:203], a[128:131], v[212:215]\n"
"ds_read_b128 a[88:91], v225 offset:25600\n"
"v_mfma_f32_16x16x32_bf16 v[212:215], a[204:207], a[132:135], v[212:215]\n"
"s_waitcnt lgkmcnt(6)\n"
"v_cvt_pk_bf16_f32 v192, v192, v193\n"
"v_cvt_pk_bf16_f32 v193, v194, v195\n"
"v_mfma_f32_16x16x32_bf16 v[16:19], a[40:43], a[136:139], v[16:19]\n"
"ds_read_b128 a[92:95], v226 offset:25600\n"
"v_mfma_f32_16x16x32_bf16 v[16:19], a[44:47], a[140:143], v[16:19]\n"
"buffer_store_dwordx2 v[192:193], v244, s[32:35], s93 offen offset:3072 sc1\n"
"v_mfma_f32_16x16x32_bf16 v[16:19], a[48:51], a[144:147], v[16:19]\n"
"ds_read_b128 a[120:123], v227 offset:25600\n"
"v_mfma_f32_16x16x32_bf16 v[16:19], a[52:55], a[148:151], v[16:19]\n"
"v_mfma_f32_16x16x32_bf16 v[212:215], a[208:211], a[136:139], v[212:215]\n"
"ds_read_b128 a[124:127], v224 offset:5120\n"
"v_mfma_f32_16x16x32_bf16 v[212:215], a[212:215], a[140:143], v[212:215]\n"
"v_mfma_f32_16x16x32_bf16 v[212:215], a[216:219], a[144:147], v[212:215]\n"
"ds_read_b128 a[156:159], v225 offset:5120\n"
"v_cvt_pk_bf16_f32 v0, v0, v1\n"
"v_mfma_f32_16x16x32_bf16 v[212:215], a[220:223], a[148:151], v[212:215]\n"
"s_waitcnt lgkmcnt(6)\n"
"v_cvt_pk_bf16_f32 v1, v2, v3\n"
"v_cvt_pk_bf16_f32 v2, v4, v5\n"
"v_mfma_f32_16x16x32_bf16 v[16:19], a[72:75], a[152:155], v[16:19]\n"
"ds_read_b128 a[160:163], v226 offset:5120\n"
"v_cvt_pk_bf16_f32 v3, v6, v7\n"
"v_mfma_f32_16x16x32_bf16 v[16:19], a[76:79], a[24:27], v[16:19]\n"
"buffer_store_dwordx4 v[0:3], %[tA], s[32:35], s86 offen sc1\n"
"v_mfma_f32_16x16x32_bf16 v[16:19], a[80:83], a[28:31], v[16:19]\n"
"ds_read_b128 a[164:167], v227 offset:5120\n"
"v_mfma_f32_16x16x32_bf16 v[16:19], a[84:87], a[56:59], v[16:19]\n"
"v_mfma_f32_16x16x32_bf16 v[212:215], a[0:3], a[152:155], v[212:215]\n"
"ds_read_b128 a[168:171], v224 offset:12288\n"
"v_mfma_f32_16x16x32_bf16 v[212:215], a[4:7], a[24:27], v[212:215]\n"
"v_mfma_f32_16x16x32_bf16 v[212:215], a[32:35], a[28:31], v[212:215]\n"
"ds_read_b128 a[172:175], v225 offset:12288\n"
"v_mfma_f32_16x16x32_bf16 v[212:215], a[36:39], a[56:59], v[212:215]\n"
"s_waitcnt lgkmcnt(6)\n"
"v_mfma_f32_16x16x32_bf16 v[16:19], a[104:107], a[60:63], v[16:19]\n"
"ds_read_b128 a[176:179], v226 offset:12288\n"
"v_mfma_f32_16x16x32_bf16 v[16:19], a[108:111], a[88:91], v[16:19]\n"
"v_mfma_f32_16x16x32_bf16 v[16:19], a[112:115], a[92:95], v[16:19]\n"
"ds_read_b128 a[180:183], v227 offset:12288\n"
"v_mfma_f32_16x16x32_bf16 v[16:19], a[116:119], a[120:123], v[16:19]\n"
"v_mfma_f32_16x16x32_bf16 v[212:215], a[64:67], a[60:63], v[212:215]\n"
"ds_read_b128 a[128:131], v224 offset:19456\n"
"v_cvt_pk_bf16_f32 v8, v8, v9\n"
"v_mfma_f32_16x16x32_bf16 v[212:215], a[68:71], a[88:91], v[212:215]\n"
"v_cvt_pk_bf16_f32 v9, v10, v11\n"
"v_cvt_pk_bf16_f32 v10, v12, v13\n"
"v_mfma_f32_16x16x32_bf16 v[212:215], a[96:99], a[92:95], v[212:215]\n"
"ds_read_b128 a[132:135], v225 offset:19456\n"
"v_cvt_pk_bf16_f32 v11, v14, v15\n"
"v_mfma_f32_16x16x32_bf16 v[212:215], a[100:103], a[120:123], v[212:215]\n"
"s_waitcnt lgkmcnt(6)\n"
"buffer_store_dwordx4 v[8:11], %[tA], s[32:35], s86 offen offset:1024 sc1\n"
"v_mfma_f32_16x16x32_bf16 v[20:23], a[8:11], a[124:127], 0\n"
"ds_read_b128 a[136:139], v226 offset:19456\n"
"v_mfma_f32_16x16x32_bf16 v[20:23], a[12:15], a[156:159], v[20:23]\n"
"v_mfma_f32_16x16x32_bf16 v[20:23], a[16:19], a[160:163], v[20:23]\n"
"ds_read_b128 a[140:143], v227 offset:19456\n"
"v_mfma_f32_16x16x32_bf16 v[20:23], a[20:23], a[164:167], v[20:23]\n"
"v_mfma_f32_16x16x32_bf16 v[216:219], a[192:195], a[124:127], 0\n"
"ds_read_b128 a[144:147], v224 offset:26624\n"
"v_mfma_f32_16x16x32_bf16 v[216:219], a[196:199], a[156:159], v[216:219]\n"
"v_mfma_f32_16x16x32_bf16 v[216:219], a[200:203], a[160:163], v[216:219]\n"
"ds_read_b128 a[148:151], v225 offset:26624\n"
"v_mfma_f32_16x16x32_bf16 v[216:219], a[204:207], a[164:167], v[216:219]\n"
"s_waitcnt lgkmcnt(6)\n"
"v_mfma_f32_16x16x32_bf16 v[20:23], a[40:43], a[168:171], v[20:23]\n"
"ds_read_b128 a[152:155], v226 offset:26624\n"
"v_mfma_f32_16x16x32_bf16 v[20:23], a[44:47], a[172:175], v[20:23]\n"
"v_mfma_f32_16x16x32_bf16 v[20:23], a[48:51], a[176:179], v[20:23]\n"
"ds_read_b128 a[24:27], v227 offset:26624\n"
"v_mfma_f32_16x16x32_bf16 v[20:23], a[52:55], a[180:183], v[20:23]\n"
"v_mfma_f32_16x16x32_bf16 v[216:219], a[208:211], a[168:171], v[216:219]\n"
"ds_read_b128 a[28:31], v224 offset:6144\n"
"v_mfma_f32_16x16x32_bf16 v[216:219], a[212:215], a[172:175], v[216:219]\n"
"v_mfma_f32_16x16x32_bf16 v[216:219], a[216:219], a[176:179], v[216:219]\n"
"ds_read_b128 a[56:59], v225 offset:6144\n"
"v_mfma_f32_16x16x32_bf16 v[216:219], a[220:223], a[180:183], v[216:219]\n"
"s_waitcnt lgkmcnt(6)\n"
"buffer_load_dword v250, v249, s[32:35], s53 offen sc1\n"
"v_mfma_f32_16x16x32_bf16 v[20:23], a[72:75], a[128:131], v[20:23]\n"
"ds_read_b128 a[60:63], v226 offset:6144\n"
"v_mfma_f32_16x16x32_bf16 v[20:23], a[76:79], a[132:135], v[20:23]\n"
"v_mfma_f32_16x16x32_bf16 v[20:23], a[80:83], a[136:139], v[20:23]\n"
"ds_read_b128 a[88:91], v227 offset:6144\n"
"v_mfma_f32_16x16x32_bf16 v[20:23], a[84:87], a[140:143], v[20:23]\n"
"v_mfma_f32_16x16x32_bf16 v[216:219], a[0:3], a[128:131], v[216:219]\n"
"ds_read_b128 a[92:95], v224 offset:13312\n"
"v_mfma_f32_16x16x32_bf16 v[216:219], a[4:7], a[132:135], v[216:219]\n"
"v_mfma_f32_16x16x32_bf16 v[216:219], a[32:35], a[136:139], v[216:219]\n"
"ds_read_b128 a[120:123], v225 offset:13312\n"
"v_mfma_f32_16x16x32_bf16 v[216:219], a[36:39], a[140:143], v[216:219]\n"
"s_waitcnt lgkmcnt(6)\n"
"v_mfma_f32_16x16x32_bf16 v[20:23], a[104:107], a[144:147], v[20:23]\n"
"ds_read_b128 a[124:127], v226 offset:13312\n"
"v_mfma_f32_16x16x32_bf16 v[20:23], a[108:111], a[148:151], v[20:23]\n"
"v_mfma_f32_16x16x32_bf16 v[20:23], a[112:115], a[152:155], v[20:23]\n"
"ds_read_b128 a[156:159], v227 offset:13312\n"
"v_mfma_f32_16x16x32_bf16 v[20:23], a[116:119], a[24:27], v[20:23]\n"
"v_mfma_f32_16x16x32_bf16 v[216:219], a[64:67], a[144:147], v[216:219]\n"
"ds_read_b128 a[160:163], v224 offset:20480\n"
"v_mfma_f32_16x16x32_bf16 v[216:219], a[68:71], a[148:151], v[216:219]\n"
"v_mfma_f32_16x16x32_bf16 v[216:219], a[96:99], a[152:155], v[216:219]\n"
"ds_read_b128 a[164:167], v225 offset:20480\n"
"v_mfma_f32_16x16x32_bf16 v[216:219], a[100:103], a[24:27], v[216:219]\n"
"s_waitcnt lgkmcnt(6)\n"
"v_mfma_f32_16x16x32_bf16 v[24:27], a[8:11], a[28:31], 0\n"
"ds_read_b128 a[168:171], v226 offset:20480\n"
"v_cvt_pk_bf16_f32 v16, v16, v17\n"
"v_mfma_f32_16x16x32_bf16 v[24:27], a[12:15], a[56:59], v[24:27]\n"
"v_cvt_pk_bf16_f32 v17, v18, v19\n"
"v_cvt_pk_bf16_f32 v18, v20, v21\n"
"v_mfma_f32_16x16x32_bf16 v[24:27], a[16:19], a[60:63], v[24:27]\n"
"ds_read_b128 a[172:175], v227 offset:20480\n"
"v_cvt_pk_bf16_f32 v19, v22, v23\n"
"v_mfma_f32_16x16x32_bf16 v[24:27], a[20:23], a[88:91], v[24:27]\n"
"buffer_store_dwordx4 v[16:19], %[tA], s[32:35], s86 offen offset:2048 sc1\n"
"v_mfma_f32_16x16x32_bf16 v[220:223], a[192:195], a[28:31], 0\n"
"ds_read_b128 a[176:179], v224 offset:27648\n"
"v_mfma_f32_16x16x32_bf16 v[220:223], a[196:199], a[56:59], v[220:223]\n"
"v_mfma_f32_16x16x32_bf16 v[220:223], a[200:203], a[60:63], v[220:223]\n"
"ds_read_b128 a[180:183], v225 offset:27648\n"
"v_mfma_f32_16x16x32_bf16 v[220:223], a[204:207], a[88:91], v[220:223]\n"
"s_waitcnt lgkmcnt(6)\n"
"v_mfma_f32_16x16x32_bf16 v[24:27], a[40:43], a[92:95], v[24:27]\n"
"ds_read_b128 a[128:131], v226 offset:27648\n"
"v_mfma_f32_16x16x32_bf16 v[24:27], a[44:47], a[120:123], v[24:27]\n"
"v_mfma_f32_16x16x32_bf16 v[24:27], a[48:51], a[124:127], v[24:27]\n"
"ds_read_b128 a[132:135], v227 offset:27648\n"
"v_mfma_f32_16x16x32_bf16 v[24:27], a[52:55], a[156:159], v[24:27]\n"
"v_mfma_f32_16x16x32_bf16 v[220:223], a[208:211], a[92:95], v[220:223]\n"
"v_mfma_f32_16x16x32_bf16 v[220:223], a[212:215], a[120:123], v[220:223]\n"
"v_mfma_f32_16x16x32_bf16 v[220:223], a[216:219], a[124:127], v[220:223]\n"
"v_mfma_f32_16x16x32_bf16 v[220:223], a[220:223], a[156:159], v[220:223]\n"
"s_waitcnt lgkmcnt(4)\n"
"v_mfma_f32_16x16x32_bf16 v[24:27], a[72:75], a[160:163], v[24:27]\n"
"v_mfma_f32_16x16x32_bf16 v[24:27], a[76:79], a[164:167], v[24:27]\n"
"v_mfma_f32_16x16x32_bf16 v[24:27], a[80:83], a[168:171], v[24:27]\n"
"v_mfma_f32_16x16x32_bf16 v[24:27], a[84:87], a[172:175], v[24:27]\n"
"v_mfma_f32_16x16x32_bf16 v[220:223], a[0:3], a[160:163], v[220:223]\n"
"v_mfma_f32_16x16x32_bf16 v[220:223], a[4:7], a[164:167], v[220:223]\n"
"v_mfma_f32_16x16x32_bf16 v[220:223], a[32:35], a[168:171], v[220:223]\n"
"v_mfma_f32_16x16x32_bf16 v[220:223], a[36:39], a[172:175], v[220:223]\n"
"s_waitcnt lgkmcnt(0)\n"
"v_mfma_f32_16x16x32_bf16 v[24:27], a[104:107], a[176:179], v[24:27]\n"
"v_mfma_f32_16x16x32_bf16 v[24:27], a[108:111], a[180:183], v[24:27]\n"
"v_mfma_f32_16x16x32_bf16 v[24:27], a[112:115], a[128:131], v[24:27]\n"
"v_mfma_f32_16x16x32_bf16 v[24:27], a[116:119], a[132:135], v[24:27]\n"
"v_mfma_f32_16x16x32_bf16 v[220:223], a[64:67], a[176:179], v[220:223]\n"
"v_mfma_f32_16x16x32_bf16 v[220:223], a[68:71], a[180:183], v[220:223]\n"
"v_mfma_f32_16x16x32_bf16 v[220:223], a[96:99], a[128:131], v[220:223]\n"
"v_mfma_f32_16x16x32_bf16 v[220:223], a[100:103], a[132:135], v[220:223]\n"
"; tail\n"
"s_nop 3 ; auto\n"
"v_cvt_pk_bf16_f32 v24, v24, v25\n"
"v_cvt_pk_bf16_f32 v25, v26, v27\n"
"buffer_store_dwordx2 v[24:25], v244, s[32:35], s86 offen offset:3072 sc1\n"
"s_waitcnt vmcnt(2)\n"
"v_cmp_gt_u32 vcc, 1, v250\n"
"s_nop 1\n"
"s_cbranch_vccnz Lspin1\n"
"Lret1:\n"
"s_sub_u32 s63, s38, 1\n"
"s_and_b32 s63, s63, 7\n"
"s_mul_i32 s63, s63, 115712\n"
"s_add_u32 s63, s63, s57\n"
"s_add_u32 s63, s63, 28928\n"
"buffer_load_dwordx4 v[32:35], %[tA], s[32:35], s63 offen sc1\n"
"buffer_load_dwordx4 v[36:39], %[tA], s[32:35], s63 offen offset:1024 sc1\n"
"buffer_load_dwordx4 v[40:43], %[tA], s[32:35], s63 offen offset:2048 sc1\n"
"buffer_load_dwordx2 v[44:45], v244, s[32:35], s63 offen offset:3072 sc1\n"
"s_waitcnt vmcnt(4)\n"
"s_barrier\n"
"v_mov_b32 v248, 3\n"
"buffer_store_dword v248, v243, s[32:35], s54 offen sc1\n"
"s_sub_u32 s63, s38, 2\n"
"s_and_b32 s63, s63, 7\n"
"s_mul_i32 s63, s63, 115712\n"
"s_add_u32 s63, s63, s57\n"
"s_add_u32 s63, s63, 57856\n"
"buffer_load_dwordx4 v[46:49], %[tA], s[32:35], s63 offen sc1\n"
"buffer_load_dwordx4 v[50:53], %[tA], s[32:35], s63 offen offset:1024 sc1\n"
"buffer_load_dwordx4 v[54:57], %[tA], s[32:35], s63 offen offset:2048 sc1\n"
"buffer_load_dwordx2 v[58:59], v244, s[32:35], s63 offen offset:3072 sc1\n"
"s_sub_u32 s63, s38, 3\n"
"s_and_b32 s63, s63, 7\n"
"s_mul_i32 s63, s63, 115712\n"
"s_add_u32 s63, s63, s57\n"
"s_add_u32 s63, s63, 86784\n"
"buffer_load_dwordx4 v[60:63], %[tA], s[32:35], s63 offen sc1\n"
"buffer_load_dwordx4 v[64:67], %[tA], s[32:35], s63 offen offset:1024 sc1\n"
"buffer_load_dwordx4 v[68:71], %[tA], s[32:35], s63 offen offset:2048 sc1\n"
"buffer_load_dwordx2 v[72:73], v244, s[32:35], s63 offen offset:3072 sc1\n"
"s_add_u32 s63, s100, 0\n"
"buffer_load_dwordx2 v[76:77], v245, s[32:35], s63 offen sc1\n"
"s_add_u32 s63, s100, 115712\n"
"buffer_load_dwordx2 v[78:79], v245, s[32:35], s63 offen sc1\n"
"s_add_u32 s63, s100, 231424\n"
"buffer_load_dwordx2 v[80:81], v245, s[32:35], s63 offen sc1\n"
"s_add_u32 s63, s100, 347136\n"
"buffer_load_dwordx2 v[82:83], v245, s[32:35], s63 offen sc1\n"
"s_add_u32 s63, s100, 462848\n"
"buffer_load_dwordx2 v[84:85], v245, s[32:35], s63 offen sc1\n"
"s_add_u32 s63, s100, 578560\n"
"buffer_load_dwordx2 v[86:87], v245, s[32:35], s63 offen sc1\n"
"s_add_u32 s63, s100, 694272\n"
"buffer_load_dwordx2 v[88:89], v245, s[32:35], s63 offen sc1\n"
"s_add_u32 s63, s100, 809984\n"
"buffer_load_dwordx2 v[90:91], v245, s[32:35], s63 offen sc1\n"
"v_bfe_u32 v249, %[tA], 4, 2\n"
"s_add_u32 s62, s38, 1\n"
"v_add_u32 v249, s62, v249\n"
"v_and_b32 v249, 7, v249\n"
"v_lshlrev_b32 v249, 2, v249\n"
"s_waitcnt vmcnt(20) ; auto\n"
"v_lshlrev_b32 v228, 16, v32\n"
"v_and_b32 v229, s65, v32\n"
"v_pk_add_f32 v[196:197], v[196:197], v[228:229]\n"
"v_lshlrev_b32 v230, 16, v33\n"
"v_and_b32 v231, s65, v33\n"
"v_pk_add_f32 v[198:199], v[198:199], v[230:231]\n"
"v_lshlrev_b32 v228, 16, v34\n"
"v_and_b32 v229, s65, v34\n"
"v_pk_add_f32 v[200:201], v[200:201], v[228:229]\n"
"v_lshlrev_b32 v230, 16, v35\n"
"v_and_b32 v231, s65, v35\n"
"v_pk_add_f32 v[202:203], v[202:203], v[230:231]\n"
"s_waitcnt vmcnt(19) ; auto\n"
"v_lshlrev_b32 v228, 16, v36\n"
"v_and_b32 v229, s65, v36\n"
"v_pk_add_f32 v[204:205], v[204:205], v[228:229]\n"
"v_lshlrev_b32 v230, 16, v37\n"
"v_and_b32 v231, s65, v37\n"
"v_pk_add_f32 v[206:207], v[206:207], v[230:231]\n"
"v_lshlrev_b32 v228, 16, v38\n"
"v_and_b32 v229, s65, v38\n"
"v_pk_add_f32 v[208:209], v[208:209], v[228:229]\n"
"v_lshlrev_b32 v230, 16, v39\n"
"v_and_b32 v231, s65, v39\n"
"v_pk_add_f32 v[210:211], v[210:211], v[230:231]\n"
"s_waitcnt vmcnt(18) ; auto\n"
"v_lshlrev_b32 v228, 16, v40\n"
"v_and_b32 v229, s65, v40\n"
"v_pk_add_f32 v[212:213], v[212:213], v[228:229]\n"
"v_lshlrev_b32 v230, 16, v41\n"
"v_and_b32 v231, s65, v41\n"
"v_pk_add_f32 v[214:215], v[214:215], v[230:231]\n"
"v_lshlrev_b32 v228, 16, v42\n"
"v_and_b32 v229, s65, v42\n"
"v_pk_add_f32 v[216:217], v[216:217], v[228:229]\n"
"v_lshlrev_b32 v230, 16, v43\n"
"v_and_b32 v231, s65, v43\n"
"v_pk_add_f32 v[218:219], v[218:219], v[230:231]\n"
"s_waitcnt vmcnt(17) ; auto\n"
"v_lshlrev_b32 v228, 16, v44\n"
"v_and_b32 v229, s65, v44\n"
"v_pk_add_f32 v[220:221], v[220:221], v[228:229]\n"
"v_lshlrev_b32 v230, 16, v45\n"
"v_and_b32 v231, s65, v45\n"
"v_pk_add_f32 v[222:223], v[222:223], v[230:231]\n"
"s_waitcnt vmcnt(15) ; auto\n"
"v_lshlrev_b32 v228, 16, v46\n"
"v_and_b32 v229, s65, v46\n"
"v_pk_add_f32 v[196:197], v[196:197], v[228:229]\n"
"buffer_load_dword v250, v249, s[32:35], s53 offen sc1\n"
"v_lshlrev_b32 v230, 16, v47\n"
"v_and_b32 v231, s65, v47\n"
"v_pk_add_f32 v[198:199], v[198:199], v[230:231]\n"
"v_lshlrev_b32 v228, 16, v48\n"
"v_and_b32 v229, s65, v48\n"
"v_pk_add_f32 v[200:201], v[200:201], v[228:229]\n"
"v_lshlrev_b32 v230, 16, v49\n"
"v_and_b32 v231, s65, v49\n"
"v_pk_add_f32 v[202:203], v[202:203], v[230:231]\n"
"s_waitcnt vmcnt(15) ; auto\n"
"v_lshlrev_b32 v228, 16, v50\n"
"v_and_b32 v229, s65, v50\n"
"v_pk_add_f32 v[204:205], v[204:205], v[228:229]\n"
"v_lshlrev_b32 v230, 16, v51\n"
"v_and_b32 v231, s65, v51\n"
"v_pk_add_f32 v[206:207], v[206:207], v[230:231]\n"
"v_lshlrev_b32 v228, 16, v52\n"
"v_and_b32 v229, s65, v52\n"
"v_pk_add_f32 v[208:209], v[208:209], v[228:229]\n"
"v_lshlrev_b32 v230, 16, v53\n"
"v_and_b32 v231, s65, v53\n"
"v_pk_add_f32 v[210:211], v[210:211], v[230:231]\n"
"s_waitcnt vmcnt(14) ; auto\n"
"v_lshlrev_b32 v228, 16, v54\n"
"v_and_b32 v229, s65, v54\n"
"v_pk_add_f32 v[212:213], v[212:213], v[228:229]\n"
"v_lshlrev_b32 v230, 16, v55\n"
"v_and_b32 v231, s65, v55\n"
"v_pk_add_f32 v[214:215], v[214:215], v[230:231]\n"
"v_lshlrev_b32 v228, 16, v56\n"
"v_and_b32 v229, s65, v56\n"
"v_pk_add_f32 v[216:217], v[216:217], v[228:229]\n"
"v_lshlrev_b32 v230, 16, v57\n"
"v_and_b32 v231, s65, v57\n"
"v_pk_add_f32 v[218:219], v[218:219], v[230:231]\n"
"s_waitcnt vmcnt(13) ; auto\n"
"v_lshlrev_b32 v228, 16, v58\n"
"v_and_b32 v229, s65, v58\n"
"v_pk_add_f32 v[220:221], v[220:221], v[228:229]\n"
"v_lshlrev_b32 v230, 16, v59\n"
"v_and_b32 v231, s65, v59\n"
"v_pk_add_f32 v[222:223], v[222:223], v[230:231]\n"
"s_waitcnt vmcnt(12) ; auto\n"
"v_lshlrev_b32 v228, 16, v60\n"
"v_and_b32 v229, s65, v60\n"
"v_pk_add_f32 v[196:197], v[196:197], v[228:229]\n"
"v_lshlrev_b32 v230, 16, v61\n"
"v_and_b32 v231, s65, v61\n"
"v_pk_add_f32 v[198:199], v[198:199], v[230:231]\n"
"v_lshlrev_b32 v228, 16, v62\n"
"v_and_b32 v229, s65, v62\n"
"v_pk_add_f32 v[200:201], v[200:201], v[228:229]\n"
"v_lshlrev_b32 v230, 16, v63\n"
"v_and_b32 v231, s65, v63\n"
"v_pk_add_f32 v[202:203], v[202:203], v[230:231]\n"
"s_waitcnt vmcnt(11) ; auto\n"
"v_lshlrev_b32 v228, 16, v64\n"
"v_and_b32 v229, s65, v64\n"
"v_pk_add_f32 v[204:205], v[204:205], v[228:229]\n"
"v_lshlrev_b32 v230, 16, v65\n"
"v_and_b32 v231, s65, v65\n"
"v_pk_add_f32 v[206:207], v[206:207], v[230:231]\n"
"v_lshlrev_b32 v228, 16, v66\n"
"v_and_b32 v229, s65, v66\n"
"v_pk_add_f32 v[208:209], v[208:209], v[228:229]\n"
"v_lshlrev_b32 v230, 16, v67\n"
"v_and_b32 v231, s65, v67\n"
"v_pk_add_f32 v[210:211], v[210:211], v[230:231]\n"
"s_waitcnt vmcnt(10) ; auto\n"
"v_lshlrev_b32 v228, 16, v68\n"
"v_and_b32 v229, s65, v68\n"
"v_pk_add_f32 v[212:213], v[212:213], v[228:229]\n"
"v_lshlrev_b32 v230, 16, v69\n"
"v_and_b32 v231, s65, v69\n"
"v_pk_add_f32 v[214:215], v[214:215], v[230:231]\n"
"v_lshlrev_b32 v228, 16, v70\n"
"v_and_b32 v229, s65, v70\n"
"v_pk_add_f32 v[216:217], v[216:217], v[228:229]\n"
"v_lshlrev_b32 v230, 16, v71\n"
"v_and_b32 v231, s65, v71\n"
"v_pk_add_f32 v[218:219], v[218:219], v[230:231]\n"
"s_waitcnt vmcnt(9) ; auto\n"
"v_lshlrev_b32 v228, 16, v72\n"
"v_and_b32 v229, s65, v72\n"
"v_pk_add_f32 v[220:221], v[220:221], v[228:229]\n"
"v_lshlrev_b32 v230, 16, v73\n"
"v_and_b32 v231, s65, v73\n"
"v_pk_add_f32 v[222:223], v[222:223], v[230:231]\n"
"s_waitcnt vmcnt(8) ; auto\n"
"v_lshlrev_b32 v92, 16, v76\n"
"v_and_b32 v93, s65, v76\n"
"v_lshlrev_b32 v94, 16, v77\n"
"v_and_b32 v95, s65, v77\n"
"s_waitcnt vmcnt(7) ; auto\n"
"v_lshlrev_b32 v228, 16, v78\n"
"v_and_b32 v229, s65, v78\n"
"v_pk_add_f32 v[92:93], v[92:93], v[228:229]\n"
"v_lshlrev_b32 v230, 16, v79\n"
"v_and_b32 v231, s65, v79\n"
"v_pk_add_f32 v[94:95], v[94:95], v[230:231]\n"
"s_waitcnt vmcnt(6) ; auto\n"
"v_lshlrev_b32 v228, 16, v80\n"
"v_and_b32 v229, s65, v80\n"
"v_pk_add_f32 v[92:93], v[92:93], v[228:229]\n"
"v_lshlrev_b32 v230, 16, v81\n"
"v_and_b32 v231, s65, v81\n"
"v_pk_add_f32 v[94:95], v[94:95], v[230:231]\n"
"s_waitcnt vmcnt(5) ; auto\n"
"v_lshlrev_b32 v228, 16, v82\n"
"v_and_b32 v229, s65, v82\n"
"v_pk_add_f32 v[92:93], v[92:93], v[228:229]\n"
"v_lshlrev_b32 v230, 16, v83\n"
"v_and_b32 v231, s65, v83\n"
"v_pk_add_f32 v[94:95], v[94:95], v[230:231]\n"
"s_waitcnt vmcnt(4) ; auto\n"
"v_lshlrev_b32 v228, 16, v84\n"
"v_and_b32 v229, s65, v84\n"
"v_pk_add_f32 v[92:93], v[92:93], v[228:229]\n"
"v_lshlrev_b32 v230, 16, v85\n"
"v_and_b32 v231, s65, v85\n"
"v_pk_add_f32 v[94:95], v[94:95], v[230:231]\n"
"s_waitcnt vmcnt(3) ; auto\n"
"v_lshlrev_b32 v228, 16, v86\n"
"v_and_b32 v229, s65, v86\n"
"v_pk_add_f32 v[92:93], v[92:93], v[228:229]\n"
"v_lshlrev_b32 v230, 16, v87\n"
"v_and_b32 v231, s65, v87\n"
"v_pk_add_f32 v[94:95], v[94:95], v[230:231]\n"
"s_waitcnt vmcnt(2) ; auto\n"
"v_lshlrev_b32 v228, 16, v88\n"
"v_and_b32 v229, s65, v88\n"
"v_pk_add_f32 v[92:93], v[92:93], v[228:229]\n"
"v_lshlrev_b32 v230, 16, v89\n"
"v_and_b32 v231, s65, v89\n"
"v_pk_add_f32 v[94:95], v[94:95], v[230:231]\n"
"s_waitcnt vmcnt(1) ; auto\n"
"v_lshlrev_b32 v228, 16, v90\n"
"v_and_b32 v229, s65, v90\n"
"v_pk_add_f32 v[92:93], v[92:93], v[228:229]\n"
"v_lshlrev_b32 v230, 16, v91\n"
"v_and_b32 v231, s65, v91\n"
"v_pk_add_f32 v[94:95], v[94:95], v[230:231]\n"
"v_cvt_pk_bf16_f32 v92, v92, v93\n"
"v_cvt_pk_bf16_f32 v93, v94, v95\n"
"v_bfe_u32 v100, %[tA], 4, 4\n"
"v_lshrrev_b32 v101, 9, %[tA]\n"
"s_lshl_b32 s62, s36, 1\n"
"v_add_u32 v101, s62, v101\n"
"v_and_b32 v102, 7, v100\n"
"v_xor_b32 v101, v101, v102\n"
"v_lshlrev_b32 v101, 4, v101\n"
"v_lshl_add_u32 v101, v100, 7, v101\n"
"v_bfe_u32 v102, %[tA], 8, 1\n"
"v_lshl_add_u32 v101, v102, 3, v101\n"
"v_lshrrev_b32 v102, 7, %[tA]\n"
"v_bfe_u32 v103, %[tA], 4, 3\n"
"v_xor_b32 v103, v103, v102\n"
"v_mul_u32_u24 v246, 2304, v102\n"
"v_lshl_add_u32 v246, v103, 4, v246\n"
"s_lshl_b32 s62, s36, 10\n"
"v_add_u32 v102, s62, %[tA]\n"
"s_mul_i32 s62, s36, 18432\n"
"s_add_u32 s58, s58, s62\n"
"s_waitcnt vmcnt(0)\n"
"v_cmp_gt_u32 vcc, 3, v250\n"
"s_nop 1\n"
"s_cbranch_vccnz Lspin3\n"
"Lret3:\n"
"s_add_u32 s62, s36, 1\n"
"s_and_b32 s62, s62, 3\n"
"s_sub_u32 s63, s38, s62\n"
"s_add_u32 s63, s63, 4\n"
"s_and_b32 s63, s63, 7\n"
"s_lshl_b32 s63, s63, 2\n"
"s_add_u32 s63, s63, s62\n"
"s_mul_i32 s63, s63, 28928\n"
"s_add_u32 s63, s63, s57\n"
"s_add_u32 s63, s63, 14336\n"
"buffer_load_dwordx4 v[112:115], %[tA], s[32:35], s63 offen sc1\n"
"buffer_load_dwordx4 v[116:119], %[tA], s[32:35], s63 offen offset:1024 sc1\n"
"buffer_load_dwordx4 v[120:123], %[tA], s[32:35], s63 offen offset:2048 sc1\n"
"buffer_load_dwordx2 v[124:125], v244, s[32:35], s63 offen offset:3072 sc1\n"
"s_add_u32 s62, s36, 2\n"
"s_and_b32 s62, s62, 3\n"
"s_sub_u32 s63, s38, s62\n"
"s_add_u32 s63, s63, 4\n"
"s_and_b32 s63, s63, 7\n"
"s_lshl_b32 s63, s63, 2\n"
"s_add_u32 s63, s63, s62\n"
"s_mul_i32 s63, s63, 28928\n"
"s_add_u32 s63, s63, s57\n"
"s_add_u32 s63, s63, 14336\n"
"buffer_load_dwordx4 v[126:129], %[tA], s[32:35], s63 offen sc1\n"
"buffer_load_dwordx4 v[130:133], %[tA], s[32:35], s63 offen offset:1024 sc1\n"
"buffer_load_dwordx4 v[134:137], %[tA], s[32:35], s63 offen offset:2048 sc1\n"
"buffer_load_dwordx2 v[138:139], v244, s[32:35], s63 offen offset:3072 sc1\n"
"s_add_u32 s62, s36, 3\n"
"s_and_b32 s62, s62, 3\n"
"s_sub_u32 s63, s38, s62\n"
"s_add_u32 s63, s63, 4\n"
"s_and_b32 s63, s63, 7\n"
"s_lshl_b32 s63, s63, 2\n"
"s_add_u32 s63, s63, s62\n"
"s_mul_i32 s63, s63, 28928\n"
"s_add_u32 s63, s63, s57\n"
"s_add_u32 s63, s63, 14336\n"
"buffer_load_dwordx4 v[140:143], %[tA], s[32:35], s63 offen sc1\n"
"buffer_load_dwordx4 v[144:147], %[tA], s[32:35], s63 offen offset:1024 sc1\n"
"buffer_load_dwordx4 v[148:151], %[tA], s[32:35], s63 offen offset:2048 sc1\n"
"buffer_load_dwordx2 v[152:153], v244, s[32:35], s63 offen offset:3072 sc1\n"
"s_add_u32 s62, s36, 0\n"
"s_and_b32 s62, s62, 3\n"
"s_sub_u32 s63, s38, s62\n"
"s_add_u32 s63, s63, 4\n"
"s_and_b32 s63, s63, 7\n"
"s_lshl_b32 s63, s63, 2\n"
"s_add_u32 s63, s63, s62\n"
"s_mul_i32 s63, s63, 28928\n"
"s_add_u32 s63, s63, s57\n"
"s_add_u32 s63, s63, 14336\n"
"buffer_load_dwordx4 v[154:157], %[tA], s[32:35], s63 offen sc1\n"
"buffer_load_dwordx4 v[158:161], %[tA], s[32:35], s63 offen offset:1024 sc1\n"
"buffer_load_dwordx4 v[162:165], %[tA], s[32:35], s63 offen offset:2048 sc1\n"
"buffer_load_dwordx2 v[166:167], v244, s[32:35], s63 offen offset:3072 sc1\n"
"buffer_store_dwordx2 v[92:93], v251, s[80:83], s101 offen\n"
"buffer_store_dword v248, v243, s[32:35], s54 offen sc1\n"
"s_waitcnt vmcnt(17) ; auto\n"
"v_lshlrev_b32 v228, 16, v112\n"
"v_and_b32 v229, s65, v112\n"
"v_pk_add_f32 v[196:197], v[196:197], v[228:229]\n"
"v_lshlrev_b32 v230, 16, v113\n"
"v_and_b32 v231, s65, v113\n"
"v_pk_add_f32 v[198:199], v[198:199], v[230:231]\n"
"v_lshlrev_b32 v228, 16, v114\n"
"v_and_b32 v229, s65, v114\n"
"v_pk_add_f32 v[200:201], v[200:201], v[228:229]\n"
"v_lshlrev_b32 v230, 16, v115\n"
"v_and_b32 v231, s65, v115\n"
"v_pk_add_f32 v[202:203], v[202:203], v[230:231]\n"
"s_waitcnt vmcnt(16) ; auto\n"
"v_lshlrev_b32 v228, 16, v116\n"
"v_and_b32 v229, s65, v116\n"
"v_pk_add_f32 v[204:205], v[204:205], v[228:229]\n"
"v_lshlrev_b32 v230, 16, v117\n"
"v_and_b32 v231, s65, v117\n"
"v_pk_add_f32 v[206:207], v[206:207], v[230:231]\n"
"v_lshlrev_b32 v228, 16, v118\n"
"v_and_b32 v229, s65, v118\n"
"v_pk_add_f32 v[208:209], v[208:209], v[228:229]\n"
"v_lshlrev_b32 v230, 16, v119\n"
"v_and_b32 v231, s65, v119\n"
"v_pk_add_f32 v[210:211], v[210:211], v[230:231]\n"
"s_waitcnt vmcnt(15) ; auto\n"
"v_lshlrev_b32 v228, 16, v120\n"
"v_and_b32 v229, s65, v120\n"
"v_pk_add_f32 v[212:213], v[212:213], v[228:229]\n"
"v_lshlrev_b32 v230, 16, v121\n"
"v_and_b32 v231, s65, v121\n"
"v_pk_add_f32 v[214:215], v[214:215], v[230:231]\n"
"v_lshlrev_b32 v228, 16, v122\n"
"v_and_b32 v229, s65, v122\n"
"v_pk_add_f32 v[216:217], v[216:217], v[228:229]\n"
"v_lshlrev_b32 v230, 16, v123\n"
"v_and_b32 v231, s65, v123\n"
"v_pk_add_f32 v[218:219], v[218:219], v[230:231]\n"
"s_waitcnt vmcnt(14) ; auto\n"
"v_lshlrev_b32 v228, 16, v124\n"
"v_and_b32 v229, s65, v124\n"
"v_pk_add_f32 v[220:221], v[220:221], v[228:229]\n"
"v_lshlrev_b32 v230, 16, v125\n"
"v_and_b32 v231, s65, v125\n"
"v_pk_add_f32 v[222:223], v[222:223], v[230:231]\n"
"s_waitcnt vmcnt(13) ; auto\n"
"v_lshlrev_b32 v228, 16, v126\n"
"v_and_b32 v229, s65, v126\n"
"v_pk_add_f32 v[196:197], v[196:197], v[228:229]\n"
"v_lshlrev_b32 v230, 16, v127\n"
"v_and_b32 v231, s65, v127\n"
"v_pk_add_f32 v[198:199], v[198:199], v[230:231]\n"
"v_lshlrev_b32 v228, 16, v128\n"
"v_and_b32 v229, s65, v128\n"
"v_pk_add_f32 v[200:201], v[200:201], v[228:229]\n"
"v_lshlrev_b32 v230, 16, v129\n"
"v_and_b32 v231, s65, v129\n"
"v_pk_add_f32 v[202:203], v[202:203], v[230:231]\n"
"s_waitcnt vmcnt(12) ; auto\n"
"v_lshlrev_b32 v228, 16, v130\n"
"v_and_b32 v229, s65, v130\n"
"v_pk_add_f32 v[204:205], v[204:205], v[228:229]\n"
"v_lshlrev_b32 v230, 16, v131\n"
"v_and_b32 v231, s65, v131\n"
"v_pk_add_f32 v[206:207], v[206:207], v[230:231]\n"
"v_lshlrev_b32 v228, 16, v132\n"
"v_and_b32 v229, s65, v132\n"
"v_pk_add_f32 v[208:209], v[208:209], v[228:229]\n"
"v_lshlrev_b32 v230, 16, v133\n"
"v_and_b32 v231, s65, v133\n"
"v_pk_add_f32 v[210:211], v[210:211], v[230:231]\n"
"s_waitcnt vmcnt(11) ; auto\n"
"v_lshlrev_b32 v228, 16, v134\n"
"v_and_b32 v229, s65, v134\n"
"v_pk_add_f32 v[212:213], v[212:213], v[228:229]\n"
"v_lshlrev_b32 v230, 16, v135\n"
"v_and_b32 v231, s65, v135\n"
"v_pk_add_f32 v[214:215], v[214:215], v[230:231]\n"
"v_lshlrev_b32 v228, 16, v136\n"
"v_and_b32 v229, s65, v136\n"
"v_pk_add_f32 v[216:217], v[216:217], v[228:229]\n"
"v_lshlrev_b32 v230, 16, v137\n"
"v_and_b32 v231, s65, v137\n"
"v_pk_add_f32 v[218:219], v[218:219], v[230:231]\n"
"s_waitcnt vmcnt(10) ; auto\n"
"v_lshlrev_b32 v228, 16, v138\n"
"v_and_b32 v229, s65, v138\n"
"v_pk_add_f32 v[220:221], v[220:221], v[228:229]\n"
"v_lshlrev_b32 v230, 16, v139\n"
"v_and_b32 v231, s65, v139\n"
"v_pk_add_f32 v[222:223], v[222:223], v[230:231]\n"
"s_waitcnt vmcnt(9) ; auto\n"
"v_lshlrev_b32 v228, 16, v140\n"
"v_and_b32 v229, s65, v140\n"
"v_pk_add_f32 v[196:197], v[196:197], v[228:229]\n"
"v_lshlrev_b32 v230, 16, v141\n"
"v_and_b32 v231, s65, v141\n"
"v_pk_add_f32 v[198:199], v[198:199], v[230:231]\n"
"v_lshlrev_b32 v228, 16, v142\n"
"v_and_b32 v229, s65, v142\n"
"v_pk_add_f32 v[200:201], v[200:201], v[228:229]\n"
"v_lshlrev_b32 v230, 16, v143\n"
"v_and_b32 v231, s65, v143\n"
"v_pk_add_f32 v[202:203], v[202:203], v[230:231]\n"
"s_waitcnt vmcnt(8) ; auto\n"
"v_lshlrev_b32 v228, 16, v144\n"
"v_and_b32 v229, s65, v144\n"
"v_pk_add_f32 v[204:205], v[204:205], v[228:229]\n"
"v_lshlrev_b32 v230, 16, v145\n"
"v_and_b32 v231, s65, v145\n"
"v_pk_add_f32 v[206:207], v[206:207], v[230:231]\n"
"v_lshlrev_b32 v228, 16, v146\n"
"v_and_b32 v229, s65, v146\n"
"v_pk_add_f32 v[208:209], v[208:209], v[228:229]\n"
"v_lshlrev_b32 v230, 16, v147\n"
"v_and_b32 v231, s65, v147\n"
"v_pk_add_f32 v[210:211], v[210:211], v[230:231]\n"
"s_waitcnt vmcnt(7) ; auto\n"
"v_lshlrev_b32 v228, 16, v148\n"
"v_and_b32 v229, s65, v148\n"
"v_pk_add_f32 v[212:213], v[212:213], v[228:229]\n"
"v_lshlrev_b32 v230, 16, v149\n"
"v_and_b32 v231, s65, v149\n"
"v_pk_add_f32 v[214:215], v[214:215], v[230:231]\n"
"v_lshlrev_b32 v228, 16, v150\n"
"v_and_b32 v229, s65, v150\n"
"v_pk_add_f32 v[216:217], v[216:217], v[228:229]\n"
"v_lshlrev_b32 v230, 16, v151\n"
"v_and_b32 v231, s65, v151\n"
"v_pk_add_f32 v[218:219], v[218:219], v[230:231]\n"
"s_waitcnt vmcnt(6) ; auto\n"
"v_lshlrev_b32 v228, 16, v152\n"
"v_and_b32 v229, s65, v152\n"
"v_pk_add_f32 v[220:221], v[220:221], v[228:229]\n"
"v_lshlrev_b32 v230, 16, v153\n"
"Lh13:\n"
"s_cbranch_execz Lw3\n"
"v_and_b32 v231, s65, v153\n"
"v_pk_add_f32 v[222:223], v[222:223], v[230:231]\n"
"s_waitcnt vmcnt(5) ; auto\n"
"v_lshlrev_b32 v228, 16, v154\n"
"v_and_b32 v229, s65, v154\n"
"v_pk_add_f32 v[196:197], v[196:197], v[228:229]\n"
"v_lshlrev_b32 v230, 16, v155\n"
"v_and_b32 v231, s65, v155\n"
"v_pk_add_f32 v[198:199], v[198:199], v[230:231]\n"
"v_cvt_pk_bf16_f32 v196, v196, v197\n"
"v_cvt_pk_bf16_f32 v197, v198, v199\n"
"ds_write_b64 v101, v[196:197] offset:0\n"
"v_lshlrev_b32 v228, 16, v156\n"
"v_and_b32 v229, s65, v156\n"
"v_pk_add_f32 v[200:201], v[200:201], v[228:229]\n"
"v_lshlrev_b32 v230, 16, v157\n"
"v_and_b32 v231, s65, v157\n"
"v_pk_add_f32 v[202:203], v[202:203], v[230:231]\n"
"v_cvt_pk_bf16_f32 v200, v200, v201\n"
"v_cvt_pk_bf16_f32 v201, v202, v203\n"
"ds_write_b64 v101, v[200:201] offset:2048\n"
"s_waitcnt vmcnt(4) ; auto\n"
"v_lshlrev_b32 v228, 16, v158\n"
"v_and_b32 v229, s65, v158\n"
"v_pk_add_f32 v[204:205], v[204:205], v[228:229]\n"
"v_lshlrev_b32 v230, 16, v159\n"
"v_and_b32 v231, s65, v159\n"
"v_pk_add_f32 v[206:207], v[206:207], v[230:231]\n"
"v_cvt_pk_bf16_f32 v204, v204, v205\n"
"v_cvt_pk_bf16_f32 v205, v206, v207\n"
"ds_write_b64 v101, v[204:205] offset:4096\n"
"v_lshlrev_b32 v228, 16, v160\n"
"v_and_b32 v229, s65, v160\n"
"v_pk_add_f32 v[208:209], v[208:209], v[228:229]\n"
"v_lshlrev_b32 v230, 16, v161\n"
"v_and_b32 v231, s65, v161\n"
"v_pk_add_f32 v[210:211], v[210:211], v[230:231]\n"
"v_cvt_pk_bf16_f32 v208, v208, v209\n"
"v_cvt_pk_bf16_f32 v209, v210, v211\n"
"ds_write_b64 v101, v[208:209] offset:6144\n"
"s_waitcnt vmcnt(3) ; auto\n"
"v_lshlrev_b32 v228, 16, v162\n"
"v_and_b32 v229, s65, v162\n"
"v_pk_add_f32 v[212:213], v[212:213], v[228:229]\n"
"v_lshlrev_b32 v230, 16, v163\n"
"v_and_b32 v231, s65, v163\n"
"v_pk_add_f32 v[214:215], v[214:215], v[230:231]\n"
"v_cvt_pk_bf16_f32 v212, v212, v213\n"
"v_cvt_pk_bf16_f32 v213, v214, v215\n"
"ds_write_b64 v101, v[212:213] offset:8192\n"
"v_lshlrev_b32 v228, 16, v164\n"
"v_and_b32 v229, s65, v164\n"
"v_pk_add_f32 v[216:217], v[216:217], v[228:229]\n"
"v_lshlrev_b32 v230, 16, v165\n"
"v_and_b32 v231, s65, v165\n"
"v_pk_add_f32 v[218:219], v[218:219], v[230:231]\n"
"v_cvt_pk_bf16_f32 v216, v216, v217\n"
"v_cvt_pk_bf16_f32 v217, v218, v219\n"
"ds_write_b64 v101, v[216:217] offset:10240\n"
"s_waitcnt vmcnt(2) ; auto\n"
"v_lshlrev_b32 v228, 16, v166\n"
"v_and_b32 v229, s65, v166\n"
"v_pk_add_f32 v[220:221], v[220:221], v[228:229]\n"
"v_lshlrev_b32 v230, 16, v167\n"
"v_and_b32 v231, s65, v167\n"
"v_pk_add_f32 v[222:223], v[222:223], v[230:231]\n"
"v_cvt_pk_bf16_f32 v220, v220, v221\n"
"v_cvt_pk_bf16_f32 v221, v222, v223\n"
"ds_write_b64 v101, v[220:221] offset:12288\n"
"s_waitcnt lgkmcnt(0)\n"
"s_barrier\n"
"ds_read_b128 v[0:3], v102 offset:0\n"
"ds_read_b128 v[4:7], v102 offset:4096\n"
"ds_read_b128 v[8:11], v102 offset:8192\n"
"ds_read_b128 v[12:15], v102 offset:12288\n"
"s_add_u32 s63, s58, 0\n"
"s_waitcnt lgkmcnt(3) ; auto\n"
"buffer_store_dwordx4 v[0:3], v246, s[80:83], s63 offen\n"
"s_add_u32 s63, s58, 73728\n"
"s_waitcnt lgkmcnt(2) ; auto\n"
"buffer_store_dwordx4 v[4:7], v246, s[80:83], s63 offen\n"
"s_add_u32 s63, s58, 147456\n"
"s_waitcnt lgkmcnt(1) ; auto\n"
"buffer_store_dwordx4 v[8:11], v246, s[80:83], s63 offen\n"
"s_add_u32 s63, s58, 221184\n"
"s_waitcnt lgkmcnt(0) ; auto\n"
"buffer_store_dwordx4 v[12:15], v246, s[80:83], s63 offen\n"
"s_endpgm\n"
"Lspin1:\n"
"s_mov_b32 s59, 0\n"
"Lsp1a:\n"
"s_add_u32 s59, s59, 1\n"
"s_and_b32 s63, s59, 15\n"
"s_cmp_lg_u32 s63, 0\n"
"s_cbranch_scc1 Lsp1b\n"
"buffer_store_dword v248, v243, s[32:35], s55 offen sc1\n"
"Lsp1b:\n"
"buffer_load_dword v250, v249, s[32:35], s53 offen sc1\n"
"s_waitcnt vmcnt(0)\n"
"v_cmp_gt_u32 vcc, 1, v250\n"
"s_nop 1\n"
"s_cbranch_vccz Lret1\n"
"s_sleep 1\n"
"s_cmp_lt_u32 s59, 0x100000\n"
"s_cbranch_scc1 Lsp1a\n"
"s_branch Lret1\n"
"Lspin3:\n"
"s_mov_b32 s59, 0\n"
"Lsp3a:\n"
"s_add_u32 s59, s59, 1\n"
"s_and_b32 s63, s59, 15\n"
"s_cmp_lg_u32 s63, 0\n"
"s_cbranch_scc1 Lsp3b\n"
"buffer_store_dword v248, v243, s[32:35], s55 offen sc1\n"
"Lsp3b:\n"
"buffer_load_dword v250, v249, s[32:35], s53 offen sc1\n"
"s_waitcnt vmcnt(0)\n"
"v_cmp_gt_u32 vcc, 3, v250\n"
"s_nop 1\n"
"s_cbranch_vccz Lret3\n"
"s_sleep 1\n"
"s_cmp_lt_u32 s59, 0x100000\n"
"s_cbranch_scc1 Lsp3a\n"
"s_branch Lret3\n"
"Lcode1:\n"
"Lslow:\n"
"s_waitcnt vmcnt(0) lgkmcnt(0)\n"
"s_nop 7\n"
"s_mov_b64 s[84:85], s[10:11]\n"
"s_mov_b32 s86, 131072\n"
"s_mov_b32 s87, 0x20000\n"
"s_mov_b64 s[88:89], s[12:13]\n"
"s_mov_b32 s90, 131072\n"
"s_mov_b32 s91, 0x20000\n"
"v_lshrrev_b32 v216, 8, %[tA]\n"
"v_bfe_u32 v217, %[tA], 6, 2\n"
"v_bfe_u32 v218, %[tA], 4, 2\n"
"v_lshlrev_b32 v220, 8, v217\n"
"v_lshl_add_u32 v220, v216, 6, v220\n"
"v_lshl_add_u32 v220, v218, 4, v220\n"
"v_xor_b32 v221, 32, v220\n"
"v_and_b32 v219, 1, v217\n"
"v_lshlrev_b32 v219, 1, v219\n"
"v_xor_b32 v219, v219, v216\n"
"v_lshlrev_b32 v222, 4, v219\n"
"v_lshl_add_u32 v222, v217, 10, v222\n"
"v_lshl_add_u32 v222, v218, 6, v222\n"
"v_add_u32 v222, s66, v222\n"
"v_and_b32 v219, 1, v216\n"
"v_lshlrev_b32 v223, 5, v219\n"
"v_lshl_add_u32 v223, v216, 10, v223\n"
"v_lshl_add_u32 v223, v217, 6, v223\n"
"v_lshl_add_u32 v223, v218, 3, v223\n"
"v_add_u32 v223, s66, v223\n"
"v_xor_b32 v235, 32, v223\n"
"v_mov_b32 v228, 0xff800000\n"
"v_mov_b32 v229, 0xff800000\n"
"v_mov_b32 v230, 0xff800000\n"
"v_mov_b32 v231, 0xff800000\n"
"v_mov_b32 v232, 0xff800000\n"
"v_mov_b32 v233, 0xff800000\n"
"v_mov_b32 v234, 0xff800000\n"
"s_mov_b32 s92, 16\n"
"s_mov_b32 s93, 0\n"
"Ls1:\n"
"s_nop 7\n"
"s_add_u32 m0, s66, 0\n"
"s_add_u32 s94, s93, 0\n"
"buffer_load_dwordx4 v220, s[84:87], s94 offen lds\n"
"buffer_load_dwordx4 v221, s[84:87], s94 offen offset:1024 lds\n"
"buffer_load_dwordx4 v220, s[84:87], s94 offen offset:2048 lds\n"
"buffer_load_dwordx4 v221, s[84:87], s94 offen offset:3072 lds\n"
"s_add_u32 m0, s66, 4096\n"
"s_add_u32 s94, s93, 4096\n"
"buffer_load_dwordx4 v220, s[84:87], s94 offen lds\n"
"buffer_load_dwordx4 v221, s[84:87], s94 offen offset:1024 lds\n"
"buffer_load_dwordx4 v220, s[84:87], s94 offen offset:2048 lds\n"
"buffer_load_dwordx4 v221, s[84:87], s94 offen offset:3072 lds\n"
"s_waitcnt vmcnt(0)\n"
"ds_read_b128 a[160:163], v222 offset:0\n"
"ds_read_b128 a[164:167], v222 offset:256\n"
"ds_read_b128 a[168:171], v222 offset:512\n"
"ds_read_b128 a[172:175], v222 offset:768\n"
"ds_read_b128 a[176:179], v222 offset:4096\n"
"ds_read_b128 a[180:183], v222 offset:4352\n"
"ds_read_b128 a[184:187], v222 offset:4608\n"
"ds_read_b128 a[188:191], v222 offset:4864\n"
"s_waitcnt lgkmcnt(0)\n"
"v_mfma_f32_16x16x32_bf16 v[208:211], a[160:163], v[0:3], 0\n"
"v_mfma_f32_16x16x32_bf16 v[208:211], a[164:167], v[4:7], v[208:211]\n"
"v_mfma_f32_16x16x32_bf16 v[208:211], a[168:171], v[8:11], v[208:211]\n"
"v_mfma_f32_16x16x32_bf16 v[208:211], a[172:175], v[12:15], v[208:211]\n"
"v_mfma_f32_16x16x32_bf16 v[212:215], a[176:179], v[0:3], 0\n"
"v_mfma_f32_16x16x32_bf16 v[212:215], a[180:183], v[4:7], v[212:215]\n"
"v_mfma_f32_16x16x32_bf16 v[212:215], a[184:187], v[8:11], v[212:215]\n"
"v_mfma_f32_16x16x32_bf16 v[212:215], a[188:191], v[12:15], v[212:215]\n"
"s_nop 3 ; auto\n"
"v_max3_f32 v228, v228, v208, v209\n"
"v_max3_f32 v228, v228, v210, v211\n"
"s_nop 1 ; auto\n"
"v_max3_f32 v228, v228, v212, v213\n"
"v_max3_f32 v228, v228, v214, v215\n"
"v_mfma_f32_16x16x32_bf16 v[208:211], a[160:163], v[16:19], 0\n"
"v_mfma_f32_16x16x32_bf16 v[208:211], a[164:167], v[20:23], v[208:211]\n"
"v_mfma_f32_16x16x32_bf16 v[208:211], a[168:171], v[24:27], v[208:211]\n"
"v_mfma_f32_16x16x32_bf16 v[208:211], a[172:175], v[28:31], v[208:211]\n"
"v_mfma_f32_16x16x32_bf16 v[212:215], a[176:179], v[16:19], 0\n"
"v_mfma_f32_16x16x32_bf16 v[212:215], a[180:183], v[20:23], v[212:215]\n"
"v_mfma_f32_16x16x32_bf16 v[212:215], a[184:187], v[24:27], v[212:215]\n"
"v_mfma_f32_16x16x32_bf16 v[212:215], a[188:191], v[28:31], v[212:215]\n"
"s_nop 3 ; auto\n"
"v_max3_f32 v229, v229, v208, v209\n"
"v_max3_f32 v229, v229, v210, v211\n"
"s_nop 1 ; auto\n"
"v_max3_f32 v229, v229, v212, v213\n"
"v_max3_f32 v229, v229, v214, v215\n"
"v_mfma_f32_16x16x32_bf16 v[208:211], a[160:163], v[32:35], 0\n"
"v_mfma_f32_16x16x32_bf16 v[208:211], a[164:167], v[36:39], v[208:211]\n"
"v_mfma_f32_16x16x32_bf16 v[208:211], a[168:171], v[40:43], v[208:211]\n"
"v_mfma_f32_16x16x32_bf16 v[208:211], a[172:175], v[44:47], v[208:211]\n"
"v_mfma_f32_16x16x32_bf16 v[212:215], a[176:179], v[32:35], 0\n"
"v_mfma_f32_16x16x32_bf16 v[212:215], a[180:183], v[36:39], v[212:215]\n"
"v_mfma_f32_16x16x32_bf16 v[212:215], a[184:187], v[40:43], v[212:215]\n"
"v_mfma_f32_16x16x32_bf16 v[212:215], a[188:191], v[44:47], v[212:215]\n"
"s_nop 3 ; auto\n"
"v_max3_f32 v230, v230, v208, v209\n"
"v_max3_f32 v230, v230, v210, v211\n"
"s_nop 1 ; auto\n"
"v_max3_f32 v230, v230, v212, v213\n"
"v_max3_f32 v230, v230, v214, v215\n"
"v_mfma_f32_16x16x32_bf16 v[208:211], a[160:163], v[48:51], 0\n"
"v_mfma_f32_16x16x32_bf16 v[208:211], a[164:167], v[52:55], v[208:211]\n"
"v_mfma_f32_16x16x32_bf16 v[208:211], a[168:171], v[56:59], v[208:211]\n"
"v_mfma_f32_16x16x32_bf16 v[208:211], a[172:175], v[60:63], v[208:211]\n"
"v_mfma_f32_16x16x32_bf16 v[212:215], a[176:179], v[48:51], 0\n"
"v_mfma_f32_16x16x32_bf16 v[212:215], a[180:183], v[52:55], v[212:215]\n"
"v_mfma_f32_16x16x32_bf16 v[212:215], a[184:187], v[56:59], v[212:215]\n"
"v_mfma_f32_16x16x32_bf16 v[212:215], a[188:191], v[60:63], v[212:215]\n"
"s_nop 3 ; auto\n"
"v_max3_f32 v231, v231, v208, v209\n"
"v_max3_f32 v231, v231, v210, v211\n"
"s_nop 1 ; auto\n"
"v_max3_f32 v231, v231, v212, v213\n"
"v_max3_f32 v231, v231, v214, v215\n"
"v_mfma_f32_16x16x32_bf16 v[208:211], a[160:163], v[64:67], 0\n"
"v_mfma_f32_16x16x32_bf16 v[208:211], a[164:167], v[68:71], v[208:211]\n"
"v_mfma_f32_16x16x32_bf16 v[208:211], a[168:171], v[72:75], v[208:211]\n"
"v_mfma_f32_16x16x32_bf16 v[208:211], a[172:175], v[76:79], v[208:211]\n"
"v_mfma_f32_16x16x32_bf16 v[212:215], a[176:179], v[64:67], 0\n"
"v_mfma_f32_16x16x32_bf16 v[212:215], a[180:183], v[68:71], v[212:215]\n"
"v_mfma_f32_16x16x32_bf16 v[212:215], a[184:187], v[72:75], v[212:215]\n"
"v_mfma_f32_16x16x32_bf16 v[212:215], a[188:191], v[76:79], v[212:215]\n"
"s_nop 3 ; auto\n"
"v_max3_f32 v232, v232, v208, v209\n"
"v_max3_f32 v232, v232, v210, v211\n"
"s_nop 1 ; auto\n"
"v_max3_f32 v232, v232, v212, v213\n"
"v_max3_f32 v232, v232, v214, v215\n"
"v_mfma_f32_16x16x32_bf16 v[208:211], a[160:163], v[80:83], 0\n"
"v_mfma_f32_16x16x32_bf16 v[208:211], a[164:167], v[84:87], v[208:211]\n"
"v_mfma_f32_16x16x32_bf16 v[208:211], a[168:171], v[88:91], v[208:211]\n"
"v_mfma_f32_16x16x32_bf16 v[208:211], a[172:175], v[92:95], v[208:211]\n"
"v_mfma_f32_16x16x32_bf16 v[212:215], a[176:179], v[80:83], 0\n"
"v_mfma_f32_16x16x32_bf16 v[212:215], a[180:183], v[84:87], v[212:215]\n"
"v_mfma_f32_16x16x32_bf16 v[212:215], a[184:187], v[88:91], v[212:215]\n"
"v_mfma_f32_16x16x32_bf16 v[212:215], a[188:191], v[92:95], v[212:215]\n"
"s_nop 3 ; auto\n"
"v_max3_f32 v233, v233, v208, v209\n"
"v_max3_f32 v233, v233, v210, v211\n"
"s_nop 1 ; auto\n"
"v_max3_f32 v233, v233, v212, v213\n"
"v_max3_f32 v233, v233, v214, v215\n"
"v_mfma_f32_16x16x32_bf16 v[208:211], a[160:163], v[96:99], 0\n"
"v_mfma_f32_16x16x32_bf16 v[208:211], a[164:167], v[100:103], v[208:211]\n"
"v_mfma_f32_16x16x32_bf16 v[208:211], a[168:171], v[104:107], v[208:211]\n"
"v_mfma_f32_16x16x32_bf16 v[208:211], a[172:175], v[108:111], v[208:211]\n"
"v_mfma_f32_16x16x32_bf16 v[212:215], a[176:179], v[96:99], 0\n"
"v_mfma_f32_16x16x32_bf16 v[212:215], a[180:183], v[100:103], v[212:215]\n"
"v_mfma_f32_16x16x32_bf16 v[212:215], a[184:187], v[104:107], v[212:215]\n"
"v_mfma_f32_16x16x32_bf16 v[212:215], a[188:191], v[108:111], v[212:215]\n"
"s_nop 3 ; auto\n"
"v_max3_f32 v234, v234, v208, v209\n"
"v_max3_f32 v234, v234, v210, v211\n"
"s_nop 1 ; auto\n"
"v_max3_f32 v234, v234, v212, v213\n"
"v_max3_f32 v234, v234, v214, v215\n"
"s_add_u32 s93, s93, 8192\n"
"s_sub_u32 s92, s92, 1\n"
"s_cmp_lg_u32 s92, 0\n"
"s_cbranch_scc1 Ls1\n"
"v_mov_b32 v216, v228\n"
"s_nop 1 ; auto\n"
"v_permlane32_swap_b32 v228, v216\n"
"v_max_f32 v228, v228, v216\n"
"v_mov_b32 v216, v229\n"
"s_nop 1 ; auto\n"
"v_permlane32_swap_b32 v229, v216\n"
"v_max_f32 v229, v229, v216\n"
"v_mov_b32 v216, v230\n"
"s_nop 1 ; auto\n"
"v_permlane32_swap_b32 v230, v216\n"
"v_max_f32 v230, v230, v216\n"
"v_mov_b32 v216, v231\n"
"s_nop 1 ; auto\n"
"v_permlane32_swap_b32 v231, v216\n"
"v_max_f32 v231, v231, v216\n"
"v_mov_b32 v216, v232\n"
"s_nop 1 ; auto\n"
"v_permlane32_swap_b32 v232, v216\n"
"v_max_f32 v232, v232, v216\n"
"v_mov_b32 v216, v233\n"
"s_nop 1 ; auto\n"
"v_permlane32_swap_b32 v233, v216\n"
"v_max_f32 v233, v233, v216\n"
"v_mov_b32 v216, v234\n"
"s_nop 1 ; auto\n"
"v_permlane32_swap_b32 v234, v216\n"
"v_max_f32 v234, v234, v216\n"
"v_mov_b32 v216, v228\n"
"s_nop 1 ; auto\n"
"v_permlane16_swap_b32 v228, v216\n"
"v_max_f32 v228, v228, v216\n"
"v_mov_b32 v216, v229\n"
"s_nop 1 ; auto\n"
"v_permlane16_swap_b32 v229, v216\n"
"v_max_f32 v229, v229, v216\n"
"v_mov_b32 v216, v230\n"
"s_nop 1 ; auto\n"
"v_permlane16_swap_b32 v230, v216\n"
"v_max_f32 v230, v230, v216\n"
"v_mov_b32 v216, v231\n"
"s_nop 1 ; auto\n"
"v_permlane16_swap_b32 v231, v216\n"
"v_max_f32 v231, v231, v216\n"
"v_mov_b32 v216, v232\n"
"s_nop 1 ; auto\n"
"v_permlane16_swap_b32 v232, v216\n"
"v_max_f32 v232, v232, v216\n"
"v_mov_b32 v216, v233\n"
"s_nop 1 ; auto\n"
"v_permlane16_swap_b32 v233, v216\n"
"v_max_f32 v233, v233, v216\n"
"v_mov_b32 v216, v234\n"
"s_nop 1 ; auto\n"
"v_permlane16_swap_b32 v234, v216\n"
"v_max_f32 v234, v234, v216\n"
"v_mov_b32 v236, 0\n"
"v_mov_b32 v237, 0\n"
"v_mov_b32 v238, 0\n"
"v_mov_b32 v239, 0\n"
"v_mov_b32 v240, 0\n"
"v_mov_b32 v241, 0\n"
"v_mov_b32 v242, 0\n"
"v_accvgpr_write_b32 a0, 0\n"
"v_accvgpr_write_b32 a1, 0\n"
"v_accvgpr_write_b32 a2, 0\n"
"v_accvgpr_write_b32 a3, 0\n"
"v_accvgpr_write_b32 a4, 0\n"
"v_accvgpr_write_b32 a5, 0\n"
"v_accvgpr_write_b32 a6, 0\n"
"v_accvgpr_write_b32 a7, 0\n"
"v_accvgpr_write_b32 a8, 0\n"
"v_accvgpr_write_b32 a9, 0\n"
"v_accvgpr_write_b32 a10, 0\n"
"v_accvgpr_write_b32 a11, 0\n"
"v_accvgpr_write_b32 a12, 0\n"
"v_accvgpr_write_b32 a13, 0\n"
"v_accvgpr_write_b32 a14, 0\n"
"v_accvgpr_write_b32 a15, 0\n"
"v_accvgpr_write_b32 a16, 0\n"
"v_accvgpr_write_b32 a17, 0\n"
"v_accvgpr_write_b32 a18, 0\n"
"v_accvgpr_write_b32 a19, 0\n"
"v_accvgpr_write_b32 a20, 0\n"
"v_accvgpr_write_b32 a21, 0\n"
"v_accvgpr_write_b32 a22, 0\n"
"v_accvgpr_write_b32 a23, 0\n"
"v_accvgpr_write_b32 a24, 0\n"
"v_accvgpr_write_b32 a25, 0\n"
"v_accvgpr_write_b32 a26, 0\n"
"v_accvgpr_write_b32 a27, 0\n"
"v_accvgpr_write_b32 a28, 0\n"
"v_accvgpr_write_b32 a29, 0\n"
"v_accvgpr_write_b32 a30, 0\n"
"v_accvgpr_write_b32 a31, 0\n"
"v_accvgpr_write_b32 a32, 0\n"
"v_accvgpr_write_b32 a33, 0\n"
"v_accvgpr_write_b32 a34, 0\n"
"v_accvgpr_write_b32 a35, 0\n"
"v_accvgpr_write_b32 a36, 0\n"
"v_accvgpr_write_b32 a37, 0\n"
"v_accvgpr_write_b32 a38, 0\n"
"v_accvgpr_write_b32 a39, 0\n"
"v_accvgpr_write_b32 a40, 0\n"
"v_accvgpr_write_b32 a41, 0\n"
"v_accvgpr_write_b32 a42, 0\n"
"v_accvgpr_write_b32 a43, 0\n"
"v_accvgpr_write_b32 a44, 0\n"
"v_accvgpr_write_b32 a45, 0\n"
"v_accvgpr_write_b32 a46, 0\n"
"v_accvgpr_write_b32 a47, 0\n"
"v_accvgpr_write_b32 a48, 0\n"
"v_accvgpr_write_b32 a49, 0\n"
"v_accvgpr_write_b32 a50, 0\n"
"v_accvgpr_write_b32 a51, 0\n"
"v_accvgpr_write_b32 a52, 0\n"
"v_accvgpr_write_b32 a53, 0\n"
"v_accvgpr_write_b32 a54, 0\n"
"v_accvgpr_write_b32 a55, 0\n"
"v_accvgpr_write_b32 a56, 0\n"
"v_accvgpr_write_b32 a57, 0\n"
"v_accvgpr_write_b32 a58, 0\n"
"v_accvgpr_write_b32 a59, 0\n"
"v_accvgpr_write_b32 a60, 0\n"
"v_accvgpr_write_b32 a61, 0\n"
"v_accvgpr_write_b32 a62, 0\n"
"v_accvgpr_write_b32 a63, 0\n"
"v_accvgpr_write_b32 a64, 0\n"
"v_accvgpr_write_b32 a65, 0\n"
"v_accvgpr_write_b32 a66, 0\n"
"v_accvgpr_write_b32 a67, 0\n"
"v_accvgpr_write_b32 a68, 0\n"
"v_accvgpr_write_b32 a69, 0\n"
"v_accvgpr_write_b32 a70, 0\n"
"v_accvgpr_write_b32 a71, 0\n"
"v_accvgpr_write_b32 a72, 0\n"
"v_accvgpr_write_b32 a73, 0\n"
"v_accvgpr_write_b32 a74, 0\n"
"v_accvgpr_write_b32 a75, 0\n"
"v_accvgpr_write_b32 a76, 0\n"
"v_accvgpr_write_b32 a77, 0\n"
"v_accvgpr_write_b32 a78, 0\n"
"v_accvgpr_write_b32 a79, 0\n"
"v_accvgpr_write_b32 a80, 0\n"
"v_accvgpr_write_b32 a81, 0\n"
"v_accvgpr_write_b32 a82, 0\n"
"v_accvgpr_write_b32 a83, 0\n"
"v_accvgpr_write_b32 a84, 0\n"
"v_accvgpr_write_b32 a85, 0\n"
"v_accvgpr_write_b32 a86, 0\n"
"v_accvgpr_write_b32 a87, 0\n"
"v_accvgpr_write_b32 a88, 0\n"
"v_accvgpr_write_b32 a89, 0\n"
"v_accvgpr_write_b32 a90, 0\n"
"v_accvgpr_write_b32 a91, 0\n"
"v_accvgpr_write_b32 a92, 0\n"
"v_accvgpr_write_b32 a93, 0\n"
"v_accvgpr_write_b32 a94, 0\n"
"v_accvgpr_write_b32 a95, 0\n"
"v_accvgpr_write_b32 a96, 0\n"
"v_accvgpr_write_b32 a97, 0\n"
"v_accvgpr_write_b32 a98, 0\n"
"v_accvgpr_write_b32 a99, 0\n"
"v_accvgpr_write_b32 a100, 0\n"
"v_accvgpr_write_b32 a101, 0\n"
"v_accvgpr_write_b32 a102, 0\n"
"v_accvgpr_write_b32 a103, 0\n"
"v_accvgpr_write_b32 a104, 0\n"
"v_accvgpr_write_b32 a105, 0\n"
"v_accvgpr_write_b32 a106, 0\n"
"v_accvgpr_write_b32 a107, 0\n"
"v_accvgpr_write_b32 a108, 0\n"
"v_accvgpr_write_b32 a109, 0\n"
"v_accvgpr_write_b32 a110, 0\n"
"v_accvgpr_write_b32 a111, 0\n"
"v_accvgpr_write_b32 a112, 0\n"
"v_accvgpr_write_b32 a113, 0\n"
"v_accvgpr_write_b32 a114, 0\n"
"v_accvgpr_write_b32 a115, 0\n"
"v_accvgpr_write_b32 a116, 0\n"
"v_accvgpr_write_b32 a117, 0\n"
"v_accvgpr_write_b32 a118, 0\n"
"v_accvgpr_write_b32 a119, 0\n"
"v_accvgpr_write_b32 a120, 0\n"
"v_accvgpr_write_b32 a121, 0\n"
"v_accvgpr_write_b32 a122, 0\n"
"v_accvgpr_write_b32 a123, 0\n"
"v_accvgpr_write_b32 a124, 0\n"
"v_accvgpr_write_b32 a125, 0\n"
"v_accvgpr_write_b32 a126, 0\n"
"v_accvgpr_write_b32 a127, 0\n"
"v_mov_b32 v112, 0\n"
"v_mov_b32 v113, 0\n"
"v_mov_b32 v114, 0\n"
"v_mov_b32 v115, 0\n"
"v_mov_b32 v116, 0\n"
"v_mov_b32 v117, 0\n"
"v_mov_b32 v118, 0\n"
"v_mov_b32 v119, 0\n"
"v_mov_b32 v120, 0\n"
"v_mov_b32 v121, 0\n"
"v_mov_b32 v122, 0\n"
"v_mov_b32 v123, 0\n"
"v_mov_b32 v124, 0\n"
"v_mov_b32 v125, 0\n"
"v_mov_b32 v126, 0\n"
"v_mov_b32 v127, 0\n"
"v_mov_b32 v128, 0\n"
"v_mov_b32 v129, 0\n"
"v_mov_b32 v130, 0\n"
"v_mov_b32 v131, 0\n"
"v_mov_b32 v132, 0\n"
"v_mov_b32 v133, 0\n"
"v_mov_b32 v134, 0\n"
"v_mov_b32 v135, 0\n"
"v_mov_b32 v136, 0\n"
"v_mov_b32 v137, 0\n"
"v_mov_b32 v138, 0\n"
"v_mov_b32 v139, 0\n"
"v_mov_b32 v140, 0\n"
"v_mov_b32 v141, 0\n"
"v_mov_b32 v142, 0\n"
"v_mov_b32 v143, 0\n"
"v_mov_b32 v144, 0\n"
"v_mov_b32 v145, 0\n"
"v_mov_b32 v146, 0\n"
"v_mov_b32 v147, 0\n"
"v_mov_b32 v148, 0\n"
"v_mov_b32 v149, 0\n"
"v_mov_b32 v150, 0\n"
"v_mov_b32 v151, 0\n"
"v_mov_b32 v152, 0\n"
"v_mov_b32 v153, 0\n"
"v_mov_b32 v154, 0\n"
"v_mov_b32 v155, 0\n"
"v_mov_b32 v156, 0\n"
"v_mov_b32 v157, 0\n"
"v_mov_b32 v158, 0\n"
"v_mov_b32 v159, 0\n"
"v_mov_b32 v160, 0\n"
"v_mov_b32 v161, 0\n"
"v_mov_b32 v162, 0\n"
"v_mov_b32 v163, 0\n"
"v_mov_b32 v164, 0\n"
"v_mov_b32 v165, 0\n"
"v_mov_b32 v166, 0\n"
"v_mov_b32 v167, 0\n"
"v_mov_b32 v168, 0\n"
"v_mov_b32 v169, 0\n"
"v_mov_b32 v170, 0\n"
"v_mov_b32 v171, 0\n"
"v_mov_b32 v172, 0\n"
"v_mov_b32 v173, 0\n"
"v_mov_b32 v174, 0\n"
"v_mov_b32 v175, 0\n"
"v_mov_b32 v176, 0\n"
"v_mov_b32 v177, 0\n"
"v_mov_b32 v178, 0\n"
"v_mov_b32 v179, 0\n"
"v_mov_b32 v180, 0\n"
"v_mov_b32 v181, 0\n"
"v_mov_b32 v182, 0\n"
"v_mov_b32 v183, 0\n"
"v_mov_b32 v184, 0\n"
"v_mov_b32 v185, 0\n"
"v_mov_b32 v186, 0\n"
"v_mov_b32 v187, 0\n"
"v_mov_b32 v188, 0\n"
"v_mov_b32 v189, 0\n"
"v_mov_b32 v190, 0\n"
"v_mov_b32 v191, 0\n"
"v_mov_b32 v192, 0\n"
"v_mov_b32 v193, 0\n"
"v_mov_b32 v194, 0\n"
"v_mov_b32 v195, 0\n"
"v_mov_b32 v196, 0\n"
"v_mov_b32 v197, 0\n"
"v_mov_b32 v198, 0\n"
"v_mov_b32 v199, 0\n"
"v_mov_b32 v200, 0\n"
"v_mov_b32 v201, 0\n"
"v_mov_b32 v202, 0\n"
"v_mov_b32 v203, 0\n"
"v_mov_b32 v204, 0\n"
"v_mov_b32 v205, 0\n"
"v_mov_b32 v206, 0\n"
"v_mov_b32 v207, 0\n"
"s_mov_b32 s92, 16\n"
"s_mov_b32 s93, 0\n"
"Ls2:\n"
"s_nop 7\n"
"s_add_u32 m0, s66, 0\n"
"s_add_u32 s94, s93, 0\n"
"buffer_load_dwordx4 v220, s[84:87], s94 offen lds\n"
"buffer_load_dwordx4 v221, s[84:87], s94 offen offset:1024 lds\n"
"buffer_load_dwordx4 v220, s[84:87], s94 offen offset:2048 lds\n"
"buffer_load_dwordx4 v221, s[84:87], s94 offen offset:3072 lds\n"
"s_add_u32 m0, s66, 4096\n"
"s_add_u32 s94, s93, 4096\n"
"buffer_load_dwordx4 v220, s[84:87], s94 offen lds\n"
"buffer_load_dwordx4 v221, s[84:87], s94 offen offset:1024 lds\n"
"buffer_load_dwordx4 v220, s[84:87], s94 offen offset:2048 lds\n"
"buffer_load_dwordx4 v221, s[84:87], s94 offen offset:3072 lds\n"
"s_waitcnt vmcnt(0)\n"
"ds_read_b128 a[160:163], v222 offset:0\n"
"ds_read_b128 a[164:167], v222 offset:256\n"
"ds_read_b128 a[168:171], v222 offset:512\n"
"ds_read_b128 a[172:175], v222 offset:768\n"
"ds_read_b128 a[176:179], v222 offset:4096\n"
"ds_read_b128 a[180:183], v222 offset:4352\n"
"ds_read_b128 a[184:187], v222 offset:4608\n"
"ds_read_b128 a[188:191], v222 offset:4864\n"
"s_waitcnt lgkmcnt(0)\n"
"s_add_u32 m0, s66, 0\n"
"s_add_u32 s94, s93, 0\n"
"buffer_load_dwordx4 v220, s[88:91], s94 offen lds\n"
"buffer_load_dwordx4 v221, s[88:91], s94 offen offset:1024 lds\n"
"buffer_load_dwordx4 v220, s[88:91], s94 offen offset:2048 lds\n"
"buffer_load_dwordx4 v221, s[88:91], s94 offen offset:3072 lds\n"
"s_add_u32 m0, s66, 4096\n"
"s_add_u32 s94, s93, 4096\n"
"buffer_load_dwordx4 v220, s[88:91], s94 offen lds\n"
"buffer_load_dwordx4 v221, s[88:91], s94 offen offset:1024 lds\n"
"buffer_load_dwordx4 v220, s[88:91], s94 offen offset:2048 lds\n"
"buffer_load_dwordx4 v221, s[88:91], s94 offen offset:3072 lds\n"
"s_waitcnt vmcnt(0)\n"
"ds_read_b64_tr_b16 a[128:129], v223 offset:0\n"
"ds_read_b64_tr_b16 a[130:131], v223 offset:4096\n"
"ds_read_b64_tr_b16 a[132:133], v235 offset:0\n"
"ds_read_b64_tr_b16 a[134:135], v235 offset:4096\n"
"ds_read_b64_tr_b16 a[136:137], v223 offset:256\n"
"ds_read_b64_tr_b16 a[138:139], v223 offset:4352\n"
"ds_read_b64_tr_b16 a[140:141], v235 offset:256\n"
"ds_read_b64_tr_b16 a[142:143], v235 offset:4352\n"
"ds_read_b64_tr_b16 a[144:145], v223 offset:512\n"
"ds_read_b64_tr_b16 a[146:147], v223 offset:4608\n"
"ds_read_b64_tr_b16 a[148:149], v235 offset:512\n"
"ds_read_b64_tr_b16 a[150:151], v235 offset:4608\n"
"ds_read_b64_tr_b16 a[152:153], v223 offset:768\n"
"ds_read_b64_tr_b16 a[154:155], v223 offset:4864\n"
"ds_read_b64_tr_b16 a[156:157], v235 offset:768\n"
"ds_read_b64_tr_b16 a[158:159], v235 offset:4864\n"
"s_waitcnt lgkmcnt(0)\n"
"v_mfma_f32_16x16x32_bf16 v[208:211], a[160:163], v[0:3], 0\n"
"v_mfma_f32_16x16x32_bf16 v[208:211], a[164:167], v[4:7], v[208:211]\n"
"v_mfma_f32_16x16x32_bf16 v[208:211], a[168:171], v[8:11], v[208:211]\n"
"v_mfma_f32_16x16x32_bf16 v[208:211], a[172:175], v[12:15], v[208:211]\n"
"v_mfma_f32_16x16x32_bf16 v[212:215], a[176:179], v[0:3], 0\n"
"v_mfma_f32_16x16x32_bf16 v[212:215], a[180:183], v[4:7], v[212:215]\n"
"v_mfma_f32_16x16x32_bf16 v[212:215], a[184:187], v[8:11], v[212:215]\n"
"v_mfma_f32_16x16x32_bf16 v[212:215], a[188:191], v[12:15], v[212:215]\n"
"s_nop 3 ; auto\n"
"v_sub_f32 v208, v208, v228\n"
"v_sub_f32 v209, v209, v228\n"
"v_sub_f32 v210, v210, v228\n"
"v_sub_f32 v211, v211, v228\n"
"v_sub_f32 v212, v212, v228\n"
"v_sub_f32 v213, v213, v228\n"
"v_sub_f32 v214, v214, v228\n"
"v_sub_f32 v215, v215, v228\n"
"v_exp_f32 v208, v208\n"
"v_exp_f32 v209, v209\n"
"v_exp_f32 v210, v210\n"
"v_exp_f32 v211, v211\n"
"v_exp_f32 v212, v212\n"
"v_exp_f32 v213, v213\n"
"v_exp_f32 v214, v214\n"
"v_exp_f32 v215, v215\n"
"v_add_f32 v236, v236, v208\n"
"v_add_f32 v236, v236, v209\n"
"v_add_f32 v236, v236, v210\n"
"v_add_f32 v236, v236, v211\n"
"v_add_f32 v236, v236, v212\n"
"v_add_f32 v236, v236, v213\n"
"v_add_f32 v236, v236, v214\n"
"v_add_f32 v236, v236, v215\n"
"v_cvt_pk_bf16_f32 v224, v208, v209\n"
"v_cvt_pk_bf16_f32 v225, v210, v211\n"
"v_cvt_pk_bf16_f32 v226, v212, v213\n"
"v_cvt_pk_bf16_f32 v227, v214, v215\n"
"s_nop 1 ; auto\n"
"v_mfma_f32_16x16x32_bf16 v[112:115], a[128:131], v[224:227], v[112:115]\n"
"v_mfma_f32_16x16x32_bf16 v[116:119], a[132:135], v[224:227], v[116:119]\n"
"v_mfma_f32_16x16x32_bf16 v[120:123], a[136:139], v[224:227], v[120:123]\n"
"v_mfma_f32_16x16x32_bf16 v[124:127], a[140:143], v[224:227], v[124:127]\n"
"v_mfma_f32_16x16x32_bf16 v[128:131], a[144:147], v[224:227], v[128:131]\n"
"v_mfma_f32_16x16x32_bf16 v[132:135], a[148:151], v[224:227], v[132:135]\n"
"v_mfma_f32_16x16x32_bf16 v[136:139], a[152:155], v[224:227], v[136:139]\n"
"v_mfma_f32_16x16x32_bf16 v[140:143], a[156:159], v[224:227], v[140:143]\n"
"v_mfma_f32_16x16x32_bf16 v[208:211], a[160:163], v[16:19], 0\n"
"v_mfma_f32_16x16x32_bf16 v[208:211], a[164:167], v[20:23], v[208:211]\n"
"v_mfma_f32_16x16x32_bf16 v[208:211], a[168:171], v[24:27], v[208:211]\n"
"v_mfma_f32_16x16x32_bf16 v[208:211], a[172:175], v[28:31], v[208:211]\n"
"v_mfma_f32_16x16x32_bf16 v[212:215], a[176:179], v[16:19], 0\n"
"v_mfma_f32_16x16x32_bf16 v[212:215], a[180:183], v[20:23], v[212:215]\n"
"v_mfma_f32_16x16x32_bf16 v[212:215], a[184:187], v[24:27], v[212:215]\n"
"v_mfma_f32_16x16x32_bf16 v[212:215], a[188:191], v[28:31], v[212:215]\n"
"s_nop 3 ; auto\n"
"v_sub_f32 v208, v208, v229\n"
"v_sub_f32 v209, v209, v229\n"
"v_sub_f32 v210, v210, v229\n"
"v_sub_f32 v211, v211, v229\n"
"v_sub_f32 v212, v212, v229\n"
"v_sub_f32 v213, v213, v229\n"
"v_sub_f32 v214, v214, v229\n"
"v_sub_f32 v215, v215, v229\n"
"v_exp_f32 v208, v208\n"
"v_exp_f32 v209, v209\n"
"v_exp_f32 v210, v210\n"
"v_exp_f32 v211, v211\n"
"v_exp_f32 v212, v212\n"
"v_exp_f32 v213, v213\n"
"v_exp_f32 v214, v214\n"
"v_exp_f32 v215, v215\n"
"v_add_f32 v237, v237, v208\n"
"v_add_f32 v237, v237, v209\n"
"v_add_f32 v237, v237, v210\n"
"v_add_f32 v237, v237, v211\n"
"v_add_f32 v237, v237, v212\n"
"v_add_f32 v237, v237, v213\n"
"v_add_f32 v237, v237, v214\n"
"v_add_f32 v237, v237, v215\n"
"v_cvt_pk_bf16_f32 v224, v208, v209\n"
"v_cvt_pk_bf16_f32 v225, v210, v211\n"
"v_cvt_pk_bf16_f32 v226, v212, v213\n"
"v_cvt_pk_bf16_f32 v227, v214, v215\n"
"s_nop 1 ; auto\n"
"v_mfma_f32_16x16x32_bf16 v[144:147], a[128:131], v[224:227], v[144:147]\n"
"v_mfma_f32_16x16x32_bf16 v[148:151], a[132:135], v[224:227], v[148:151]\n"
"v_mfma_f32_16x16x32_bf16 v[152:155], a[136:139], v[224:227], v[152:155]\n"
"v_mfma_f32_16x16x32_bf16 v[156:159], a[140:143], v[224:227], v[156:159]\n"
"v_mfma_f32_16x16x32_bf16 v[160:163], a[144:147], v[224:227], v[160:163]\n"
"v_mfma_f32_16x16x32_bf16 v[164:167], a[148:151], v[224:227], v[164:167]\n"
"v_mfma_f32_16x16x32_bf16 v[168:171], a[152:155], v[224:227], v[168:171]\n"
"v_mfma_f32_16x16x32_bf16 v[172:175], a[156:159], v[224:227], v[172:175]\n"
"v_mfma_f32_16x16x32_bf16 v[208:211], a[160:163], v[32:35], 0\n"
"v_mfma_f32_16x16x32_bf16 v[208:211], a[164:167], v[36:39], v[208:211]\n"
"v_mfma_f32_16x16x32_bf16 v[208:211], a[168:171], v[40:43], v[208:211]\n"
"v_mfma_f32_16x16x32_bf16 v[208:211], a[172:175], v[44:47], v[208:211]\n"
"v_mfma_f32_16x16x32_bf16 v[212:215], a[176:179], v[32:35], 0\n"
"v_mfma_f32_16x16x32_bf16 v[212:215], a[180:183], v[36:39], v[212:215]\n"
"v_mfma_f32_16x16x32_bf16 v[212:215], a[184:187], v[40:43], v[212:215]\n"
"v_mfma_f32_16x16x32_bf16 v[212:215], a[188:191], v[44:47], v[212:215]\n"
"s_nop 3 ; auto\n"
"v_sub_f32 v208, v208, v230\n"
"v_sub_f32 v209, v209, v230\n"
"v_sub_f32 v210, v210, v230\n"
"v_sub_f32 v211, v211, v230\n"
"v_sub_f32 v212, v212, v230\n"
"v_sub_f32 v213, v213, v230\n"
"v_sub_f32 v214, v214, v230\n"
"v_sub_f32 v215, v215, v230\n"
"v_exp_f32 v208, v208\n"
"v_exp_f32 v209, v209\n"
"v_exp_f32 v210, v210\n"
"v_exp_f32 v211, v211\n"
"v_exp_f32 v212, v212\n"
"v_exp_f32 v213, v213\n"
"v_exp_f32 v214, v214\n"
"v_exp_f32 v215, v215\n"
"v_add_f32 v238, v238, v208\n"
"v_add_f32 v238, v238, v209\n"
"v_add_f32 v238, v238, v210\n"
"v_add_f32 v238, v238, v211\n"
"v_add_f32 v238, v238, v212\n"
"v_add_f32 v238, v238, v213\n"
"v_add_f32 v238, v238, v214\n"
"v_add_f32 v238, v238, v215\n"
"v_cvt_pk_bf16_f32 v224, v208, v209\n"
"v_cvt_pk_bf16_f32 v225, v210, v211\n"
"v_cvt_pk_bf16_f32 v226, v212, v213\n"
"v_cvt_pk_bf16_f32 v227, v214, v215\n"
"s_nop 1 ; auto\n"
"v_mfma_f32_16x16x32_bf16 v[176:179], a[128:131], v[224:227], v[176:179]\n"
"v_mfma_f32_16x16x32_bf16 v[180:183], a[132:135], v[224:227], v[180:183]\n"
"v_mfma_f32_16x16x32_bf16 v[184:187], a[136:139], v[224:227], v[184:187]\n"
"v_mfma_f32_16x16x32_bf16 v[188:191], a[140:143], v[224:227], v[188:191]\n"
"v_mfma_f32_16x16x32_bf16 v[192:195], a[144:147], v[224:227], v[192:195]\n"
"v_mfma_f32_16x16x32_bf16 v[196:199], a[148:151], v[224:227], v[196:199]\n"
"v_mfma_f32_16x16x32_bf16 v[200:203], a[152:155], v[224:227], v[200:203]\n"
"v_mfma_f32_16x16x32_bf16 v[204:207], a[156:159], v[224:227], v[204:207]\n"
"v_mfma_f32_16x16x32_bf16 v[208:211], a[160:163], v[48:51], 0\n"
"v_mfma_f32_16x16x32_bf16 v[208:211], a[164:167], v[52:55], v[208:211]\n"
"v_mfma_f32_16x16x32_bf16 v[208:211], a[168:171], v[56:59], v[208:211]\n"
"v_mfma_f32_16x16x32_bf16 v[208:211], a[172:175], v[60:63], v[208:211]\n"
"v_mfma_f32_16x16x32_bf16 v[212:215], a[176:179], v[48:51], 0\n"
"v_mfma_f32_16x16x32_bf16 v[212:215], a[180:183], v[52:55], v[212:215]\n"
"v_mfma_f32_16x16x32_bf16 v[212:215], a[184:187], v[56:59], v[212:215]\n"
"v_mfma_f32_16x16x32_bf16 v[212:215], a[188:191], v[60:63], v[212:215]\n"
"s_nop 3 ; auto\n"
"v_sub_f32 v208, v208, v231\n"
"v_sub_f32 v209, v209, v231\n"
"v_sub_f32 v210, v210, v231\n"
"v_sub_f32 v211, v211, v231\n"
"v_sub_f32 v212, v212, v231\n"
"v_sub_f32 v213, v213, v231\n"
"v_sub_f32 v214, v214, v231\n"
"v_sub_f32 v215, v215, v231\n"
"v_exp_f32 v208, v208\n"
"v_exp_f32 v209, v209\n"
"v_exp_f32 v210, v210\n"
"v_exp_f32 v211, v211\n"
"v_exp_f32 v212, v212\n"
"v_exp_f32 v213, v213\n"
"v_exp_f32 v214, v214\n"
"v_exp_f32 v215, v215\n"
"v_add_f32 v239, v239, v208\n"
"v_add_f32 v239, v239, v209\n"
"v_add_f32 v239, v239, v210\n"
"v_add_f32 v239, v239, v211\n"
"v_add_f32 v239, v239, v212\n"
"v_add_f32 v239, v239, v213\n"
"v_add_f32 v239, v239, v214\n"
"v_add_f32 v239, v239, v215\n"
"v_cvt_pk_bf16_f32 v224, v208, v209\n"
"v_cvt_pk_bf16_f32 v225, v210, v211\n"
"v_cvt_pk_bf16_f32 v226, v212, v213\n"
"v_cvt_pk_bf16_f32 v227, v214, v215\n"
"s_nop 1 ; auto\n"
"v_mfma_f32_16x16x32_bf16 a[0:3], a[128:131], v[224:227], a[0:3]\n"
"v_mfma_f32_16x16x32_bf16 a[4:7], a[132:135], v[224:227], a[4:7]\n"
"v_mfma_f32_16x16x32_bf16 a[8:11], a[136:139], v[224:227], a[8:11]\n"
"v_mfma_f32_16x16x32_bf16 a[12:15], a[140:143], v[224:227], a[12:15]\n"
"v_mfma_f32_16x16x32_bf16 a[16:19], a[144:147], v[224:227], a[16:19]\n"
"v_mfma_f32_16x16x32_bf16 a[20:23], a[148:151], v[224:227], a[20:23]\n"
"v_mfma_f32_16x16x32_bf16 a[24:27], a[152:155], v[224:227], a[24:27]\n"
"v_mfma_f32_16x16x32_bf16 a[28:31], a[156:159], v[224:227], a[28:31]\n"
"v_mfma_f32_16x16x32_bf16 v[208:211], a[160:163], v[64:67], 0\n"
"v_mfma_f32_16x16x32_bf16 v[208:211], a[164:167], v[68:71], v[208:211]\n"
"v_mfma_f32_16x16x32_bf16 v[208:211], a[168:171], v[72:75], v[208:211]\n"
"v_mfma_f32_16x16x32_bf16 v[208:211], a[172:175], v[76:79], v[208:211]\n"
"v_mfma_f32_16x16x32_bf16 v[212:215], a[176:179], v[64:67], 0\n"
"v_mfma_f32_16x16x32_bf16 v[212:215], a[180:183], v[68:71], v[212:215]\n"
"v_mfma_f32_16x16x32_bf16 v[212:215], a[184:187], v[72:75], v[212:215]\n"
"v_mfma_f32_16x16x32_bf16 v[212:215], a[188:191], v[76:79], v[212:215]\n"
"s_nop 3 ; auto\n"
"v_sub_f32 v208, v208, v232\n"
"v_sub_f32 v209, v209, v232\n"
"v_sub_f32 v210, v210, v232\n"
"v_sub_f32 v211, v211, v232\n"
"v_sub_f32 v212, v212, v232\n"
"v_sub_f32 v213, v213, v232\n"
"v_sub_f32 v214, v214, v232\n"
"v_sub_f32 v215, v215, v232\n"
"v_exp_f32 v208, v208\n"
"v_exp_f32 v209, v209\n"
"v_exp_f32 v210, v210\n"
"v_exp_f32 v211, v211\n"
"v_exp_f32 v212, v212\n"
"v_exp_f32 v213, v213\n"
"v_exp_f32 v214, v214\n"
"v_exp_f32 v215, v215\n"
"v_add_f32 v240, v240, v208\n"
"v_add_f32 v240, v240, v209\n"
"v_add_f32 v240, v240, v210\n"
"v_add_f32 v240, v240, v211\n"
"v_add_f32 v240, v240, v212\n"
"v_add_f32 v240, v240, v213\n"
"v_add_f32 v240, v240, v214\n"
"v_add_f32 v240, v240, v215\n"
"v_cvt_pk_bf16_f32 v224, v208, v209\n"
"v_cvt_pk_bf16_f32 v225, v210, v211\n"
"v_cvt_pk_bf16_f32 v226, v212, v213\n"
"v_cvt_pk_bf16_f32 v227, v214, v215\n"
"s_nop 1 ; auto\n"
"v_mfma_f32_16x16x32_bf16 a[32:35], a[128:131], v[224:227], a[32:35]\n"
"v_mfma_f32_16x16x32_bf16 a[36:39], a[132:135], v[224:227], a[36:39]\n"
"v_mfma_f32_16x16x32_bf16 a[40:43], a[136:139], v[224:227], a[40:43]\n"
"v_mfma_f32_16x16x32_bf16 a[44:47], a[140:143], v[224:227], a[44:47]\n"
"v_mfma_f32_16x16x32_bf16 a[48:51], a[144:147], v[224:227], a[48:51]\n"
"v_mfma_f32_16x16x32_bf16 a[52:55], a[148:151], v[224:227], a[52:55]\n"
"v_mfma_f32_16x16x32_bf16 a[56:59], a[152:155], v[224:227], a[56:59]\n"
"v_mfma_f32_16x16x32_bf16 a[60:63], a[156:159], v[224:227], a[60:63]\n"
"v_mfma_f32_16x16x32_bf16 v[208:211], a[160:163], v[80:83], 0\n"
"v_mfma_f32_16x16x32_bf16 v[208:211], a[164:167], v[84:87], v[208:211]\n"
"v_mfma_f32_16x16x32_bf16 v[208:211], a[168:171], v[88:91], v[208:211]\n"
"v_mfma_f32_16x16x32_bf16 v[208:211], a[172:175], v[92:95], v[208:211]\n"
"v_mfma_f32_16x16x32_bf16 v[212:215], a[176:179], v[80:83], 0\n"
"v_mfma_f32_16x16x32_bf16 v[212:215], a[180:183], v[84:87], v[212:215]\n"
"v_mfma_f32_16x16x32_bf16 v[212:215], a[184:187], v[88:91], v[212:215]\n"
"v_mfma_f32_16x16x32_bf16 v[212:215], a[188:191], v[92:95], v[212:215]\n"
"s_nop 3 ; auto\n"
"v_sub_f32 v208, v208, v233\n"
"v_sub_f32 v209, v209, v233\n"
"v_sub_f32 v210, v210, v233\n"
"v_sub_f32 v211, v211, v233\n"
"v_sub_f32 v212, v212, v233\n"
"v_sub_f32 v213, v213, v233\n"
"v_sub_f32 v214, v214, v233\n"
"v_sub_f32 v215, v215, v233\n"
"v_exp_f32 v208, v208\n"
"v_exp_f32 v209, v209\n"
"v_exp_f32 v210, v210\n"
"v_exp_f32 v211, v211\n"
"v_exp_f32 v212, v212\n"
"v_exp_f32 v213, v213\n"
"v_exp_f32 v214, v214\n"
"v_exp_f32 v215, v215\n"
"v_add_f32 v241, v241, v208\n"
"v_add_f32 v241, v241, v209\n"
"v_add_f32 v241, v241, v210\n"
"v_add_f32 v241, v241, v211\n"
"v_add_f32 v241, v241, v212\n"
"v_add_f32 v241, v241, v213\n"
"v_add_f32 v241, v241, v214\n"
"v_add_f32 v241, v241, v215\n"
"v_cvt_pk_bf16_f32 v224, v208, v209\n"
"v_cvt_pk_bf16_f32 v225, v210, v211\n"
"v_cvt_pk_bf16_f32 v226, v212, v213\n"
"v_cvt_pk_bf16_f32 v227, v214, v215\n"
"s_nop 1 ; auto\n"
"v_mfma_f32_16x16x32_bf16 a[64:67], a[128:131], v[224:227], a[64:67]\n"
"v_mfma_f32_16x16x32_bf16 a[68:71], a[132:135], v[224:227], a[68:71]\n"
"v_mfma_f32_16x16x32_bf16 a[72:75], a[136:139], v[224:227], a[72:75]\n"
"v_mfma_f32_16x16x32_bf16 a[76:79], a[140:143], v[224:227], a[76:79]\n"
"v_mfma_f32_16x16x32_bf16 a[80:83], a[144:147], v[224:227], a[80:83]\n"
"v_mfma_f32_16x16x32_bf16 a[84:87], a[148:151], v[224:227], a[84:87]\n"
"v_mfma_f32_16x16x32_bf16 a[88:91], a[152:155], v[224:227], a[88:91]\n"
"v_mfma_f32_16x16x32_bf16 a[92:95], a[156:159], v[224:227], a[92:95]\n"
"v_mfma_f32_16x16x32_bf16 v[208:211], a[160:163], v[96:99], 0\n"
"v_mfma_f32_16x16x32_bf16 v[208:211], a[164:167], v[100:103], v[208:211]\n"
"v_mfma_f32_16x16x32_bf16 v[208:211], a[168:171], v[104:107], v[208:211]\n"
"v_mfma_f32_16x16x32_bf16 v[208:211], a[172:175], v[108:111], v[208:211]\n"
"v_mfma_f32_16x16x32_bf16 v[212:215], a[176:179], v[96:99], 0\n"
"v_mfma_f32_16x16x32_bf16 v[212:215], a[180:183], v[100:103], v[212:215]\n"
"v_mfma_f32_16x16x32_bf16 v[212:215], a[184:187], v[104:107], v[212:215]\n"
"v_mfma_f32_16x16x32_bf16 v[212:215], a[188:191], v[108:111], v[212:215]\n"
"s_nop 3 ; auto\n"
"v_sub_f32 v208, v208, v234\n"
"v_sub_f32 v209, v209, v234\n"
"v_sub_f32 v210, v210, v234\n"
"v_sub_f32 v211, v211, v234\n"
"v_sub_f32 v212, v212, v234\n"
"v_sub_f32 v213, v213, v234\n"
"v_sub_f32 v214, v214, v234\n"
"v_sub_f32 v215, v215, v234\n"
"v_exp_f32 v208, v208\n"
"v_exp_f32 v209, v209\n"
"v_exp_f32 v210, v210\n"
"v_exp_f32 v211, v211\n"
"v_exp_f32 v212, v212\n"
"v_exp_f32 v213, v213\n"
"v_exp_f32 v214, v214\n"
"v_exp_f32 v215, v215\n"
"v_add_f32 v242, v242, v208\n"
"v_add_f32 v242, v242, v209\n"
"v_add_f32 v242, v242, v210\n"
"v_add_f32 v242, v242, v211\n"
"v_add_f32 v242, v242, v212\n"
"v_add_f32 v242, v242, v213\n"
"v_add_f32 v242, v242, v214\n"
"v_add_f32 v242, v242, v215\n"
"v_cvt_pk_bf16_f32 v224, v208, v209\n"
"v_cvt_pk_bf16_f32 v225, v210, v211\n"
"v_cvt_pk_bf16_f32 v226, v212, v213\n"
"v_cvt_pk_bf16_f32 v227, v214, v215\n"
"s_nop 1 ; auto\n"
"v_mfma_f32_16x16x32_bf16 a[96:99], a[128:131], v[224:227], a[96:99]\n"
"v_mfma_f32_16x16x32_bf16 a[100:103], a[132:135], v[224:227], a[100:103]\n"
"v_mfma_f32_16x16x32_bf16 a[104:107], a[136:139], v[224:227], a[104:107]\n"
"v_mfma_f32_16x16x32_bf16 a[108:111], a[140:143], v[224:227], a[108:111]\n"
"v_mfma_f32_16x16x32_bf16 a[112:115], a[144:147], v[224:227], a[112:115]\n"
"v_mfma_f32_16x16x32_bf16 a[116:119], a[148:151], v[224:227], a[116:119]\n"
"v_mfma_f32_16x16x32_bf16 a[120:123], a[152:155], v[224:227], a[120:123]\n"
"v_mfma_f32_16x16x32_bf16 a[124:127], a[156:159], v[224:227], a[124:127]\n"
"s_add_u32 s93, s93, 8192\n"
"s_sub_u32 s92, s92, 1\n"
"s_cmp_lg_u32 s92, 0\n"
"s_cbranch_scc1 Ls2\n"
"v_mov_b32 v208, v236\n"
"v_mov_b32 v209, v237\n"
"v_mov_b32 v210, v238\n"
"v_mov_b32 v211, v239\n"
"v_mov_b32 v212, v240\n"
"v_mov_b32 v213, v241\n"
"v_mov_b32 v214, v242\n"
"v_permlane32_swap_b32 v236, v208\n"
"v_permlane32_swap_b32 v237, v209\n"
"v_permlane32_swap_b32 v238, v210\n"
"v_permlane32_swap_b32 v239, v211\n"
"v_permlane32_swap_b32 v240, v212\n"
"v_permlane32_swap_b32 v241, v213\n"
"v_permlane32_swap_b32 v242, v214\n"
"v_add_f32 v236, v236, v208\n"
"v_add_f32 v237, v237, v209\n"
"v_add_f32 v238, v238, v210\n"
"v_add_f32 v239, v239, v211\n"
"v_add_f32 v240, v240, v212\n"
"v_add_f32 v241, v241, v213\n"
"v_add_f32 v242, v242, v214\n"
"v_mov_b32 v208, v236\n"
"v_mov_b32 v209, v237\n"
"v_mov_b32 v210, v238\n"
"v_mov_b32 v211, v239\n"
"v_mov_b32 v212, v240\n"
"v_mov_b32 v213, v241\n"
"v_mov_b32 v214, v242\n"
"v_permlane16_swap_b32 v236, v208\n"
"v_permlane16_swap_b32 v237, v209\n"
"v_permlane16_swap_b32 v238, v210\n"
"v_permlane16_swap_b32 v239, v211\n"
"v_permlane16_swap_b32 v240, v212\n"
"v_permlane16_swap_b32 v241, v213\n"
"v_permlane16_swap_b32 v242, v214\n"
"v_add_f32 v236, v236, v208\n"
"v_add_f32 v237, v237, v209\n"
"v_add_f32 v238, v238, v210\n"
"v_add_f32 v239, v239, v211\n"
"v_add_f32 v240, v240, v212\n"
"v_add_f32 v241, v241, v213\n"
"v_add_f32 v242, v242, v214\n"
"s_nop 7\n"
"s_branch Lslowret\n"
"Lend:\n"
    : [tA] "+v"(tA)
    : [karg] "s"(__builtin_amdgcn_kernarg_segment_ptr()), [bid] "s"(blockIdx.x)
    :
"v0", "v1", "v2", "v3", "v4", "v5", "v6", "v7", "v8", "v9", "v10", "v11", "v12", "v13", "v14", "v15", "v16", "v17", "v18", "v19", "v20", "v21", "v22", "v23", "v24", "v25", "v26", "v27", "v28", "v29", "v30", "v31", "v32", "v33", "v34", "v35", "v36", "v37", "v38", "v39", "v40", "v41", "v42", "v43", "v44", "v45", "v46", "v47", "v48", "v49", "v50", "v51", "v52", "v53", "v54", "v55", "v56", "v57", "v58", "v59", "v60", "v61", "v62", "v63", "v64", "v65", "v66", "v67", "v68", "v69", "v70", "v71", "v72", "v73", "v74", "v75", "v76", "v77", "v78", "v79", "v80", "v81", "v82", "v83", "v84", "v85", "v86", "v87", "v88", "v89", "v90", "v91", "v92", "v93", "v94", "v95", "v96", "v97", "v98", "v99", "v100", "v101", "v102", "v103", "v104", "v105", "v106", "v107", "v108", "v109", "v110", "v111", "v112", "v113", "v114", "v115", "v116", "v117", "v118", "v119", "v120", "v121", "v122", "v123", "v124", "v125", "v126", "v127", "v128", "v129", "v130", "v131", "v132", "v133", "v134", "v135", "v136", "v137", "v138", "v139", "v140", "v141", "v142", "v143", "v144", "v145", "v146", "v147", "v148", "v149", "v150", "v151", "v152", "v153", "v154", "v155", "v156", "v157", "v158", "v159", "v160", "v161", "v162", "v163", "v164", "v165", "v166", "v167", "v168", "v169", "v170", "v171", "v172", "v173", "v174", "v175", "v176", "v177", "v178", "v179", "v180", "v181", "v182", "v183", "v184", "v185", "v186", "v187", "v188", "v189", "v190", "v191", "v192", "v193", "v194", "v195", "v196", "v197", "v198", "v199", "v200", "v201", "v202", "v203", "v204", "v205", "v206", "v207", "v208", "v209", "v210", "v211", "v212", "v213", "v214", "v215", "v216", "v217", "v218", "v219", "v220", "v221", "v222", "v223", "v224", "v225", "v226", "v227", "v228", "v229", "v230", "v231", "v232", "v233", "v234", "v235", "v236", "v237", "v238", "v239", "v240", "v241", "v242", "v243", "v244", "v245", "v246", "v247", "v248", "v249", "v250", "v251", "v252", "v253", "v254", "a0", "a1", "a2", "a3", "a4", "a5", "a6", "a7", "a8", "a9", "a10", "a11", "a12", "a13", "a14", "a15", "a16", "a17", "a18", "a19", "a20", "a21", "a22", "a23", "a24", "a25", "a26", "a27", "a28", "a29", "a30", "a31", "a32", "a33", "a34", "a35", "a36", "a37", "a38", "a39", "a40", "a41", "a42", "a43", "a44", "a45", "a46", "a47", "a48", "a49", "a50", "a51", "a52", "a53", "a54", "a55", "a56", "a57", "a58", "a59", "a60", "a61", "a62", "a63", "a64", "a65", "a66", "a67", "a68", "a69", "a70", "a71", "a72", "a73", "a74", "a75", "a76", "a77", "a78", "a79", "a80", "a81", "a82", "a83", "a84", "a85", "a86", "a87", "a88", "a89", "a90", "a91", "a92", "a93", "a94", "a95", "a96", "a97", "a98", "a99", "a100", "a101", "a102", "a103", "a104", "a105", "a106", "a107", "a108", "a109", "a110", "a111", "a112", "a113", "a114", "a115", "a116", "a117", "a118", "a119", "a120", "a121", "a122", "a123", "a124", "a125", "a126", "a127", "a128", "a129", "a130", "a131", "a132", "a133", "a134", "a135", "a136", "a137", "a138", "a139", "a140", "a141", "a142", "a143", "a144", "a145", "a146", "a147", "a148", "a149", "a150", "a151", "a152", "a153", "a154", "a155", "a156", "a157", "a158", "a159", "a160", "a161", "a162", "a163", "a164", "a165", "a166", "a167", "a168", "a169", "a170", "a171", "a172", "a173", "a174", "a175", "a176", "a177", "a178", "a179", "a180", "a181", "a182", "a183", "a184", "a185", "a186", "a187", "a188", "a189", "a190", "a191", "a192", "a193", "a194", "a195", "a196", "a197", "a198", "a199", "a200", "a201", "a202", "a203", "a204", "a205", "a206", "a207", "a208", "a209", "a210", "a211", "a212", "a213", "a214", "a215", "a216", "a217", "a218", "a219", "a220", "a221", "a222", "a223", "a224", "a225", "a226", "a227", "a228", "a229", "a230", "a231", "a232", "a233", "a234", "a235", "a236", "a237", "a238", "a239", "a240", "a241", "a242", "a243", "a244", "a245", "a246", "a247", "a248", "a249", "a250", "a251", "a252", "a253", "a254", "a255", "s4", "s5", "s6", "s7", "s8", "s9", "s10", "s11", "s12", "s13", "s14", "s15", "s16", "s17", "s18", "s19", "s20", "s21", "s22", "s23", "s24", "s25", "s26", "s27", "s28", "s29", "s30", "s31", "s32", "s33", "s34", "s35", "s36", "s37", "s38", "s39", "s40", "s41", "s42", "s43", "s44", "s45", "s46", "s47", "s48", "s49", "s50", "s51", "s52", "s53", "s54", "s55", "s56", "s57", "s58", "s59", "s60", "s61", "s62", "s63", "s64", "s65", "s66", "s67", "s68", "s69", "s70", "s71", "s72", "s73", "s74", "s75", "s76", "s77", "s78", "s79", "s80", "s81", "s82", "s83", "s84", "s85", "s86", "s87", "s88", "s89", "s90", "s91", "s92", "s93", "s94", "s95", "s96", "s97", "s98", "s99", "s100", "s101", "vcc", "scc", "memory"
  );
}

extern "C" void kernel_launch(void* const* d_in, const int* in_sizes, int n_in, void* d_out, int out_size, void* d_ws, size_t ws_size, hipStream_t stream) {
  Args A;
  for (int br = 0; br < 2; br++) {
    for (int k = 0; k < 5; k++) A.p[br][k] = (const char*)d_in[br * 5 + k];
    A.p[br][5] = (const char*)d_ws + (size_t)br * 16u * (32u * 28928u + 1024u);
    A.p[br][6] = (const char*)d_out + br * 1152;
    A.p[br][7] = (const char*)d_ws;
  }
  (void)hipFuncSetAttribute((const void*)gqa_pair_fused, hipFuncAttributeMaxDynamicSharedMemorySize, 163840);
  gqa_pair_fused<<<256, 256, 163840, stream>>>(A);
}
```
